```python
import torch

def _softmax(x, axis=-1):
    return torch.softmax(x, dim=axis)

def _gelu(x):
    return torch.nn.functional.gelu(x, approximate='tanh')

def _silu(x):
    return torch.nn.functional.silu(x)

def _sigmoid(x):
    return torch.sigmoid(x)

def _layernorm(x, axis=-1, eps=1e-05):
    m = x.mean(dim=axis, keepdim=True)
    v = x.var(dim=axis, keepdim=True, unbiased=False)
    return (x - m) / torch.sqrt(v + eps)

def _scatter_add(tgt, idx, src, axis=0):
    return tgt.clone().index_add_(axis, idx, src)

def _segment_sum(src, seg, n):
    out = torch.zeros((n,) + src.shape[1:], dtype=src.dtype)
    return out.index_add_(0, seg, src)

def _pad(x, pw):
    flat = tuple((v for pair in reversed(pw) for v in pair))
    return torch.nn.functional.pad(x, flat)
kernel_name = 'adaLN_cond__spectral_gfnet__glu_gated_bilinear__prompt'

def setup_inputs():

    _w = lambda a: (lambda t: t.to(torch.bfloat16) if t.is_floating_point() else t)(torch.as_tensor(a))

    def _A_setup():
        B, S, D = (1, 2048, 576)
        Df = D // 2 + 1
        return {'x': _w((lambda *s: torch.randn(*s, dtype=torch.bfloat16) * 0.05)(B, S, D)), 'w_r': _w((lambda *s: torch.randn(*s, dtype=torch.bfloat16) * 3.2)(S, Df) * 0.02), 'w_i': _w((lambda *s: torch.randn(*s, dtype=torch.bfloat16) * 3.2)(S, Df) * 0.02)}
    _A_in = _A_setup()

    def _B_setup():
        B, S, D, F = (1, 2048, 576, 4608)
        return {'x': _w((lambda *s: torch.randn(*s, dtype=torch.bfloat16) * 0.05)(B, S, D)), 'w_gate': _w((lambda *s: torch.randn(*s, dtype=torch.bfloat16) * 0.4)(D, F) * D ** (-0.5)), 'w_up': _w((lambda *s: torch.randn(*s, dtype=torch.bfloat16) * 0.4)(D, F) * D ** (-0.5)), 'w_down': _w((lambda *s: torch.randn(*s, dtype=torch.bfloat16) * 0.4)(F, D) * F ** (-0.5))}
    _B_in = _B_setup()
    return {'A_x': _A_in['x'], 'A_w_r': _A_in['w_r'], 'A_w_i': _A_in['w_i'], 'B_x': _B_in['x'], 'B_w_gate': _B_in['w_gate'], 'B_w_up': _B_in['w_up'], 'B_w_down': _B_in['w_down'], 'c': _w((lambda *s: torch.randn(*s, dtype=torch.bfloat16) * 0.1)(1, 576) * 8.0), 'w_mod': _w((lambda *s: torch.randn(*s, dtype=torch.bfloat16) * 0.1)(576, 6 * 576) * 576 ** (-0.5) * 8.0)}

def reference(A_x, A_w_r, A_w_i, B_x, B_w_gate, B_w_up, B_w_down, c, w_mod):

    def _A_ref(x, w_r, w_i):
        B, S, D = x.shape
        X = (lambda x, axes=(-2, -1): torch.fft.rfft2(x.float(), dim=axes))(x, axes=(-2, -1))
        W = w_r + 1j * w_i
        X = X * W
        y = (lambda x, s, axes=(-2, -1): torch.fft.irfft2(x, s=s, dim=axes).to(torch.bfloat16))(X, (S, D), axes=(-2, -1))
        return y

    def _B_ref(x, w_gate, w_up, w_down):
        g = torch.einsum('bsd,df->bsf', x, w_gate)
        u = torch.einsum('bsd,df->bsf', x, w_up)
        h = g * u
        return torch.einsum('bsf,fd->bsd', h, w_down)
    x = A_x
    mod = (_silu(c) @ w_mod).reshape(c.shape[0], 6, 1, -1)
    sa, ca, ga, sb, cb, gb = (mod[:, 0].clone(), mod[:, 1].clone(), mod[:, 2].clone(), mod[:, 3].clone(), mod[:, 4].clone(), mod[:, 5].clone())
    h = _layernorm(x, axis=-1) * (1 + ca) + sa
    x = x + ga * _A_ref(h, A_w_r, A_w_i)
    h = _layernorm(x, axis=-1) * (1 + cb) + sb
    x = x + gb * _B_ref(h, B_w_gate, B_w_up, B_w_down)
    return x
```

```cpp
#include <hip/hip_runtime.h>
#include <hip/hip_bf16.h>
#include <stdint.h>

typedef unsigned short u16;
constexpr int S = 2048, D = 576, DF = 289, F = 4608;

__device__ __forceinline__ float bf2f(u16 v) { return __uint_as_float(((unsigned)v) << 16); }
__device__ __forceinline__ u16 f2bf(float f) { unsigned u = __float_as_uint(f); return (u16)((u + 0x7FFF + ((u >> 16) & 1)) >> 16); }
__device__ __forceinline__ float rbf(float f) { return bf2f(f2bf(f)); }

__global__ void k_mod(const u16* c, const u16* wm, float* mod) {
  int j = blockIdx.x * blockDim.x + threadIdx.x;
  if (j >= 6 * D) return;
  float acc = 0.f;
  for (int k = 0; k < D; ++k) {
    float cv = bf2f(c[k]);
    float s = rbf(cv / (1.f + expf(-cv)));
    acc += s * bf2f(wm[k * 6 * D + j]);
  }
  mod[j] = rbf(acc);
}

__global__ void k_ln(const u16* x, const float* sh, const float* sc, float* h, u16* hb) {
  int s = blockIdx.x, t = threadIdx.x;
  float v[9]; float sum = 0.f;
  for (int i = 0; i < 9; ++i) { v[i] = bf2f(x[s * D + t + 64 * i]); sum += v[i]; }
  for (int o = 32; o; o >>= 1) sum += __shfl_xor(sum, o);
  float m = sum / D; float sq = 0.f;
  for (int i = 0; i < 9; ++i) { float dd = v[i] - m; sq += dd * dd; }
  for (int o = 32; o; o >>= 1) sq += __shfl_xor(sq, o);
  float r = rsqrtf(sq / D + 1e-5f);
  for (int i = 0; i < 9; ++i) {
    int d = t + 64 * i;
    float o = rbf((v[i] - m) * r * (1.f + sc[d]) + sh[d]);
    if (h) h[s * D + d] = o;
    if (hb) hb[s * D + d] = f2bf(o);
  }
}

__global__ void k_dft_d(const float* h, float2* X) {
  __shared__ float ct[D], st[D], row[D];
  int s = blockIdx.x;
  for (int i = threadIdx.x; i < D; i += blockDim.x) { ct[i] = cospif(2.f * i / D); st[i] = sinpif(2.f * i / D); row[i] = h[s * D + i]; }
  __syncthreads();
  for (int k = threadIdx.x; k < DF; k += blockDim.x) {
    float re = 0, im = 0; int idx = 0;
    for (int d = 0; d < D; ++d) { re += row[d] * ct[idx]; im -= row[d] * st[idx]; idx += k; if (idx >= D) idx -= D; }
    X[s * DF + k] = make_float2(re, im);
  }
}

__global__ void k_dft_s(const float2* X, float2* Y, const u16* wr, const u16* wi, int inv) {
  __shared__ float ct[S], st[S]; __shared__ float2 col[S];
  int k = blockIdx.x;
  for (int i = threadIdx.x; i < S; i += blockDim.x) { ct[i] = cospif(2.f * i / S); st[i] = sinpif(2.f * i / S) * (inv ? 1.f : -1.f); col[i] = X[i * DF + k]; }
  __syncthreads();
  int k1 = blockIdx.y * 256 + threadIdx.x;
  float re = 0, im = 0; int idx = 0;
  for (int s = 0; s < S; ++s) { float2 v = col[s]; float c = ct[idx], sn = st[idx]; re += v.x * c - v.y * sn; im += v.x * sn + v.y * c; idx = (idx + k1) & (S - 1); }
  if (!inv) { float a = bf2f(wr[k1 * DF + k]), b = bf2f(wi[k1 * DF + k]); float r2 = re * a - im * b, i2 = re * b + im * a; re = r2; im = i2; }
  Y[k1 * DF + k] = make_float2(re, im);
}

__global__ void k_idft_d(const float2* X, const u16* x, const float* ga, u16* x1) {
  __shared__ float ct[D], st[D]; __shared__ float2 row[DF];
  int s = blockIdx.x;
  for (int i = threadIdx.x; i < D; i += blockDim.x) { ct[i] = cospif(2.f * i / D); st[i] = sinpif(2.f * i / D); }
  for (int i = threadIdx.x; i < DF; i += blockDim.x) row[i] = X[s * DF + i];
  __syncthreads();
  for (int d = threadIdx.x; d < D; d += blockDim.x) {
    float acc = row[0].x + ((d & 1) ? -row[288].x : row[288].x); int idx = d;
    for (int k = 1; k < 288; ++k) { acc += 2.f * (row[k].x * ct[idx] - row[k].y * st[idx]); idx += d; if (idx >= D) idx -= D; }
    float y = rbf(acc * (1.f / (float)(S * D)));
    x1[s * D + d] = f2bf(bf2f(x[s * D + d]) + rbf(ga[d] * y));
  }
}

__global__ void k_g1(const u16* h, const u16* wg, const u16* wu, u16* H) {
  __shared__ float row[8][D];
  int s0 = blockIdx.y * 8; int f = blockIdx.x * 256 + threadIdx.x;
  for (int i = threadIdx.x; i < 8 * D; i += 256) row[i / D][i % D] = bf2f(h[s0 * D + i]);
  __syncthreads();
  float g[8] = {0}, u[8] = {0};
  for (int k = 0; k < D; ++k) { float a = bf2f(wg[k * F + f]), b = bf2f(wu[k * F + f]);
    #pragma unroll
    for (int r = 0; r < 8; ++r) { g[r] += row[r][k] * a; u[r] += row[r][k] * b; } }
  #pragma unroll
  for (int r = 0; r < 8; ++r) H[(s0 + r) * F + f] = f2bf(rbf(g[r]) * rbf(u[r]));
}

__global__ void k_g2(const u16* H, const u16* wd, const u16* x1, const float* gb, u16* out) {
  __shared__ float row[2][F];
  int s0 = blockIdx.x * 2; int d = threadIdx.x;
  for (int i = threadIdx.x; i < 2 * F; i += blockDim.x) row[i / F][i % F] = bf2f(H[s0 * F + i]);
  __syncthreads();
  float a0 = 0, a1 = 0;
  for (int k = 0; k < F; ++k) { float w = bf2f(wd[k * D + d]); a0 += row[0][k] * w; a1 += row[1][k] * w; }
  out[s0 * D + d] = f2bf(bf2f(x1[s0 * D + d]) + rbf(gb[d] * rbf(a0)));
  out[(s0 + 1) * D + d] = f2bf(bf2f(x1[(s0 + 1) * D + d]) + rbf(gb[d] * rbf(a1)));
}

extern "C" void kernel_launch(void* const* d_in, const int* in_sizes, int n_in,
                              void* d_out, int out_size, void* d_ws, size_t ws_size,
                              hipStream_t stream) {
  const u16* x = (const u16*)d_in[0]; const u16* wr = (const u16*)d_in[1]; const u16* wi = (const u16*)d_in[2];
  const u16* wg = (const u16*)d_in[4]; const u16* wu = (const u16*)d_in[5]; const u16* wd = (const u16*)d_in[6];
  const u16* c = (const u16*)d_in[7]; const u16* wm = (const u16*)d_in[8];
  char* ws = (char*)d_ws;
  float* mod = (float*)ws; ws += 16384;
  float* h = (float*)ws; ws += (size_t)S * D * 4;
  float2* X = (float2*)ws; ws += (size_t)S * DF * 8;
  float2* Y = (float2*)ws; ws += (size_t)S * DF * 8;
  u16* x1 = (u16*)ws; ws += (size_t)S * D * 2;
  u16* h2 = (u16*)ws; ws += (size_t)S * D * 2;
  u16* H = (u16*)ws; ws += (size_t)S * F * 2;
  k_mod<<<(6 * D + 63) / 64, 64, 0, stream>>>(c, wm, mod);
  k_ln<<<S, 64, 0, stream>>>(x, mod + 0 * D, mod + 1 * D, h, nullptr);
  k_dft_d<<<S, 320, 0, stream>>>(h, X);
  k_dft_s<<<dim3(DF, S / 256), 256, 0, stream>>>(X, Y, wr, wi, 0);
  k_dft_s<<<dim3(DF, S / 256), 256, 0, stream>>>(Y, X, wr, wi, 1);
  k_idft_d<<<S, 576, 0, stream>>>(X, x, mod + 2 * D, x1);
  k_ln<<<S, 64, 0, stream>>>(x1, mod + 3 * D, mod + 4 * D, nullptr, h2);
  k_g1<<<dim3(F / 256, S / 8), 256, 0, stream>>>(h2, wg, wu, H);
  k_g2<<<S / 2, 576, 0, stream>>>(H, wd, x1, mod + 5 * D, (u16*)d_out);
}
```

```cpp
#include <hip/hip_runtime.h>
#define MEGA_ASM_0 \
  "START_%=:\n" \
  "s_load_dwordx2 s[64:65], %0, 56\n" \
  "s_load_dwordx2 s[56:57], %0, 0\n" \
  "s_load_dwordx2 s[60:61], %0, 64\n" \
  "s_load_dwordx2 s[24:25], %0, 48\n" \
  "s_load_dwordx2 s[28:29], %0, 8\n" \
  "s_load_dwordx2 s[96:97], %0, 16\n" \
  "s_load_dwordx2 s[36:37], %0, 24\n" \
  "s_load_dwordx2 s[40:41], %0, 32\n" \
  "s_load_dwordx2 s[48:49], %0, 40\n" \
  "s_load_dwordx2 s[68:69], %0, 72\n" \
  "s_memrealtime s[94:95]\n" \
  "s_getpc_b64 s[100:101]\n" \
  "PC_%=:\n" \
  "v_and_b32 v11, 63, %2\n" \
  "v_lshrrev_b32 v32, 6, %2\n" \
  "s_and_b32 s78, %1, 7\n" \
  "v_readfirstlane_b32 s80, v32\n" \
  "s_lshr_b32 s81, %1, 3\n" \
  "s_and_b32 s77, s81, 3\n" \
  "s_lshr_b32 s79, s81, 2\n" \
  "s_lshl_b32 s72, s80, 10\n" \
  "v_and_b32 v254, 15, v11\n" \
  "v_lshrrev_b32 v255, 4, v11\n" \
  "s_lshr_b32 s82, %1, 3\n" \
  "s_lshl_b32 s4, s78, 5\n" \
  "s_add_u32 s4, s4, s82\n" \
  "s_mov_b32 s20, %1\n" \
  "s_lshl_b32 s81, s82, 2\n" \
  "s_add_u32 s81, s81, s80\n" \
  "s_lshl_b32 s81, s81, 2\n" \
  "v_add_lshl_u32 v32, v255, s81, 7\n" \
  "v_min_u32 v32, END_%=-PC_%=-4, v32\n" \
  "global_load_dword v252, v32, s[100:101]\n" \
  "s_mov_b32 s66, 3981312\n" \
  "s_mov_b32 s67, 0x20000\n" \
  "s_mov_b32 s58, 2359296\n" \
  "s_mov_b32 s59, 0x20000\n" \
  "s_mov_b32 s62, 54788096\n" \
  "s_mov_b32 s63, 0x20000\n" \
  "s_mov_b32 s26, 1152\n" \
  "s_mov_b32 s27, 0x20000\n" \
  "s_mov_b32 s30, 1183744\n" \
  "s_mov_b32 s31, 0x20000\n" \
  "s_mov_b32 s98, 1183744\n" \
  "s_mov_b32 s99, 0x20000\n" \
  "s_mov_b32 s38, 5308416\n" \
  "s_mov_b32 s39, 0x20000\n" \
  "s_mov_b32 s42, 5308416\n" \
  "s_mov_b32 s43, 0x20000\n" \
  "s_mov_b32 s50, 5308416\n" \
  "s_mov_b32 s51, 0x20000\n" \
  "s_mov_b32 s46, 5308416\n" \
  "s_mov_b32 s47, 0x20000\n" \
  "s_mov_b32 s70, 2359296\n" \
  "s_mov_b32 s71, 0x20000\n" \
  "s_mov_b32 s54, 73728\n" \
  "s_mov_b32 s55, 0x20000\n" \
  "v_bfe_u32 v32, v11, 2, 3\n" \
  "s_lshl_b32 s81, s80, 3\n" \
  "v_add_u32 v32, s81, v32\n" \
  "v_bfe_u32 v34, v32, 2, 2\n" \
  "v_and_b32 v33, 3, v32\n" \
  "v_lshl_add_u32 v33, v34, 3, v33\n" \
  "v_lshrrev_b32 v34, 4, v32\n" \
  "v_lshl_add_u32 v33, v34, 2, v33\n" \
  "v_mul_u32_u24 v250, 6912, v33\n" \
  "v_lshrrev_b32 v32, 3, v11\n" \
  "v_and_b32 v33, 2, v32\n" \
  "v_xor_b32 v33, v33, v11\n" \
  "v_and_b32 v33, 3, v33\n" \
  "v_and_or_b32 v33, v32, 4, v33\n" \
  "v_lshl_add_u32 v250, v33, 4, v250\n" \
  "s_and_b32 s81, %1, 1\n" \
  "s_mul_i32 s5, s81, 576\n" \
  "s_mul_i32 s81, s81, 1990656\n" \
  "s_lshr_b32 s6, %1, 1\n" \
  "s_lshl_b32 s6, s6, 7\n" \
  "s_add_u32 s81, s81, s6\n" \
  "v_add_u32 v250, s81, v250\n" \
  "v_lshlrev_b32 v248, 2, %2\n" \
  "v_add_u32 v35, s5, v248\n" \
  "v_and_b32 v251, 31, v11\n" \
  "v_lshrrev_b32 v32, 5, %2\n" \
  "v_mul_u32_u24 v249, 294912, v32\n" \
  "v_lshl_add_u32 v249, v251, 2, v249\n" \
  "s_mul_i32 s83, s4, 1152\n" \
  "s_sub_u32 s81, s82, 16\n" \
  "s_cselect_b32 s6, 1073741824, 0\n" \
  "s_and_b32 s81, s81, 15\n" \
  "s_lshl_b32 s81, s81, 4\n" \
  "v_lshrrev_b32 v32, 4, %2\n" \
  "v_add_u32 v32, s81, v32\n" \
  "v_mul_u32_u24 v32, 4624, v32\n" \
  "v_lshlrev_b32 v33, 7, v254\n" \
  "v_min_u32 v33, 590, v33\n" \
  "s_mul_i32 s81, s78, 578\n" \
  "s_add_u32 s81, s81, s6\n" \
  "v_add3_u32 v32, v32, v33, s81\n" \
  "v_max_u32 v32, 2, v32\n" \
  "v_subrev_u32 v32, 2, v32\n" \
  "v_add_u32 v33, 10, v254\n" \
  "v_lshrrev_b32 v33, 4, v33\n" \
  "v_lshl_add_u32 v247, v33, 29, v32\n" \
  "s_sub_u32 s81, %1, 112\n" \
  "s_and_b32 s6, s81, 1\n" \
  "s_mul_i32 s6, s6, 288\n" \
  "s_mul_i32 s7, s80, 36\n" \
  "s_add_u32 s6, s6, s7\n" \
  "s_add_u32 s6, s6, 144\n" \
  "v_add_u32 v246, s6, v11\n" \
  "v_mul_u32_u24 v246, 6912, v246\n" \
  "s_lshr_b32 s6, s81, 1\n" \
  "s_lshl_b32 s6, s6, 7\n" \
  "s_cmp_lt_u32 s81, 36\n" \
  "s_cselect_b32 s6, s6, 1073741824\n" \
  "v_add_u32 v33, 28, v11\n" \
  "v_lshrrev_b32 v33, 6, v33\n" \
  "v_lshl_add_u32 v246, v33, 29, v246\n" \
  "v_add_u32 v246, s6, v246\n" \
  "s_cmp_lt_u32 %1, 36\n" \
  "s_waitcnt lgkmcnt(0)\n" \
  "s_load_dword s84, s[60:61], 8192\n" \
  "v_mov_b32 v244, v35\n" \
  "buffer_load_dword v245, v244, s[24:27], 0 offen\n" \
  "s_cbranch_scc0 NP0_%=\n" \
  "s_add_u32 m0, s72, 81920\n" \
  "s_mov_b32 s81, 0\n" \
  "buffer_load_dwordx4 v250, s[64:67], s81 offen  lds\n" \
  "s_add_u32 m0, s72, 86016\n" \
  "s_mov_b32 s81, 221184\n" \
  "buffer_load_dwordx4 v250, s[64:67], s81 offen  lds\n" \
  "s_add_u32 m0, s72, 90112\n" \
  "s_mov_b32 s81, 442368\n" \
  "buffer_load_dwordx4 v250, s[64:67], s81 offen  lds\n" \
  "s_add_u32 m0, s72, 94208\n" \
  "s_mov_b32 s81, 663552\n" \
  "buffer_load_dwordx4 v250, s[64:67], s81 offen  lds\n" \
  "s_add_u32 m0, s72, 98304\n" \
  "s_mov_b32 s81, 884736\n" \
  "buffer_load_dwordx4 v250, s[64:67], s81 offen  lds\n" \
  "s_add_u32 m0, s72, 102400\n" \
  "s_mov_b32 s81, 1105920\n" \
  "buffer_load_dwordx4 v250, s[64:67], s81 offen  lds\n" \
  "s_add_u32 m0, s72, 106496\n" \
  "s_mov_b32 s81, 1327104\n" \
  "buffer_load_dwordx4 v250, s[64:67], s81 offen  lds\n" \
  "s_add_u32 m0, s72, 110592\n" \
  "s_mov_b32 s81, 1548288\n" \
  "buffer_load_dwordx4 v250, s[64:67], s81 offen  lds\n" \
  "s_add_u32 m0, s72, 114688\n" \
  "s_mov_b32 s81, 1769472\n" \
  "buffer_load_dwordx4 v250, s[64:67], s81 offen  lds\n" \
  "s_branch J0_%=\n" \
  "NP0_%=:\n" \
  "buffer_load_dword v252, v246, s[64:67], 0 offen\n" \
  "J0_%=:\n" \
  "v_lshlrev_b32 v1, 2, v11\n" \
  "v_lshrrev_b32 v32, 5, v11\n" \
  "v_lshl_add_u32 v1, v32, 30, v1\n" \
  "v_lshlrev_b32 v24, 4, v11\n" \
  "v_lshlrev_b32 v8, 3, v11\n" \
  "v_add_u32 v25, 4096, v24\n" \
  "v_lshrrev_b32 v32, 5, v11\n" \
  "v_lshlrev_b32 v2, 10, v32\n" \
  "v_bfe_u32 v32, v11, 2, 3\n" \
  "v_lshl_add_u32 v2, v32, 6, v2\n" \
  "v_bfe_u32 v32, v11, 4, 1\n" \
  "v_lshl_add_u32 v2, v32, 5, v2\n" \
  "v_and_b32 v32, 3, v11\n" \
  "v_lshl_add_u32 v2, v32, 3, v2\n" \
  "v_xor_b32 v3, 32, v2\n" \
  "v_bfe_u32 v32, v11, 2, 3\n" \
  "s_lshl_b32 s81, s80, 3\n" \
  "v_add_u32 v32, s81, v32\n" \
  "v_mul_u32_u24 v29, 1152, v32\n" \
  "v_bfe_u32 v34, v32, 2, 2\n" \
  "v_and_b32 v33, 3, v32\n" \
  "v_lshl_add_u32 v33, v34, 3, v33\n" \
  "v_lshrrev_b32 v34, 4, v32\n" \
  "v_lshl_add_u32 v33, v34, 2, v33\n" \
  "v_mul_u32_u24 v12, 9216, v33\n" \
  "v_lshrrev_b32 v32, 3, v11\n" \
  "v_xor_b32 v32, v32, v11\n" \
  "v_and_b32 v32, 2, v32\n" \
  "v_and_b32 v33, 1, v11\n" \
  "v_or_b32 v32, v32, v33\n" \
  "v_lshrrev_b32 v33, 5, v11\n" \
  "v_lshl_add_u32 v32, v33, 2, v32\n" \
  "v_lshl_add_u32 v12, v32, 4, v12\n" \
  "v_lshl_add_u32 v29, v32, 4, v29\n" \
  "s_mul_i32 s81, s78, 1152\n" \
  "s_lshl_b32 s82, s77, 8\n" \
  "s_add_u32 s74, s81, s82\n" \
  "v_add_u32 v12, s74, v12\n" \
  "v_add_u32 v13, 294912, v12\n" \
  "s_lshl_b32 s81, s78, 2\n" \
  "s_add_u32 s81, s81, s77\n" \
  "s_mul_i32 s81, s81, 55296\n" \
  "s_and_b32 s82, s80, 1\n" \
  "s_mul_i32 s74, s82, 18432\n" \
  "s_lshl_b32 s82, s82, 10\n" \
  "s_add_u32 s82, s82, s81\n" \
  "v_add_u32 v31, s82, v24\n" \
  "s_add_u32 s81, s81, s74\n" \
  "s_lshr_b32 s82, s80, 1\n" \
  "s_lshl_b32 s82, s82, 10\n" \
  "s_add_u32 s81, s81, s82\n" \
  "v_add_u32 v14, s81, v24\n" \
  "v_lshlrev_b32 v253, 4, %2\n" \
  "s_bitcmp1_b32 s80, 0\n" \
  "s_cselect_b64 s[44:45], s[40:41], s[36:37]\n" \
  "s_lshl_b32 s81, s79, 2\n" \
  "s_add_u32 s81, s81, s80\n" \
  "s_mul_i32 s82, s81, 73728\n" \
  "s_add_u32 s82, s82, 2097152\n" \
  "s_add_u32 s52, s60, s82\n" \
  "s_addc_u32 s53, s61, 0\n" \
  "s_lshl_b32 s75, s81, 10\n" \
  "s_add_u32 s75, s75, 262144\n" \
  "v_cvt_f32_u32 v246, v255\n" \
  "v_cvt_f32_u32 v231, v254\n" \
  "v_mul_f32 v246, 4.0, v246\n" \
  "v_and_b32 v32, 3, v254\n" \
  "v_cvt_f32_u32 v243, v32\n" \
  "v_lshrrev_b32 v33, 2, v254\n" \
  "v_cvt_f32_u32 v242, v33\n" \
  "v_bfe_i32 v230, v255, 0, 1\n" \
  "v_bfe_i32 v229, v33, 0, 1\n" \
  "v_and_b32 v230, 0x80008000, v230\n" \
  "v_and_b32 v229, 0x80008000, v229\n" \
  "s_add_u32 s81, s80, 1\n" \
  "v_cvt_f32_u32 v228, s81\n" \
  "s_cmp_lt_u32 s20, 36\n" \
  "s_cbranch_scc1 XL_%=\n" \
  "buffer_load_dword v232, v249, s[56:59], s83 offen offset:0 \n" \
  "buffer_load_dword v233, v249, s[56:59], s83 offen offset:128 \n" \
  "buffer_load_dword v234, v249, s[56:59], s83 offen offset:256 \n" \
  "buffer_load_dword v235, v249, s[56:59], s83 offen offset:384 \n" \
  "buffer_load_dword v236, v249, s[56:59], s83 offen offset:512 \n" \
  "buffer_load_dword v237, v249, s[56:59], s83 offen offset:640 \n" \
  "buffer_load_dword v238, v249, s[56:59], s83 offen offset:768 \n" \
  "buffer_load_dword v239, v249, s[56:59], s83 offen offset:896 \n" \
  "buffer_load_dword v240, v249, s[56:59], s83 offen offset:1024 \n" \
  "XL_%=:\n" \
  "s_sub_u32 s85, s20, 36\n" \
  "s_cmp_ge_u32 s85, 72\n" \
  "s_cbranch_scc1 ON1_%=\n" \
  "s_add_u32 m0, s72, 81920\n" \
  "s_mov_b32 s81, 0\n" \
  "buffer_load_dwordx4 v250, s[64:67], s81 offen  lds\n" \
  "s_add_u32 m0, s72, 86016\n" \
  "s_mov_b32 s81, 221184\n" \
  "buffer_load_dwordx4 v250, s[64:67], s81 offen  lds\n" \
  "s_add_u32 m0, s72, 90112\n" \
  "s_mov_b32 s81, 442368\n" \
  "buffer_load_dwordx4 v250, s[64:67], s81 offen  lds\n" \
  "s_add_u32 m0, s72, 94208\n" \
  "s_mov_b32 s81, 663552\n" \
  "buffer_load_dwordx4 v250, s[64:67], s81 offen  lds\n" \
  "s_add_u32 m0, s72, 98304\n" \
  "s_mov_b32 s81, 884736\n" \
  "buffer_load_dwordx4 v250, s[64:67], s81 offen  lds\n" \
  "s_add_u32 m0, s72, 102400\n" \
  "s_mov_b32 s81, 1105920\n" \
  "buffer_load_dwordx4 v250, s[64:67], s81 offen  lds\n" \
  "s_add_u32 m0, s72, 106496\n" \
  "s_mov_b32 s81, 1327104\n" \
  "buffer_load_dwordx4 v250, s[64:67], s81 offen  lds\n" \
  "s_add_u32 m0, s72, 110592\n" \
  "s_mov_b32 s81, 1548288\n" \
  "buffer_load_dwordx4 v250, s[64:67], s81 offen  lds\n" \
  "s_add_u32 m0, s72, 114688\n" \
  "s_mov_b32 s81, 1769472\n" \
  "buffer_load_dwordx4 v250, s[64:67], s81 offen  lds\n" \
  "ON1_%=:\n" \
  "v_mul_f32 v32, v246, v242\n" \
  "v_mul_f32 v32, 0x3d000000, v32\n" \
  "v_mul_f32 v34, 0x3e800000, v243\n" \
  "v_fmamk_f32 v33, v228, 0x3ce38e39, v34\n" \
  "v_add_f32 v45, v32, v33\n" \
  "v_add_f32 v46, v45, v33\n" \
  "v_add_f32 v47, v46, v33\n" \
  "v_cos_f32 v48, v32\n" \
  "v_sin_f32 v44, v32\n" \
  "v_cos_f32 v49, v45\n" \
  "v_sin_f32 v45, v45\n" \
  "v_cos_f32 v50, v46\n" \
  "v_sin_f32 v46, v46\n" \
  "v_cos_f32 v51, v47\n" \
  "v_sin_f32 v47, v47\n" \
  "v_cvt_pk_f16_f32 v52, v48, v44\n" \
  "v_cvt_pk_f16_f32 v56, -v44, v48\n" \
  "v_cvt_pk_f16_f32 v53, v49, v45\n" \
  "v_cvt_pk_f16_f32 v57, -v45, v49\n" \
  "v_cvt_pk_f16_f32 v54, v50, v46\n" \
  "v_cvt_pk_f16_f32 v58, -v46, v50\n" \
  "v_cvt_pk_f16_f32 v55, v51, v47\n" \
  "v_cvt_pk_f16_f32 v59, -v47, v51\n" \
  "v_xor_b32 v36, v230, v229\n" \
  "v_xor_b32 v44, v229, v52\n" \
  "v_xor_b32 v45, v229, v53\n" \
  "v_xor_b32 v46, v229, v54\n" \
  "v_xor_b32 v47, v229, v55\n" \
  "v_xor_b32 v48, v229, v56\n" \
  "v_xor_b32 v49, v229, v57\n" \
  "v_xor_b32 v50, v229, v58\n" \
  "v_xor_b32 v51, v229, v59\n" \
  "v_xor_b32 v60, v230, v52\n" \
  "v_xor_b32 v61, v230, v53\n" \
  "v_xor_b32 v62, v230, v54\n" \
  "v_xor_b32 v63, v230, v55\n" \
  "v_xor_b32 v64, v230, v56\n" \
  "v_xor_b32 v65, v230, v57\n" \
  "v_xor_b32 v66, v230, v58\n" \
  "v_xor_b32 v67, v230, v59\n" \
  "v_xor_b32 v68, v36, v52\n" \
  "v_xor_b32 v69, v36, v53\n" \
  "v_xor_b32 v70, v36, v54\n" \
  "v_xor_b32 v71, v36, v55\n" \
  "v_xor_b32 v72, v36, v56\n" \
  "v_xor_b32 v73, v36, v57\n" \
  "v_xor_b32 v74, v36, v58\n" \
  "v_xor_b32 v75, v36, v59\n" \
  "s_bitcmp1_b32 s80, 0\n" \
  "s_cselect_b32 s81, 0x3e000000, 0\n" \
  "v_fma_f32 v32, v246, s81, v32\n" \
  "v_add_f32 v77, v32, v34\n" \
  "v_add_f32 v78, v77, v34\n" \
  "v_add_f32 v79, v78, v34\n" \
  "v_cos_f32 v80, v32\n" \
  "v_sin_f32 v76, v32\n" \
  "v_cos_f32 v81, v77\n" \
  "v_sin_f32 v77, v77\n" \
  "v_cos_f32 v82, v78\n" \
  "v_sin_f32 v78, v78\n" \
  "v_cos_f32 v83, v79\n" \
  "v_sin_f32 v79, v79\n" \
  "v_cvt_pk_f16_f32 v84, v80, v76\n" \
  "v_cvt_pk_f16_f32 v88, -v76, v80\n" \
  "v_cvt_pk_f16_f32 v85, v81, v77\n" \
  "v_cvt_pk_f16_f32 v89, -v77, v81\n" \
  "v_cvt_pk_f16_f32 v86, v82, v78\n" \
  "v_cvt_pk_f16_f32 v90, -v78, v82\n" \
  "v_cvt_pk_f16_f32 v87, v83, v79\n" \
  "v_cvt_pk_f16_f32 v91, -v79, v83\n" \
  "v_xor_b32 v76, v229, v84\n" \
  "v_xor_b32 v77, v229, v85\n" \
  "v_xor_b32 v78, v229, v86\n" \
  "v_xor_b32 v79, v229, v87\n" \
  "v_xor_b32 v80, v229, v88\n" \
  "v_xor_b32 v81, v229, v89\n" \
  "v_xor_b32 v82, v229, v90\n" \
  "v_xor_b32 v83, v229, v91\n" \
  "s_cmp_ge_u32 s20, 36\n" \
  "s_cbranch_scc1 ON2_%=\n" \
  "s_and_b32 s81, s80, 1\n" \
  "s_lshl_b32 s81, s81, 5\n" \
  "s_and_b32 s82, s80, 2\n" \
  "s_lshl_b32 s82, s82, 8\n" \
  "s_or_b32 s81, s81, s82\n" \
  "v_xor_b32 v227, s81, v2\n" \
  "v_add_u32 v227, 81920, v227\n" \
  "v_lshlrev_b32 v226, 4, v255\n" \
  "v_add_u32 v226, 118784, v226\n" \
  "v_add_u32 v32, 118784, v248\n" \
  "s_waitcnt vmcnt(9)\n" \
  "v_lshlrev_b32 v16, 16, v245\n" \
  "v_and_b32 v17, 0xffff0000, v245\n" \
  "v_mul_f32 v18, 0xbfb8aa3b, v16\n" \
  "v_mul_f32 v19, 0xbfb8aa3b, v17\n" \
  "v_exp_f32 v18, v18\n" \
  "v_exp_f32 v19, v19\n" \
  "v_add_f32 v18, 1.0, v18\n" \
  "v_add_f32 v19, 1.0, v19\n" \
  "v_rcp_f32 v18, v18\n" \
  "v_rcp_f32 v19, v19\n" \
  "v_lshlrev_b32 v225, 5, v255\n" \
  "v_mul_f32 v16, v16, v18\n" \
  "v_mul_f32 v17, v17, v19\n" \
  "v_cvt_pk_bf16_f32 v16, v16, v17\n" \
  "ds_write_b32 v32, v16\n" \
  "v_lshl_add_u32 v225, v254, 4, v225\n" \
  "v_lshrrev_b32 v32, 1, v254\n" \
  "v_lshl_add_u32 v225, v32, 27, v225\n" \
  "s_lshl_b32 s5, s20, 2\n" \
  "s_add_u32 s5, s5, s80\n" \
  "s_lshl_b32 s5, s5, 7\n" \
  "s_add_u32 s5, s5, 131072\n" \
  "s_waitcnt lgkmcnt(0)\n" \
  "s_barrier\n" \
  "ds_read_b128 a[20:23], v226 offset:0\n" \
  "ds_read_b128 a[24:27], v226 offset:64\n" \
  "ds_read_b128 a[28:31], v226 offset:128\n" \
  "ds_read_b128 a[32:35], v226 offset:192\n" \
  "ds_read_b128 a[36:39], v226 offset:256\n" \
  "ds_read_b128 a[40:43], v226 offset:320\n" \
  "ds_read_b128 a[44:47], v226 offset:384\n" \
  "ds_read_b128 a[48:51], v226 offset:448\n" \
  "ds_read_b128 a[52:55], v226 offset:512\n" \
  "s_cmp_ge_u32 s20, 54\n" \
  "s_cselect_b32 s81, 72, 18\n" \
  "s_sub_u32 s81, s20, s81\n" \
  "s_cmp_lt_u32 s81, 18\n" \
  "s_cselect_b32 s81, 1.0, 0\n" \
  "s_bitcmp1_b32 s20, 0\n" \
  "s_cselect_b32 s81, 0, s81\n" \
  "v_mov_b32 v20, s81\n" \
  "v_mov_b32 v21, s81\n" \
  "v_mov_b32 v22, s81\n" \
  "v_mov_b32 v23, s81\n" \
  "buffer_load_dword v232, v249, s[56:59], s83 offen offset:0 \n" \
  "buffer_load_dword v233, v249, s[56:59], s83 offen offset:128 \n" \
  "buffer_load_dword v234, v249, s[56:59], s83 offen offset:256 \n" \
  "buffer_load_dword v235, v249, s[56:59], s83 offen offset:384 \n" \
  "buffer_load_dword v236, v249, s[56:59], s83 offen offset:512 \n" \
  "buffer_load_dword v237, v249, s[56:59], s83 offen offset:640 \n" \
  "buffer_load_dword v238, v249, s[56:59], s83 offen offset:768 \n" \
  "buffer_load_dword v239, v249, s[56:59], s83 offen offset:896 \n" \
  "buffer_load_dword v240, v249, s[56:59], s83 offen offset:1024 \n" \
  "ON2_%=:\n" \
  "s_waitcnt lgkmcnt(0)\n" \
  "s_add_u32 s84, s84, 1\n" \
  "v_mov_b32 v28, s84\n" \
  "s_cmp_ge_u32 s20, 36\n" \
  "s_cbranch_scc1 ON3_%=\n" \
  "v_and_b32 v32, 1, v254\n" \
  "v_cmp_eq_u32 vcc, 1, v32\n" \
  "v_mov_b32 v42, s84\n" \
  "v_mov_b32 v43, s84\n" \
  "s_waitcnt vmcnt(9)\n" \
  "s_waitcnt lgkmcnt(0)\n" \
  "s_barrier\n" \
  "ds_read_b64_tr_b16 a[0:1], v227 offset:0\n" \
  "ds_read_b64_tr_b16 a[2:3], v227 offset:2048\n" \
  "ds_read_b64_tr_b16 a[4:5], v227 offset:4096\n" \
  "ds_read_b64_tr_b16 a[6:7], v227 offset:6144\n" \
  "ds_read_b64_tr_b16 a[8:9], v227 offset:8192\n" \
  "ds_read_b64_tr_b16 a[10:11], v227 offset:10240\n" \
  "ds_read_b64_tr_b16 a[12:13], v227 offset:12288\n" \
  "ds_read_b64_tr_b16 a[14:15], v227 offset:14336\n" \
  "ds_read_b64_tr_b16 a[16:17], v227 offset:16384\n" \
  "ds_read_b64_tr_b16 a[18:19], v227 offset:18432\n" \
  "s_waitcnt lgkmcnt(8)\n" \
  "v_mfma_f32_16x16x32_bf16 v[20:23], a[0:3], a[20:23], v[20:23]\n" \
  "ds_read_b64_tr_b16 a[0:1], v227 offset:20480\n" \
  "ds_read_b64_tr_b16 a[2:3], v227 offset:22528\n" \
  "s_waitcnt lgkmcnt(8)\n" \
  "v_mfma_f32_16x16x32_bf16 v[20:23], a[4:7], a[24:27], v[20:23]\n" \
  "ds_read_b64_tr_b16 a[4:5], v227 offset:24576\n" \
  "ds_read_b64_tr_b16 a[6:7], v227 offset:26624\n" \
  "s_waitcnt lgkmcnt(8)\n" \
  "v_mfma_f32_16x16x32_bf16 v[20:23], a[8:11], a[28:31], v[20:23]\n" \
  "ds_read_b64_tr_b16 a[8:9], v227 offset:28672\n" \
  "ds_read_b64_tr_b16 a[10:11], v227 offset:30720\n" \
  "s_waitcnt lgkmcnt(8)\n" \
  "v_mfma_f32_16x16x32_bf16 v[20:23], a[12:15], a[32:35], v[20:23]\n" \
  "ds_read_b64_tr_b16 a[12:13], v227 offset:32768\n" \
  "ds_read_b64_tr_b16 a[14:15], v227 offset:34816\n" \
  "s_waitcnt lgkmcnt(8)\n" \
  "v_mfma_f32_16x16x32_bf16 v[20:23], a[16:19], a[36:39], v[20:23]\n" \
  "s_waitcnt lgkmcnt(6)\n" \
  "v_mfma_f32_16x16x32_bf16 v[20:23], a[0:3], a[40:43], v[20:23]\n" \
  "s_waitcnt lgkmcnt(4)\n" \
  "v_mfma_f32_16x16x32_bf16 v[20:23], a[4:7], a[44:47], v[20:23]\n" \
  "s_waitcnt lgkmcnt(2)\n" \
  "v_mfma_f32_16x16x32_bf16 v[20:23], a[8:11], a[48:51], v[20:23]\n" \
  "s_waitcnt lgkmcnt(0)\n" \
  "v_mfma_f32_16x16x32_bf16 v[20:23], a[12:15], a[52:55], v[20:23]\n" \
  "s_nop 7\n" \
  "v_cndmask_b32 v40, v20, v22, vcc\n" \
  "v_cndmask_b32 v41, v21, v23, vcc\n" \
  "buffer_store_dwordx4 v[40:43], v225, s[60:63], s5 offen sc1\n" \
  "s_nop 1\n" \
  "ON3_%=:\n" \
  "v_fma_f32 v33, 4.0, v243, v242\n" \
  "v_mul_f32 v33, 0x3d800000, v33\n" \
  "v_mul_f32 v32, v246, v33\n" \
  "v_add_f32 v93, v32, v33\n" \
  "v_add_f32 v94, v93, v33\n" \
  "v_add_f32 v95, v94, v33\n" \
  "v_cos_f32 v96, v32\n" \
  "v_sin_f32 v92, v32\n" \
  "v_cos_f32 v97, v93\n" \
  "v_sin_f32 v93, v93\n" \
  "v_cos_f32 v98, v94\n" \
  "v_sin_f32 v94, v94\n" \
  "v_cos_f32 v99, v95\n" \
  "v_sin_f32 v95, v95\n" \
  "v_cvt_pk_f16_f32 v100, v96, v92\n" \
  "v_cvt_pk_f16_f32 v104, -v92, v96\n" \
  "v_cvt_pk_f16_f32 v101, v97, v93\n" \
  "v_cvt_pk_f16_f32 v105, -v93, v97\n" \
  "v_cvt_pk_f16_f32 v102, v98, v94\n" \
  "v_cvt_pk_f16_f32 v106, -v94, v98\n" \
  "v_cvt_pk_f16_f32 v103, v99, v95\n" \
  "v_cvt_pk_f16_f32 v107, -v95, v99\n" \
  "v_mul_f32 v34, 0x3c800000, v243\n" \
  "v_fmamk_f32 v33, v228, 0x3ae38e39, v34\n" \
  "v_mul_f32 v32, v246, v33\n" \
  "v_add_f32 v93, v32, v33\n" \
  "v_add_f32 v94, v93, v33\n" \
  "v_add_f32 v95, v94, v33\n" \
  "v_cos_f32 v96, v32\n" \
  "v_sin_f32 v92, v32\n" \
  "v_cos_f32 v97, v93\n" \
  "v_sin_f32 v93, v93\n" \
  "v_cos_f32 v98, v94\n" \
  "v_sin_f32 v94, v94\n" \
  "v_cos_f32 v99, v95\n" \
  "v_sin_f32 v95, v95\n" \
  "s_mov_b32 s82, 0x3c715bef\n" \
  "v_pk_mul_f32 v[96:97], v[96:97], s[82:83] op_sel_hi:[1,0]\n" \
  "v_pk_mul_f32 v[98:99], v[98:99], s[82:83] op_sel_hi:[1,0]\n" \
  "v_pk_mul_f32 v[92:93], v[92:93], s[82:83] op_sel_hi:[1,0] neg_lo:[1,0] neg_hi:[1,0]\n" \
  "v_pk_mul_f32 v[94:95], v[94:95], s[82:83] op_sel_hi:[1,0] neg_lo:[1,0] neg_hi:[1,0]\n" \
  "v_mul_f32 v32, v246, v34\n" \
  "v_add_f32 v109, v32, v34\n" \
  "v_add_f32 v110, v109, v34\n" \
  "v_add_f32 v111, v110, v34\n" \
  "v_cos_f32 v112, v32\n" \
  "v_sin_f32 v108, v32\n" \
  "v_cos_f32 v113, v109\n" \
  "v_sin_f32 v109, v109\n" \
  "v_cos_f32 v114, v110\n" \
  "v_sin_f32 v110, v110\n" \
  "v_cos_f32 v115, v111\n" \
  "v_sin_f32 v111, v111\n" \
  "s_mov_b32 s82, 0x3c715bef\n" \
  "v_pk_mul_f32 v[112:113], v[112:113], s[82:83] op_sel_hi:[1,0]\n" \
  "v_pk_mul_f32 v[114:115], v[114:115], s[82:83] op_sel_hi:[1,0]\n" \
  "v_pk_mul_f32 v[108:109], v[108:109], s[82:83] op_sel_hi:[1,0] neg_lo:[1,0] neg_hi:[1,0]\n" \
  "v_pk_mul_f32 v[110:111], v[110:111], s[82:83] op_sel_hi:[1,0] neg_lo:[1,0] neg_hi:[1,0]\n" \
  "s_cmp_lt_u32 s20, 108\n" \
  "s_cbranch_scc1 ON4_%=\n" \
  "buffer_load_ushort v252, v247, s[28:31], 0 offen sc1\n" \
  "buffer_load_ushort v252, v247, s[96:99], 0 offen sc1\n" \
  "ON4_%=:\n" \
  "s_cmp_lt_u32 s20, 108\n" \
  "s_cbranch_scc1 ON5_%=\n" \
  "v_bfe_u32 v32, v11, 1, 3\n" \
  "v_lshl_add_u32 v32, s80, 3, v32\n" \
  "v_bfe_u32 v34, v32, 2, 2\n" \
  "v_and_b32 v33, 3, v32\n" \
  "v_lshl_add_u32 v33, v34, 3, v33\n" \
  "v_lshrrev_b32 v34, 4, v32\n" \
  "v_lshl_add_u32 v33, v34, 2, v33\n" \
  "s_lshr_b32 s6, s20, 3\n" \
  "s_sub_u32 s6, s6, 14\n" \
  "s_cselect_b32 s7, 1073741824, 0\n" \
  "s_cselect_b32 s6, 0, s6\n" \
  "v_and_b32 v34, 1, v11\n" \
  "v_lshl_add_u32 v34, v34, 4, s7\n" \
  "v_lshl_add_u32 v34, v255, 5, v34\n" \
  "v_mul_u32_u24 v33, 9216, v33\n" \
  "v_add_u32 v33, v33, v34\n" \
  "v_mul_u32_u24 v32, 1152, v32\n" \
  "v_add_u32 v32, v32, v34\n" \
  "s_mul_i32 s81, s6, 294912\n" \
  "s_mul_i32 s82, s78, 1152\n" \
  "s_add_u32 s81, s81, s82\n" \
  "s_mul_i32 s82, s78, 18\n" \
  "s_add_u32 s82, s82, s6\n" \
  "s_mul_i32 s82, s82, 36864\n" \
  "buffer_load_dwordx4 a[0:3], v33, s[36:39], s81 offen offset:1024 \n" \
  "buffer_load_dwordx4 a[4:7], v33, s[40:43], s81 offen offset:1024 \n" \
  "buffer_load_dwordx4 a[8:11], v32, s[48:51], s82 offen offset:1024 \n" \
  "v_mul_u32_u24 v227, 55296, v255\n" \
  "v_lshl_add_u32 v227, v254, 4, v227\n" \
  "v_add_u32 v227, s7, v227\n" \
  "s_mul_i32 s5, s78, 221184\n" \
  "s_lshl_b32 s6, s6, 2\n" \
  "s_add_u32 s6, s6, s80\n" \
  "s_lshl_b32 s6, s6, 8\n" \
  "s_add_u32 s5, s5, s6\n" \
  "s_add_u32 s5, s5, 44564480\n" \
  "ON5_%=:\n" \
  "v_lshrrev_b32 v32, 5, %2\n" \
  "v_lshlrev_b32 v250, 10, v32\n" \
  "v_lshl_add_u32 v250, v251, 4, v250\n" \
  "v_and_b32 v249, 2047, v250\n" \
  "s_mov_b32 s21, 131072\n" \
  "s_mov_b32 s22, 139264\n" \
  "s_mov_b32 s23, 147456\n" \
  "v_lshrrev_b32 v33, 1, %2\n" \
  "v_mul_u32_u24 v223, 4624, v33\n" \
  "v_and_b32 v33, 1, v11\n" \
  "v_lshl_add_u32 v223, v33, 4, v223\n" \
  "s_and_b32 s81, s4, 31\n" \
  "s_mul_i32 s81, s81, 9\n" \
  "s_mul_i32 s82, s78, 289\n" \
  "s_add_u32 s81, s81, s82\n" \
  "s_and_b32 s10, s81, 1\n" \
  "s_sub_u32 s81, s81, s10\n" \
  "s_lshl_b32 s81, s81, 1\n" \
  "v_add_u32 v223, s81, v223\n" \
  "v_add_u32 v222, 591872, v223\n" \
  "v_lshlrev_b32 v216, 8, v32\n" \
  "v_and_b32 v32, 7, v251\n" \
  "v_lshl_add_u32 v216, v32, 5, v216\n" \
  "v_lshrrev_b32 v32, 3, v251\n" \
  "v_lshl_add_u32 v216, v32, 2, v216\n" \
  "v_add_u32 v216, 127488, v216\n" \
  "v_add_u32 v217, 2048, v216\n" \
  "v_add_u32 v218, 4096, v216\n" \
  "v_add_u32 v219, 6144, v216\n" \
  "v_add_u32 v220, 8192, v216\n" \
  "v_mov_b32 v116, 0\n" \
  "v_mov_b32 v117, 0\n" \
  "v_mov_b32 v118, 0\n" \
  "v_mov_b32 v119, s84\n" \
  "v_add_u32 v213, 137728, v253\n" \
  "ds_write_b128 v213, v[116:119] offset:0\n" \
  "ds_write_b128 v213, v[116:119] offset:4096\n" \
  "ds_write_b128 v213, v[116:119] offset:8192\n" \
  "ds_write_b128 v213, v[116:119] offset:12288\n" \
  "v_lshrrev_b32 v32, 3, %2\n" \
  "v_lshl_add_u32 v212, v32, 26, v253\n" \
  "v_add_u32 v247, 122880, v253\n" \
  "v_cmp_gt_u32 vcc, 32, %2\n" \
  "v_add_u32 v32, 4096, v247\n" \
  "v_add_u32 v244, 4294966792, v247\n" \
  "v_cndmask_b32 v244, v244, v32, vcc\n" \
  "v_add_u32 v224, 126472, v24\n" \
  "v_lshlrev_b32 v221, 4, v251\n" \
  "v_add_u32 v221, 122880, v221\n" \
  "v_and_b32 v32, 3, v254\n" \
  "v_lshl_add_u32 v32, v255, 2, v32\n" \
  "v_lshrrev_b32 v33, 2, v254\n" \
  "s_and_b32 s6, s4, 7\n" \
  "v_mul_u32_u24 v35, 48, v32\n" \
  "s_movk_i32 s7, 1552\n" \
  "s_add_u32 s81, s6, 0\n" \
  "v_add_u32 v34, s81, v33\n" \
  "v_and_b32 v34, 7, v34\n" \
  "v_mad_u32_u24 v210, v34, s7, v35\n" \
  "v_sub_u32 v34, s81, v33\n" \
  "v_and_b32 v34, 7, v34\n" \
  "v_mul_u32_u24 v34, s7, v34\n" \
  "v_sub_u32 v208, v34, v35\n" \
  "s_add_u32 s81, s6, 4\n" \
  "v_add_u32 v34, s81, v33\n" \
  "v_and_b32 v34, 7, v34\n" \
  "v_mad_u32_u24 v211, v34, s7, v35\n" \
  "v_sub_u32 v34, s81, v33\n" \
  "v_and_b32 v34, 7, v34\n" \
  "v_mul_u32_u24 v34, s7, v34\n" \
  "v_sub_u32 v209, v34, v35\n" \
  "s_bitcmp1_b32 s80, 0\n" \
  "s_cselect_b64 vcc, -1, 0\n" \
  "v_cndmask_b32 v207, v210, v211, vcc\n" \
  "v_add_u32 v207, 137728, v207\n" \
  "v_add_u32 v206, 1536, v207\n" \
  "v_lshlrev_b32 v34, 2, v11\n" \
  "v_add_u32 v34, 163328, v34\n" \
  "v_cmp_eq_u32 vcc, 0, v32\n" \
  "s_lshr_b32 s81, s80, 1\n" \
  "s_add_u32 s81, s81, s80\n" \
  "s_lshl_b32 s81, s81, 2\n" \
  "s_add_u32 s81, s81, 137732\n" \
  "v_cndmask_b32 v206, v34, v206, vcc\n" \
  "s_cmp_eq_u32 s80, 3\n" \
  "s_addc_u32 s82, s80, 0\n" \
  "s_lshl_b32 s82, s82, 2\n" \
  "s_sub_u32 s82, 138488, s82\n" \
  "v_add_u32 v210, s81, v210\n" \
  "v_add_u32 v208, s82, v208\n" \
  "v_add_u32 v211, s81, v211\n" \
  "v_add_u32 v209, s82, v209\n" \
  "s_mov_b32 s12, 0x3f441b7d\n" \
  "s_mov_b32 s13, 0x3e31d0d4\n" \
  "s_mov_b32 s14, 0xbf708fb2\n" \
  "s_mov_b32 s15, 0x3f248dbb\n" \
  "s_mov_b32 s16, 0x3f7c1c5c\n" \
  "s_mov_b32 s17, 0x3f5db3d7\n" \
  "s_mov_b32 s18, 0x3eaf1d44\n" \
  "s_mov_b32 s19, 0x3f800000\n" \
  "s_mul_i32 s6, s4, 16512\n" \
  "s_add_u32 s6, s6, 46333952\n" \
  "s_add_u32 s7, s6, 4096\n" \
  "s_add_u32 s8, s6, 8192\n" \
  "s_add_u32 s9, s6, 12288\n" \
  "s_sub_u32 s85, s20, 36\n" \
  "s_cmp_ge_u32 s85, 72\n" \
  "s_cbranch_scc1 ON6_%=\n" \
  "s_and_b32 s81, s80, 1\n" \
  "s_lshl_b32 s81, s81, 5\n" \
  "s_and_b32 s82, s80, 2\n" \
  "s_lshl_b32 s82, s82, 8\n" \
  "s_or_b32 s81, s81, s82\n" \
  "v_xor_b32 v227, s81, v2\n" \
  "v_add_u32 v227, 81920, v227\n" \
  "v_lshlrev_b32 v226, 4, v255\n" \
  "v_add_u32 v226, 118784, v226\n" \
  "v_add_u32 v32, 118784, v248\n" \
  "s_waitcnt vmcnt(9)\n" \
  "v_lshlrev_b32 v16, 16, v245\n" \
  "v_and_b32 v17, 0xffff0000, v245\n" \
  "v_mul_f32 v18, 0xbfb8aa3b, v16\n" \
  "v_mul_f32 v19, 0xbfb8aa3b, v17\n" \
  "v_exp_f32 v18, v18\n" \
  "v_exp_f32 v19, v19\n" \
  "v_add_f32 v18, 1.0, v18\n" \
  "v_add_f32 v19, 1.0, v19\n" \
  "v_rcp_f32 v18, v18\n" \
  "v_rcp_f32 v19, v19\n" \
  "v_lshlrev_b32 v225, 5, v255\n" \
  "v_mul_f32 v16, v16, v18\n" \
  "v_mul_f32 v17, v17, v19\n" \
  "v_cvt_pk_bf16_f32 v16, v16, v17\n" \
  "ds_write_b32 v32, v16\n" \
  "v_lshl_add_u32 v225, v254, 4, v225\n" \
  "v_lshrrev_b32 v32, 1, v254\n" \
  "v_lshl_add_u32 v225, v32, 27, v225\n" \
  "s_lshl_b32 s5, s20, 2\n" \
  "s_add_u32 s5, s5, s80\n" \
  "s_lshl_b32 s5, s5, 7\n" \
  "s_add_u32 s5, s5, 131072\n" \
  "s_waitcnt lgkmcnt(0)\n" \
  "s_barrier\n" \
  "ds_read_b128 a[20:23], v226 offset:0\n" \
  "ds_read_b128 a[24:27], v226 offset:64\n" \
  "ds_read_b128 a[28:31], v226 offset:128\n" \
  "ds_read_b128 a[32:35], v226 offset:192\n" \
  "ds_read_b128 a[36:39], v226 offset:256\n" \
  "ds_read_b128 a[40:43], v226 offset:320\n" \
  "ds_read_b128 a[44:47], v226 offset:384\n" \
  "ds_read_b128 a[48:51], v226 offset:448\n" \
  "ds_read_b128 a[52:55], v226 offset:512\n" \
  "s_cmp_ge_u32 s20, 54\n" \
  "s_cselect_b32 s81, 72, 18\n" \
  "s_sub_u32 s81, s20, s81\n" \
  "s_cmp_lt_u32 s81, 18\n" \
  "s_cselect_b32 s81, 1.0, 0\n" \
  "s_bitcmp1_b32 s20, 0\n" \
  "s_cselect_b32 s81, 0, s81\n" \
  "v_mov_b32 v20, s81\n" \
  "v_mov_b32 v21, s81\n" \
  "v_mov_b32 v22, s81\n" \
  "v_mov_b32 v23, s81\n" \
  "ON6_%=:\n" \
  "s_cmp_ge_u32 s20, 36\n" \
  "s_cbranch_scc1 ON7_%=\n" \
  "s_waitcnt vmcnt(1)\n" \
  "ON7_%=:\n" \
  "s_cmp_lt_u32 s20, 108\n" \
  "s_cbranch_scc1 ON8_%=\n" \
  "s_waitcnt vmcnt(5)\n" \
  "ON8_%=:\n" \
  "s_getpc_b64 s[100:101]\n" \
  "s_branch HOPIN_%=\n" \
  "HOPAT1_%=:\n" \
  "UT0_%=:\n" \
  "s_memrealtime s[92:93]\n" \
  "s_waitcnt lgkmcnt(0)\n" \
  "s_sub_u32 s92, s92, s94\n" \
  "s_cmp_lt_u32 s92, 200\n" \
  "s_cbranch_scc1 UT0_%=\n" \
  "buffer_load_dwordx3 v[116:118], v250, s[60:63], s21 offen offset:0 sc1\n" \
  "buffer_load_dwordx3 v[120:122], v250, s[60:63], s21 offen offset:512 sc1\n" \
  "buffer_load_dwordx3 v[124:126], v250, s[60:63], s22 offen offset:0 sc1\n" \
  "buffer_load_dwordx3 v[128:130], v250, s[60:63], s22 offen offset:512 sc1\n" \
  "buffer_load_dwordx3 v[132:134], v249, s[60:63], s23 offen offset:0 sc1\n" \
  "buffer_load_dwordx3 v[136:138], v249, s[60:63], s23 offen offset:512 sc1\n" \
  "v_lshlrev_b32 v188, 16, v232\n" \
  "v_and_b32 v189, 0xffff0000, v232\n" \
  "v_lshlrev_b32 v190, 16, v233\n" \
  "v_and_b32 v191, 0xffff0000, v233\n" \
  "v_lshlrev_b32 v192, 16, v234\n" \
  "v_and_b32 v193, 0xffff0000, v234\n" \
  "v_lshlrev_b32 v194, 16, v235\n" \
  "v_and_b32 v195, 0xffff0000, v235\n" \
  "v_lshlrev_b32 v196, 16, v236\n" \
  "v_and_b32 v197, 0xffff0000, v236\n" \
  "v_lshlrev_b32 v198, 16, v237\n" \
  "v_and_b32 v199, 0xffff0000, v237\n" \
  "v_lshlrev_b32 v200, 16, v238\n" \
  "v_and_b32 v201, 0xffff0000, v238\n" \
  "v_lshlrev_b32 v202, 16, v239\n" \
  "v_and_b32 v203, 0xffff0000, v239\n" \
  "v_lshlrev_b32 v204, 16, v240\n" \
  "v_and_b32 v205, 0xffff0000, v240\n" \
  "v_readlane_b32 s81, v188, 0\n" \
  "v_readlane_b32 s82, v188, 32\n" \
  "s_mov_b32 vcc_lo, 0\n" \
  "s_mov_b32 vcc_hi, -1\n" \
  "v_mov_b32 v140, s81\n" \
  "v_mov_b32 v141, s82\n" \
  "v_cndmask_b32 v140, v140, v141, vcc\n" \
  "v_pk_add_f32 v[188:189], v[188:189], v[140:141] op_sel_hi:[1,0] neg_lo:[0,1] neg_hi:[0,1]\n" \
  "v_pk_add_f32 v[190:191], v[190:191], v[140:141] op_sel_hi:[1,0] neg_lo:[0,1] neg_hi:[0,1]\n" \
  "v_pk_add_f32 v[192:193], v[192:193], v[140:141] op_sel_hi:[1,0] neg_lo:[0,1] neg_hi:[0,1]\n" \
  "v_pk_add_f32 v[194:195], v[194:195], v[140:141] op_sel_hi:[1,0] neg_lo:[0,1] neg_hi:[0,1]\n" \
  "v_pk_add_f32 v[196:197], v[196:197], v[140:141] op_sel_hi:[1,0] neg_lo:[0,1] neg_hi:[0,1]\n" \
  "v_pk_add_f32 v[198:199], v[198:199], v[140:141] op_sel_hi:[1,0] neg_lo:[0,1] neg_hi:[0,1]\n" \
  "v_pk_add_f32 v[200:201], v[200:201], v[140:141] op_sel_hi:[1,0] neg_lo:[0,1] neg_hi:[0,1]\n" \
  "v_pk_add_f32 v[202:203], v[202:203], v[140:141] op_sel_hi:[1,0] neg_lo:[0,1] neg_hi:[0,1]\n" \
  "v_pk_add_f32 v[204:205], v[204:205], v[140:141] op_sel_hi:[1,0] neg_lo:[0,1] neg_hi:[0,1]\n" \
  "v_pk_add_f32 v[144:145], v[188:189], v[190:191]\n" \
  "v_pk_mul_f32 v[146:147], v[188:189], v[188:189]\n" \
  "v_pk_fma_f32 v[146:147], v[190:191], v[190:191], v[146:147]\n" \
  "v_pk_add_f32 v[144:145], v[144:145], v[192:193]\n" \
  "v_pk_fma_f32 v[146:147], v[192:193], v[192:193], v[146:147]\n" \
  "v_pk_add_f32 v[144:145], v[144:145], v[194:195]\n" \
  "v_pk_fma_f32 v[146:147], v[194:195], v[194:195], v[146:147]\n" \
  "v_pk_add_f32 v[144:145], v[144:145], v[196:197]\n" \
  "v_pk_fma_f32 v[146:147], v[196:197], v[196:197], v[146:147]\n" \
  "v_pk_add_f32 v[144:145], v[144:145], v[198:199]\n" \
  "v_pk_fma_f32 v[146:147], v[198:199], v[198:199], v[146:147]\n" \
  "v_pk_add_f32 v[144:145], v[144:145], v[200:201]\n" \
  "v_pk_fma_f32 v[146:147], v[200:201], v[200:201], v[146:147]\n" \
  "v_pk_add_f32 v[144:145], v[144:145], v[202:203]\n" \
  "v_pk_fma_f32 v[146:147], v[202:203], v[202:203], v[146:147]\n" \
  "v_pk_add_f32 v[144:145], v[144:145], v[204:205]\n" \
  "v_pk_fma_f32 v[146:147], v[204:205], v[204:205], v[146:147]\n" \
  "v_add_f32 v140, v144, v145\n" \
  "v_add_f32 v141, v146, v147\n" \
  "s_mov_b32 s82, 0x3ae38e39\n" \
  "v_add_f32_dpp v140, v140, v140 quad_perm:[1,0,3,2] row_mask:0xf bank_mask:0xf bound_ctrl:1\n" \
  "v_add_f32_dpp v141, v141, v141 quad_perm:[1,0,3,2] row_mask:0xf bank_mask:0xf bound_ctrl:1\n" \
  "s_nop 0\n" \
  "v_add_f32_dpp v140, v140, v140 quad_perm:[2,3,0,1] row_mask:0xf bank_mask:0xf bound_ctrl:1\n" \
  "v_add_f32_dpp v141, v141, v141 quad_perm:[2,3,0,1] row_mask:0xf bank_mask:0xf bound_ctrl:1\n" \
  "s_nop 0\n" \
  "v_add_f32_dpp v140, v140, v140 row_half_mirror row_mask:0xf bank_mask:0xf bound_ctrl:1\n" \
  "v_add_f32_dpp v141, v141, v141 row_half_mirror row_mask:0xf bank_mask:0xf bound_ctrl:1\n" \
  "s_nop 0\n" \
  "v_add_f32_dpp v140, v140, v140 row_mirror row_mask:0xf bank_mask:0xf bound_ctrl:1\n" \
  "v_add_f32_dpp v141, v141, v141 row_mirror row_mask:0xf bank_mask:0xf bound_ctrl:1\n" \
  "v_pk_mov_b32 v[142:143], v[140:141], v[140:141] op_sel:[0,1]\n" \
  "s_nop 1\n" \
  "v_permlane16_swap_b32 v140, v142\n" \
  "v_permlane16_swap_b32 v141, v143\n" \
  "v_pk_add_f32 v[140:141], v[140:141], v[142:143]\n" \
  "v_pk_mul_f32 v[140:141], v[140:141], s[82:83] op_sel_hi:[1,0]\n" \
  "v_fma_f32 v141, -v140, v140, v141\n" \
  "v_max_f32 v141, 0, v141\n" \
  "v_add_f32 v141, 0x3727c5ac, v141\n" \
  "v_rsq_f32 v141, v141\n" \
  "v_add_u32 v214, 127488, v24\n" \
  "s_lshl_b32 s81, s72, 1\n" \
  "v_add_u32 v215, s81, v214\n" \
  "v_pk_add_f32 v[188:189], v[188:189], v[140:141] op_sel_hi:[1,0] neg_lo:[0,1] neg_hi:[0,1]\n" \
  "v_pk_add_f32 v[190:191], v[190:191], v[140:141] op_sel_hi:[1,0] neg_lo:[0,1] neg_hi:[0,1]\n" \
  "v_pk_add_f32 v[192:193], v[192:193], v[140:141] op_sel_hi:[1,0] neg_lo:[0,1] neg_hi:[0,1]\n" \
  "v_pk_add_f32 v[194:195], v[194:195], v[140:141] op_sel_hi:[1,0] neg_lo:[0,1] neg_hi:[0,1]\n" \
  "v_pk_add_f32 v[196:197], v[196:197], v[140:141] op_sel_hi:[1,0] neg_lo:[0,1] neg_hi:[0,1]\n" \
  "v_pk_add_f32 v[198:199], v[198:199], v[140:141] op_sel_hi:[1,0] neg_lo:[0,1] neg_hi:[0,1]\n" \
  "v_pk_add_f32 v[200:201], v[200:201], v[140:141] op_sel_hi:[1,0] neg_lo:[0,1] neg_hi:[0,1]\n" \
  "v_pk_add_f32 v[202:203], v[202:203], v[140:141] op_sel_hi:[1,0] neg_lo:[0,1] neg_hi:[0,1]\n" \
  "v_pk_add_f32 v[204:205], v[204:205], v[140:141] op_sel_hi:[1,0] neg_lo:[0,1] neg_hi:[0,1]\n" \
  "v_pk_mul_f32 v[188:189], v[188:189], v[140:141] op_sel:[0,1] op_sel_hi:[1,1]\n" \
  "v_pk_mul_f32 v[190:191], v[190:191], v[140:141] op_sel:[0,1] op_sel_hi:[1,1]\n" \
  "v_pk_mul_f32 v[192:193], v[192:193], v[140:141] op_sel:[0,1] op_sel_hi:[1,1]\n" \
  "v_pk_mul_f32 v[194:195], v[194:195], v[140:141] op_sel:[0,1] op_sel_hi:[1,1]\n" \
  "v_pk_mul_f32 v[196:197], v[196:197], v[140:141] op_sel:[0,1] op_sel_hi:[1,1]\n" \
  "v_pk_mul_f32 v[198:199], v[198:199], v[140:141] op_sel:[0,1] op_sel_hi:[1,1]\n" \
  "v_pk_mul_f32 v[200:201], v[200:201], v[140:141] op_sel:[0,1] op_sel_hi:[1,1]\n" \
  "v_pk_mul_f32 v[202:203], v[202:203], v[140:141] op_sel:[0,1] op_sel_hi:[1,1]\n" \
  "v_pk_mul_f32 v[204:205], v[204:205], v[140:141] op_sel:[0,1] op_sel_hi:[1,1]\n" \
  "s_sub_u32 s85, s20, 36\n" \
  "s_cmp_ge_u32 s85, 72\n" \
  "s_cbranch_scc1 ON9_%=\n" \
  "v_and_b32 v32, 1, v254\n" \
  "v_cmp_eq_u32 vcc, 1, v32\n" \
  "v_mov_b32 v42, s84\n" \
  "v_mov_b32 v43, s84\n" \
  "s_waitcnt vmcnt(6)\n" \
  "s_waitcnt lgkmcnt(0)\n" \
  "s_barrier\n" \
  "ds_read_b64_tr_b16 a[0:1], v227 offset:0\n" \
  "ds_read_b64_tr_b16 a[2:3], v227 offset:2048\n" \
  "ds_read_b64_tr_b16 a[4:5], v227 offset:4096\n" \
  "ds_read_b64_tr_b16 a[6:7], v227 offset:6144\n" \
  "ds_read_b64_tr_b16 a[8:9], v227 offset:8192\n" \
  "ds_read_b64_tr_b16 a[10:11], v227 offset:10240\n" \
  "ds_read_b64_tr_b16 a[12:13], v227 offset:12288\n" \
  "ds_read_b64_tr_b16 a[14:15], v227 offset:14336\n" \
  "ds_read_b64_tr_b16 a[16:17], v227 offset:16384\n" \
  "ds_read_b64_tr_b16 a[18:19], v227 offset:18432\n" \
  "s_waitcnt lgkmcnt(8)\n" \
  "v_mfma_f32_16x16x32_bf16 v[20:23], a[0:3], a[20:23], v[20:23]\n" \
  "ds_read_b64_tr_b16 a[0:1], v227 offset:20480\n" \
  "ds_read_b64_tr_b16 a[2:3], v227 offset:22528\n" \
  "s_waitcnt lgkmcnt(8)\n" \
  "v_mfma_f32_16x16x32_bf16 v[20:23], a[4:7], a[24:27], v[20:23]\n" \
  "ds_read_b64_tr_b16 a[4:5], v227 offset:24576\n" \
  "ds_read_b64_tr_b16 a[6:7], v227 offset:26624\n" \
  "s_waitcnt lgkmcnt(8)\n" \
  "v_mfma_f32_16x16x32_bf16 v[20:23], a[8:11], a[28:31], v[20:23]\n" \
  "ds_read_b64_tr_b16 a[8:9], v227 offset:28672\n" \
  "ds_read_b64_tr_b16 a[10:11], v227 offset:30720\n" \
  "s_waitcnt lgkmcnt(8)\n" \
  "v_mfma_f32_16x16x32_bf16 v[20:23], a[12:15], a[32:35], v[20:23]\n" \
  "ds_read_b64_tr_b16 a[12:13], v227 offset:32768\n" \
  "ds_read_b64_tr_b16 a[14:15], v227 offset:34816\n" \
  "s_waitcnt lgkmcnt(8)\n" \
  "v_mfma_f32_16x16x32_bf16 v[20:23], a[16:19], a[36:39], v[20:23]\n" \
  "s_waitcnt lgkmcnt(6)\n" \
  "v_mfma_f32_16x16x32_bf16 v[20:23], a[0:3], a[40:43], v[20:23]\n" \
  "s_waitcnt lgkmcnt(4)\n" \
  "v_mfma_f32_16x16x32_bf16 v[20:23], a[4:7], a[44:47], v[20:23]\n" \
  "s_waitcnt lgkmcnt(2)\n" \
  "v_mfma_f32_16x16x32_bf16 v[20:23], a[8:11], a[48:51], v[20:23]\n" \
  "s_waitcnt lgkmcnt(0)\n" \
  "v_mfma_f32_16x16x32_bf16 v[20:23], a[12:15], a[52:55], v[20:23]\n" \
  "s_nop 7\n" \
  "v_cndmask_b32 v40, v20, v22, vcc\n" \
  "v_cndmask_b32 v41, v21, v23, vcc\n" \
  "buffer_store_dwordx4 v[40:43], v225, s[60:63], s5 offen sc1\n" \
  "s_nop 1\n" \
  "ON9_%=:\n" \
  "s_cmp_lt_u32 s20, 108\n" \
  "s_cbranch_scc1 ON10_%=\n" \
  "s_waitcnt vmcnt(6)\n" \
  "buffer_store_dwordx4 a[0:3], v227, s[60:63], s5 offen sc1\n" \
  "s_add_u32 s5, s5, 18432\n" \
  "buffer_store_dwordx4 a[4:7], v227, s[60:63], s5 offen sc1\n" \
  "s_add_u32 s5, s5, 18432\n" \
  "buffer_store_dwordx4 a[8:11], v227, s[60:63], s5 offen sc1\n" \
  "s_add_u32 s5, s5, 18432\n" \
  "ON10_%=:\n" \
  "s_cmp_ge_u32 s20, 36\n" \
  "s_cbranch_scc1 ON11_%=\n" \
  "s_waitcnt vmcnt(0)\n" \
  "ON11_%=:\n" \
  "s_sub_u32 s85, s20, 36\n" \
  "s_cmp_ge_u32 s85, 72\n" \
  "s_cbranch_scc1 ON12_%=\n" \
  "s_waitcnt vmcnt(1)\n" \
  "ON12_%=:\n" \
  "s_cmp_lt_u32 s20, 108\n" \
  "s_cbranch_scc1 ON13_%=\n" \
  "s_waitcnt vmcnt(3)\n" \
  "ON13_%=:\n" \
  "v_xor_b32 v27, s84, v118\n" \
  "v_bitop3_b32 v27, v27, v122, s84 bitop3:0xf6\n" \
  "v_bitop3_b32 v27, v27, v126, s84 bitop3:0xf6\n" \
  "v_bitop3_b32 v27, v27, v130, s84 bitop3:0xf6\n" \
  "v_bitop3_b32 v27, v27, v134, s84 bitop3:0xf6\n" \
  "v_bitop3_b32 v27, v27, v138, s84 bitop3:0xf6\n" \
  "v_cmp_ne_u32 vcc, 0, v27\n" \
  "s_cbranch_vccnz SLOW_M0_%=\n" \
  "BACK_M0_%=:\n" \
  "v_pk_add_f32 v[116:117], v[116:117], v[120:121]\n" \
  "ds_write_b64 v247, v[116:117]\n" \
  "v_pk_add_f32 v[124:125], v[124:125], v[128:129]\n" \
  "ds_write_b64 v244, v[124:125]\n" \
  "v_pk_add_f32 v[132:133], v[132:133], v[136:137]\n" \
  "ds_write_b64 v224, v[132:133]\n" \
  "s_waitcnt lgkmcnt(0)\n" \
  "s_barrier\n" \
  "ds_read_b128 v[20:23], v221 offset:512\n" \
  "ds_read_b128 v[136:139], v221 offset:4096\n" \
  "ds_read_b128 v[40:43], v221 offset:1024\n" \
  "ds_read_b128 v[132:135], v221 offset:3584\n" \
  "ds_read_b128 v[116:119], v221 offset:1536\n" \
  "ds_read_b128 v[128:131], v221 offset:3072\n" \
  "ds_read_b128 v[120:123], v221 offset:2048\n" \
  "ds_read_b128 v[124:127], v221 offset:2560\n" \
  "ds_read_b128 v[16:19], v221 offset:0\n" \
  "s_add_u32 m0, s72, 61440\n" \
  "s_nop 0\n" \
  "buffer_load_dwordx4 v223, s[28:31], 0 offen lds\n" \
  "s_add_u32 m0, s72, 65536\n" \
  "s_nop 0\n" \
  "buffer_load_dwordx4 v222, s[28:31], 0 offen lds\n" \
  "s_waitcnt lgkmcnt(8)\n" \
  "v_pk_fma_f32 v[190:191], v[190:191], v[22:23], v[20:21]\n" \
  "s_waitcnt lgkmcnt(7)\n" \
  "v_pk_fma_f32 v[204:205], v[204:205], v[138:139], v[136:137]\n" \
  "v_pk_add_f32 v[140:141], v[190:191], v[204:205]\n" \
  "v_pk_add_f32 v[190:191], v[190:191], v[204:205] neg_lo:[0,1] neg_hi:[0,1]\n" \
  "s_waitcnt lgkmcnt(6)\n" \
  "v_pk_fma_f32 v[192:193], v[192:193], v[42:43], v[40:41]\n" \
  "s_waitcnt lgkmcnt(5)\n" \
  "v_pk_fma_f32 v[202:203], v[202:203], v[134:135], v[132:133]\n" \
  "v_pk_add_f32 v[142:143], v[192:193], v[202:203]\n" \
  "v_pk_add_f32 v[192:193], v[192:193], v[202:203] neg_lo:[0,1] neg_hi:[0,1]\n" \
  "s_waitcnt lgkmcnt(4)\n" \
  "v_pk_fma_f32 v[194:195], v[194:195], v[118:119], v[116:117]\n" \
  "s_waitcnt lgkmcnt(3)\n" \
  "v_pk_fma_f32 v[200:201], v[200:201], v[130:131], v[128:129]\n" \
  "v_pk_add_f32 v[144:145], v[194:195], v[200:201]\n" \
  "v_pk_add_f32 v[194:195], v[194:195], v[200:201] neg_lo:[0,1] neg_hi:[0,1]\n" \
  "s_waitcnt lgkmcnt(2)\n" \
  "v_pk_fma_f32 v[196:197], v[196:197], v[122:123], v[120:121]\n" \
  "s_waitcnt lgkmcnt(1)\n" \
  "v_pk_fma_f32 v[198:199], v[198:199], v[126:127], v[124:125]\n" \
  "v_pk_add_f32 v[146:147], v[196:197], v[198:199]\n" \
  "v_pk_add_f32 v[196:197], v[196:197], v[198:199] neg_lo:[0,1] neg_hi:[0,1]\n" \
  "s_waitcnt lgkmcnt(0)\n" \
  "v_pk_fma_f32 v[188:189], v[188:189], v[18:19], v[16:17]\n" \
  "v_pk_add_f32 v[20:21], v[140:141], v[142:143]\n" \
  "v_pk_add_f32 v[22:23], v[188:189], v[144:145]\n" \
  "v_pk_add_f32 v[118:119], v[192:193], v[190:191] neg_lo:[0,1] neg_hi:[0,1]\n" \
  "v_pk_add_f32 v[20:21], v[20:21], v[146:147]\n" \
  "v_pk_add_f32 v[118:119], v[118:119], v[196:197] neg_lo:[0,1] neg_hi:[0,1]\n" \
  "v_pk_fma_f32 v[40:41], v[144:145], -0.5, v[188:189] op_sel_hi:[1,0,1]\n" \
  "v_pk_mul_f32 v[122:123], v[194:195], s[16:17] op_sel:[0,1] op_sel_hi:[1,1]\n" \
  "v_pk_add_f32 v[124:125], v[22:23], v[20:21]\n" \
  "v_pk_fma_f32 v[202:203], v[20:21], -0.5, v[22:23] op_sel_hi:[1,0,1]\n" \
  "v_pk_mul_f32 v[118:119], v[118:119], s[16:17] op_sel:[0,1] op_sel_hi:[1,1]\n" \
  "v_cvt_pk_f16_f32 v124, v124, 0\n" \
  "v_cvt_pk_f16_f32 v125, v125, 0\n" \
  "ds_write2_b32 v216, v124, v125 offset0:0 offset1:4\n" \
  "v_cvt_pk_f16_f32 v202, v202, v118\n" \
  "v_cvt_pk_f16_f32 v203, v203, v119\n" \
  "ds_write2_b32 v219, v202, v203 offset0:0 offset1:4\n" \
  "s_add_u32 m0, s72, 69632\n" \
  "s_nop 0\n" \
  "buffer_load_dwordx4 v223, s[96:99], 0 offen lds\n" \
  "v_pk_fma_f32 v[198:199], v[140:141], s[12:13], v[40:41] op_sel_hi:[1,0,1]\n" \
  "v_pk_fma_f32 v[42:43], v[190:191], s[14:15], v[122:123] op_sel:[0,1,0] op_sel_hi:[1,1,1] neg_lo:[1,0,1] neg_hi:[1,0,1]\n" \
  "v_pk_fma_f32 v[198:199], v[142:143], s[12:13], v[198:199] op_sel:[0,1,0] op_sel_hi:[1,1,1]\n" \
  "v_pk_fma_f32 v[42:43], v[192:193], s[16:17], v[42:43] op_sel_hi:[1,0,1] neg_lo:[1,0,0] neg_hi:[1,0,0]\n" \
  "v_pk_fma_f32 v[198:199], v[146:147], s[14:15], v[198:199] op_sel_hi:[1,0,1]\n" \
  "v_pk_fma_f32 v[42:43], v[196:197], s[18:19], v[42:43] op_sel_hi:[1,0,1] neg_lo:[1,0,0] neg_hi:[1,0,0]\n" \
  "v_cvt_pk_f16_f32 v198, v198, v42\n" \
  "v_cvt_pk_f16_f32 v199, v199, v43\n" \
  "ds_write2_b32 v217, v198, v199 offset0:0 offset1:4\n" \
  "v_pk_fma_f32 v[200:201], v[140:141], s[12:13], v[40:41] op_sel:[0,1,0] op_sel_hi:[1,1,1]\n" \
  "v_pk_fma_f32 v[116:117], v[190:191], s[16:17], v[122:123] op_sel_hi:[1,0,1] neg_lo:[1,0,0] neg_hi:[1,0,0]\n" \
  "v_pk_fma_f32 v[200:201], v[142:143], s[14:15], v[200:201] op_sel_hi:[1,0,1]\n" \
  "v_pk_fma_f32 v[116:117], v[192:193], s[18:19], v[116:117] op_sel_hi:[1,0,1] neg_lo:[1,0,0] neg_hi:[1,0,0]\n" \
  "v_pk_fma_f32 v[200:201], v[146:147], s[12:13], v[200:201] op_sel_hi:[1,0,1]\n" \
  "v_pk_fma_f32 v[116:117], v[196:197], s[14:15], v[116:117] op_sel:[0,1,0] op_sel_hi:[1,1,1]\n" \
  "v_cvt_pk_f16_f32 v200, v200, v116\n" \
  "v_cvt_pk_f16_f32 v201, v201, v117\n" \
  "ds_write2_b32 v218, v200, v201 offset0:0 offset1:4\n" \
  "v_pk_fma_f32 v[204:205], v[140:141], s[14:15], v[40:41] op_sel_hi:[1,0,1]\n" \
  "v_pk_fma_f32 v[120:121], v[190:191], s[18:19], v[122:123] op_sel_hi:[1,0,1] neg_lo:[1,0,1] neg_hi:[1,0,1]\n" \
  "v_pk_fma_f32 v[204:205], v[142:143], s[12:13], v[204:205] op_sel_hi:[1,0,1]\n" \
  "v_pk_fma_f32 v[120:121], v[192:193], s[14:15], v[120:121] op_sel:[0,1,0] op_sel_hi:[1,1,1]\n" \
  "v_pk_fma_f32 v[204:205], v[146:147], s[12:13], v[204:205] op_sel:[0,1,0] op_sel_hi:[1,1,1]\n" \
  "v_pk_fma_f32 v[120:121], v[196:197], s[16:17], v[120:121] op_sel_hi:[1,0,1]\n" \
  "v_cvt_pk_f16_f32 v204, v204, v120\n" \
  "v_cvt_pk_f16_f32 v205, v205, v121\n" \
  "ds_write2_b32 v220, v204, v205 offset0:0 offset1:4\n" \
  "s_waitcnt lgkmcnt(0)\n" \
  "s_barrier\n" \
  "ds_read_b128 v[116:119], v215 offset:2048\n" \
  "ds_read_b128 v[120:123], v215 offset:3072\n" \
  "ds_read_b128 v[124:127], v214 offset:0\n" \
  "ds_read_b128 v[128:131], v214 offset:1024\n" \
  "s_add_u32 m0, s72, 73728\n" \
  "s_nop 0\n" \
  "buffer_load_dwordx4 v222, s[96:99], 0 offen lds\n" \
  "s_waitcnt lgkmcnt(3)\n" \
  "v_mfma_f32_16x16x32_f16 v[136:139], v[116:119], v[56:59], 0\n" \
  "v_mfma_f32_16x16x32_f16 v[132:135], v[116:119], v[52:55], 0\n" \
  "s_waitcnt lgkmcnt(2)\n" \
  "v_mfma_f32_16x16x32_f16 v[136:139], v[120:123], v[48:51], v[136:139]\n" \
  "v_mfma_f32_16x16x32_f16 v[132:135], v[120:123], v[44:47], v[132:135]\n" \
  "v_mfma_f32_16x16x32_f16 v[144:147], v[116:119], v[64:67], 0\n" \
  "v_mfma_f32_16x16x32_f16 v[140:143], v[116:119], v[60:63], 0\n" \
  "v_mfma_f32_16x16x32_f16 v[144:147], v[120:123], v[72:75], v[144:147]\n" \
  "v_mfma_f32_16x16x32_f16 v[140:143], v[120:123], v[68:71], v[140:143]\n" \
  "s_waitcnt lgkmcnt(0)\n" \
  "v_mfma_f32_16x16x32_f16 v[152:155], v[124:127], v[88:91], 0\n" \
  "v_mfma_f32_16x16x32_f16 v[148:151], v[124:127], v[84:87], 0\n" \
  "v_pk_mul_f32 v[16:17], v[136:137], v[92:93]\n" \
  "v_pk_mul_f32 v[20:21], v[136:137], v[96:97]\n" \
  "v_pk_mul_f32 v[18:19], v[138:139], v[94:95]\n" \
  "v_pk_mul_f32 v[22:23], v[138:139], v[98:99]\n" \
  "v_pk_fma_f32 v[16:17], v[132:133], v[96:97], v[16:17] neg_lo:[0,0,1] neg_hi:[0,0,1]\n" \
  "v_pk_fma_f32 v[20:21], v[132:133], v[92:93], v[20:21]\n" \
  "v_pk_fma_f32 v[18:19], v[134:135], v[98:99], v[18:19] neg_lo:[0,0,1] neg_hi:[0,0,1]\n" \
  "v_pk_fma_f32 v[22:23], v[134:135], v[94:95], v[22:23]\n" \
  "v_mfma_f32_16x16x32_f16 v[152:155], v[128:131], v[80:83], v[152:155]\n" \
  "v_cvt_pk_f16_f32 v40, v16, v20\n" \
  "v_cvt_pk_f16_f32 v41, v17, v21\n" \
  "v_mfma_f32_16x16x32_f16 v[148:151], v[128:131], v[76:79], v[148:151]\n" \
  "v_cvt_pk_f16_f32 v42, v18, v22\n" \
  "v_cvt_pk_f16_f32 v43, v19, v23\n" \
  "v_not_b32 v156, v28\n" \
  "v_not_b32 v157, v28\n" \
  "v_not_b32 v158, v28\n" \
  "v_not_b32 v159, v28\n" \
  "s_lshr_b32 s81, s20, 3\n" \
  "s_lshl_b32 s81, s81, 2\n" \
  "s_add_u32 s81, s81, s80\n" \
  "s_lshl_b32 s81, s81, 3\n" \
  "s_add_u32 s81, s81, s78\n" \
  "s_lshl_b32 s81, s81, 10\n" \
  "s_add_u32 s81, s81, 1048576\n" \
  "buffer_store_dwordx4 v[156:159], v24, s[60:63], s81 offen sc1\n" \
  "s_lshl_b32 s81, s79, 2\n" \
  "s_add_u32 s81, s81, s80\n" \
  "s_lshl_b32 s81, s81, 3\n" \
  "s_add_u32 s81, s81, s78\n" \
  "s_lshl_b32 s81, s81, 4\n" \
  "s_add_u32 s81, s81, s77\n" \
  "s_lshl_b32 s81, s81, 7\n" \
  "s_add_u32 s81, s81, 524288\n" \
  "buffer_store_dword v156, v1, s[60:63], s81 offen offset:0 sc1\n" \
  "buffer_store_dword v156, v1, s[60:63], s81 offen offset:512 sc1\n" \
  "buffer_store_dword v156, v1, s[60:63], s81 offen offset:1024 sc1\n" \
  "s_lshl_b32 s81, s4, 7\n" \
  "s_add_u32 s81, s81, 262144\n" \
  "s_cmp_eq_u32 s80, 0\n" \
  "s_cselect_b32 s82, 0, 0x20000000\n" \
  "v_add_u32 v32, s82, v1\n" \
  "buffer_store_dword v156, v32, s[60:63], s81 offen sc1\n" \
  "v_pk_mul_f32 v[16:17], v[144:145], v[92:93]\n" \
  "v_pk_mul_f32 v[20:21], v[144:145], v[96:97]\n" \
  "v_pk_mul_f32 v[18:19], v[146:147], v[94:95]\n" \
  "v_pk_mul_f32 v[22:23], v[146:147], v[98:99]\n" \
  "v_pk_fma_f32 v[16:17], v[140:141], v[96:97], v[16:17] neg_lo:[0,0,1] neg_hi:[0,0,1]\n" \
  "v_pk_fma_f32 v[20:21], v[140:141], v[92:93], v[20:21]\n" \
  "v_pk_fma_f32 v[18:19], v[142:143], v[98:99], v[18:19] neg_lo:[0,0,1] neg_hi:[0,0,1]\n" \
  "v_pk_fma_f32 v[22:23], v[142:143], v[94:95], v[22:23]\n" \
  "v_mfma_f32_16x16x32_f16 v[132:135], v[100:103], v[40:43], 0\n" \
  "v_cvt_pk_f16_f32 v116, v16, v20\n" \
  "v_cvt_pk_f16_f32 v117, v17, v21\n" \
  "v_mfma_f32_16x16x32_f16 v[136:139], v[104:107], v[40:43], 0\n" \
  "v_cvt_pk_f16_f32 v118, v18, v22\n" \
  "v_cvt_pk_f16_f32 v119, v19, v23\n" \
  "v_pk_mul_f32 v[16:17], v[152:153], v[108:109]\n" \
  "v_pk_mul_f32 v[20:21], v[152:153], v[112:113]\n" \
  "v_pk_mul_f32 v[18:19], v[154:155], v[110:111]\n" \
  "v_pk_mul_f32 v[22:23], v[154:155], v[114:115]\n" \
  "v_pk_fma_f32 v[16:17], v[148:149], v[112:113], v[16:17] neg_lo:[0,0,1] neg_hi:[0,0,1]\n" \
  "v_pk_fma_f32 v[20:21], v[148:149], v[108:109], v[20:21]\n" \
  "v_pk_fma_f32 v[18:19], v[150:151], v[114:115], v[18:19] neg_lo:[0,0,1] neg_hi:[0,0,1]\n" \
  "v_pk_fma_f32 v[22:23], v[150:151], v[110:111], v[22:23]\n" \
  "v_mfma_f32_16x16x32_f16 v[140:143], v[100:103], v[116:119], 0\n" \
  "v_cvt_pk_f16_f32 v120, v16, v20\n" \
  "v_cvt_pk_f16_f32 v121, v17, v21\n" \
  "v_mfma_f32_16x16x32_f16 v[144:147], v[104:107], v[116:119], 0\n" \
  "v_cvt_pk_f16_f32 v122, v18, v22\n" \
  "v_cvt_pk_f16_f32 v123, v19, v23\n" \
  "v_cvt_pk_f16_f32 v40, v132, v136\n" \
  "v_cvt_pk_f16_f32 v41, v133, v137\n" \
  "v_mfma_f32_16x16x32_f16 v[148:151], v[100:103], v[120:123], 0\n" \
  "ds_write2_b32 v210, v40, v41 offset0:0 offset1:192\n" \
  "v_cvt_pk_f16_f32 v42, v134, v138\n" \
  "v_mfma_f32_16x16x32_f16 v[152:155], v[104:107], v[120:123], 0\n" \
  "v_cvt_pk_f16_f32 v43, v135, v139\n" \
  "ds_write2_b32 v208, v43, v42 offset0:0 offset1:192\n" \
  "v_cvt_pk_f16_f32 v116, v140, v144\n" \
  "v_cvt_pk_f16_f32 v117, v141, v145\n" \
  "ds_write2_b32 v211, v116, v117 offset0:0 offset1:192\n" \
  "v_cvt_pk_f16_f32 v118, v142, v146\n" \
  "v_cvt_pk_f16_f32 v119, v143, v147\n" \
  "ds_write2_b32 v209, v119, v118 offset0:0 offset1:192\n" \
  "v_cvt_pk_f16_f32 v120, v148, v152\n" \
  "v_cvt_pk_f16_f32 v121, v149, v153\n" \
  "ds_write2_b32 v207, v120, v121 offset0:0 offset1:192\n" \
  "v_cvt_pk_f16_f32 v122, v150, v154\n" \
  "ds_write_b32 v206, v122\n" \
  "s_waitcnt vmcnt(5)\n" \
  "s_waitcnt lgkmcnt(0)\n" \
  "s_barrier\n" \
  "ds_read_b128 v[40:43], v213 offset:0\n" \
  "ds_read_b128 v[44:47], v213 offset:4096\n" \
  "ds_read_b128 v[48:51], v213 offset:8192\n" \
  "s_cmp_lg_u32 s80, 0\n" \
  "s_cbranch_scc1 ON14_%=\n" \
  "ds_read_b128 v[52:55], v213 offset:12288\n" \
  "s_waitcnt lgkmcnt(0)\n" \
  "buffer_store_dwordx4 v[52:55], v212, s[60:63], s9 offen sc1\n" \
  "ON14_%=:\n" \
  "s_waitcnt lgkmcnt(2)\n" \
  "buffer_store_dwordx4 v[40:43], v253, s[60:63], s6 offen sc1\n" \
  "s_waitcnt lgkmcnt(1)\n" \
  "buffer_store_dwordx4 v[44:47], v253, s[60:63], s7 offen sc1\n" \
  "s_waitcnt lgkmcnt(0)\n" \
  "buffer_store_dwordx4 v[48:51], v253, s[60:63], s8 offen sc1\n" \
  "v_mov_b64 v[56:57], 0\n" \
  "v_mov_b64 v[58:59], 0\n" \
  "v_mov_b64 v[60:61], 0\n" \
  "v_mov_b64 v[62:63], 0\n" \
  "v_mov_b64 v[64:65], 0\n" \
  "v_mov_b64 v[66:67], 0\n" \
  "v_mov_b64 v[68:69], 0\n" \
  "v_mov_b64 v[70:71], 0\n" \
  "v_mov_b64 v[72:73], 0\n" \
  "v_mov_b64 v[74:75], 0\n" \
  "v_mov_b64 v[76:77], 0\n" \
  "v_mov_b64 v[78:79], 0\n" \
  "v_lshl_add_u32 v32, v255, 6, v254\n" \
  "s_mul_i32 s81, s80, 3\n" \
  "s_add_u32 s81, s81, s10\n" \
  "s_lshl_b32 s81, s81, 1\n" \
  "s_add_u32 s81, s81, 61440\n" \
  "v_lshl_add_u32 v248, v32, 5, s81\n" \
  "ds_read_u16_d16_hi v56, v248 offset:0\n" \
  "ds_read_u16_d16_hi v57, v248 offset:512\n" \
  "ds_read_u16_d16_hi v58, v248 offset:1024\n" \
  "ds_read_u16_d16_hi v59, v248 offset:1536\n" \
  "ds_read_u16_d16_hi v60, v248 offset:8192\n" \
  "ds_read_u16_d16_hi v61, v248 offset:8704\n" \
  "ds_read_u16_d16_hi v62, v248 offset:9216\n" \
  "ds_read_u16_d16_hi v63, v248 offset:9728\n" \
  "v_lshrrev_b32 v32, 2, v11\n" \
  "s_lshl_b32 s81, s80, 6\n" \
  "v_add_u32 v33, s81, v32\n" \
  "v_mul_u32_u24 v245, 16512, v33\n" \
  "v_and_b32 v33, 3, v11\n" \
  "v_and_b32 v34, 7, v32\n" \
  "v_add_u32 v34, s78, v34\n" \
  "v_and_b32 v34, 7, v34\n" \
  "v_mul_u32_u24 v34, 97, v34\n" \
  "s_and_b32 s82, s4, 31\n" \
  "s_mul_i32 s81, s82, 3\n" \
  "v_add3_u32 v34, v34, v33, s81\n" \
  "v_lshlrev_b32 v34, 4, v34\n" \
  "s_cmp_eq_u32 s82, 31\n" \
  "s_cselect_b32 s82, 4, 3\n" \
  "v_cmp_eq_u32 vcc, s82, v33\n" \
  "v_mov_b32 v35, 1073741824\n" \
  "s_nop 0\n" \
  "v_cndmask_b32 v34, v34, v35, vcc\n" \
  "v_cndmask_b32 v227, v28, 0, vcc\n" \
  "v_add_u32 v245, v245, v34\n" \
  "v_mul_u32_u24 v244, 3360, v33\n" \
  "v_lshl_add_u32 v244, v32, 4, v244\n" \
  "s_lshl_b32 s81, s80, 8\n" \
  "s_add_u32 s81, s81, 122880\n" \
  "v_add_u32 v244, s81, v244\n" \
  "v_and_b32 v249, 4095, v250\n" \
  "s_mov_b32 s21, 149504\n" \
  "s_mov_b32 s22, 157696\n" \
  "s_mov_b32 s23, 165888\n" \
  "s_mov_b32 s5, 174080\n" \
  "s_mov_b32 s11, 182272\n" \
  "v_cvt_f32_u32 v34, s78\n" \
  "v_fmamk_f32 v35, v231, 0x41000000, v34\n" \
  "v_mul_f32 v33, 0x3c000000, v35\n" \
  "v_mul_f32 v32, v246, v33\n" \
  "v_add_f32 v113, v32, v33\n" \
  "v_add_f32 v114, v113, v33\n" \
  "v_add_f32 v115, v114, v33\n" \
  "v_cos_f32 v116, v32\n" \
  "v_sin_f32 v112, v32\n" \
  "v_cos_f32 v117, v113\n" \
  "v_sin_f32 v113, v113\n" \
  "v_cos_f32 v118, v114\n" \
  "v_sin_f32 v114, v114\n" \
  "v_cos_f32 v119, v115\n" \
  "v_sin_f32 v115, v115\n" \
  "v_cvt_pk_f16_f32 v120, v116, v112\n" \
  "v_cvt_pk_f16_f32 v124, -v112, v116\n" \
  "v_cvt_pk_f16_f32 v121, v117, v113\n" \
  "v_cvt_pk_f16_f32 v125, -v113, v117\n" \
  "v_cvt_pk_f16_f32 v122, v118, v114\n" \
  "v_cvt_pk_f16_f32 v126, -v114, v118\n" \
  "v_cvt_pk_f16_f32 v123, v119, v115\n" \
  "v_cvt_pk_f16_f32 v127, -v115, v119\n" \
  "s_sub_u32 s81, s80, 1\n" \
  "s_cmp_lt_u32 s81, 2\n" \
  "s_cselect_b32 s81, 0x80000000, 0\n" \
  "v_xor_b32 v112, s81, v120\n" \
  "v_xor_b32 v113, s81, v121\n" \
  "v_xor_b32 v114, s81, v122\n" \
  "v_xor_b32 v115, s81, v123\n" \
  "v_xor_b32 v116, s81, v124\n" \
  "v_xor_b32 v117, s81, v125\n" \
  "v_xor_b32 v118, s81, v126\n" \
  "v_xor_b32 v119, s81, v127\n" \
  "s_cmp_eq_u32 s80, 2\n" \
  "s_cselect_b32 s81, 0x80000000, 0\n" \
  "v_xor_b32 v120, s81, v120\n" \
  "v_xor_b32 v121, s81, v121\n" \
  "v_xor_b32 v122, s81, v122\n" \
  "v_xor_b32 v123, s81, v123\n" \
  "v_xor_b32 v124, s81, v124\n" \
  "v_xor_b32 v125, s81, v125\n" \
  "v_xor_b32 v126, s81, v126\n" \
  "v_xor_b32 v127, s81, v127\n" \
  "v_mul_f32 v33, 0x3a000000, v35\n" \
  "v_mul_f32 v32, v246, v33\n" \
  "v_add_f32 v129, v32, v33\n" \
  "v_add_f32 v130, v129, v33\n" \
  "v_add_f32 v131, v130, v33\n" \
  "v_cos_f32 v132, v32\n" \
  "v_sin_f32 v128, v32\n" \
  "v_cos_f32 v133, v129\n" \
  "v_sin_f32 v129, v129\n" \
  "v_cos_f32 v134, v130\n" \
  "v_sin_f32 v130, v130\n" \
  "v_cos_f32 v135, v131\n" \
  "v_sin_f32 v131, v131\n" \
  "s_mov_b32 s82, 0x3d800000\n" \
  "v_pk_mul_f32 v[132:133], v[132:133], s[82:83] op_sel_hi:[1,0]\n" \
  "v_pk_mul_f32 v[134:135], v[134:135], s[82:83] op_sel_hi:[1,0]\n" \
  "v_pk_mul_f32 v[128:129], v[128:129], s[82:83] op_sel_hi:[1,0] neg_lo:[1,0] neg_hi:[1,0]\n" \
  "v_pk_mul_f32 v[130:131], v[130:131], s[82:83] op_sel_hi:[1,0] neg_lo:[1,0] neg_hi:[1,0]\n" \
  "ds_read_u16_d16_hi v64, v248 offset:2\n" \
  "ds_read_u16_d16_hi v65, v248 offset:514\n" \
  "ds_read_u16_d16_hi v66, v248 offset:1026\n" \
  "ds_read_u16_d16_hi v67, v248 offset:1538\n" \
  "ds_read_u16_d16_hi v68, v248 offset:8194\n" \
  "ds_read_u16_d16_hi v69, v248 offset:8706\n" \
  "ds_read_u16_d16_hi v70, v248 offset:9218\n" \
  "ds_read_u16_d16_hi v71, v248 offset:9730\n" \
  "v_mul_f32 v33, 0x3d800000, v231\n" \
  "v_mul_f32 v32, v246, v33\n" \
  "v_add_f32 v137, v32, v33\n" \
  "v_add_f32 v138, v137, v33\n" \
  "v_add_f32 v139, v138, v33\n" \
  "v_cos_f32 v140, v32\n" \
  "v_sin_f32 v136, v32\n" \
  "v_cos_f32 v141, v137\n" \
  "v_sin_f32 v137, v137\n" \
  "v_cos_f32 v142, v138\n" \
  "v_sin_f32 v138, v138\n" \
  "v_cos_f32 v143, v139\n" \
  "v_sin_f32 v139, v139\n" \
  "v_cvt_pk_f16_f32 v144, v140, v136\n" \
  "v_cvt_pk_f16_f32 v148, -v136, v140\n" \
  "v_cvt_pk_f16_f32 v145, v141, v137\n" \
  "v_cvt_pk_f16_f32 v149, -v137, v141\n" \
  "v_cvt_pk_f16_f32 v146, v142, v138\n" \
  "v_cvt_pk_f16_f32 v150, -v138, v142\n" \
  "v_cvt_pk_f16_f32 v147, v143, v139\n" \
  "v_cvt_pk_f16_f32 v151, -v139, v143\n" \
  "v_xor_b32 v136, 0x80000000, v144\n" \
  "v_xor_b32 v137, 0x80000000, v145\n" \
  "v_xor_b32 v138, 0x80000000, v146\n" \
  "v_xor_b32 v139, 0x80000000, v147\n" \
  "v_xor_b32 v140, 0x8000, v148\n" \
  "v_xor_b32 v141, 0x8000, v149\n" \
  "v_xor_b32 v142, 0x8000, v150\n" \
  "v_xor_b32 v143, 0x8000, v151\n" \
  "v_cvt_f32_u32 v34, s78\n" \
  "v_mul_f32 v35, v231, v34\n" \
  "v_fmamk_f32 v32, v35, 0x3c000000, v32\n" \
  "v_add_f32 v153, v32, v33\n" \
  "v_add_f32 v154, v153, v33\n" \
  "v_add_f32 v155, v154, v33\n" \
  "v_cos_f32 v156, v32\n" \
  "v_sin_f32 v152, v32\n" \
  "v_cos_f32 v157, v153\n" \
  "v_sin_f32 v153, v153\n" \
  "v_cos_f32 v158, v154\n" \
  "v_sin_f32 v154, v154\n" \
  "v_cos_f32 v159, v155\n" \
  "v_sin_f32 v155, v155\n" \
  "v_cvt_pk_f16_f32 v160, v156, -v152\n" \
  "v_cvt_pk_f16_f32 v164, v152, v156\n" \
  "v_cvt_pk_f16_f32 v161, v157, -v153\n" \
  "v_cvt_pk_f16_f32 v165, v153, v157\n" \
  "v_cvt_pk_f16_f32 v162, v158, -v154\n" \
  "v_cvt_pk_f16_f32 v166, v154, v158\n" \
  "v_cvt_pk_f16_f32 v163, v159, -v155\n" \
  "v_cvt_pk_f16_f32 v167, v155, v159\n" \
  "s_sub_u32 s81, s80, 1\n" \
  "s_cmp_lt_u32 s81, 2\n" \
  "s_cselect_b32 s81, 0x80008000, 0\n" \
  "PG0_%=:\n" \
  "HOP0_%=:\n" \
  "s_cbranch_execz PG4_%=\n" \
  "v_xor_b32 v152, s81, v164\n" \
  "v_xor_b32 v153, s81, v165\n" \
  "v_xor_b32 v154, s81, v166\n" \
  "v_xor_b32 v155, s81, v167\n" \
  "s_cmp_eq_u32 s80, 2\n" \
  "s_cselect_b32 s81, 0x80008000, 0\n" \
  "v_xor_b32 v164, s81, v164\n" \
  "v_xor_b32 v165, s81, v165\n" \
  "v_xor_b32 v166, s81, v166\n" \
  "v_xor_b32 v167, s81, v167\n" \
  "v_mul_f32 v32, 0x3d800000, v32\n" \
  "v_mul_f32 v33, 0x3d800000, v33\n" \
  "v_add_f32 v157, v32, v33\n" \
  "v_add_f32 v158, v157, v33\n" \
  "v_add_f32 v159, v158, v33\n" \
  "v_cos_f32 v168, v32\n" \
  "v_sin_f32 v156, v32\n" \
  "v_cos_f32 v169, v157\n" \
  "v_sin_f32 v157, v157\n" \
  "v_cos_f32 v170, v158\n" \
  "v_sin_f32 v158, v158\n" \
  "v_cos_f32 v171, v159\n" \
  "v_sin_f32 v159, v159\n" \
  "s_mov_b32 s82, 0x3d800000\n" \
  "v_pk_mul_f32 v[168:169], v[168:169], s[82:83] op_sel_hi:[1,0]\n" \
  "v_pk_mul_f32 v[170:171], v[170:171], s[82:83] op_sel_hi:[1,0]\n" \
  "v_pk_mul_f32 v[156:157], v[156:157], s[82:83] op_sel_hi:[1,0]\n" \
  "v_pk_mul_f32 v[158:159], v[158:159], s[82:83] op_sel_hi:[1,0]\n" \
  "s_mov_b32 s6, 46333952\n" \
  "s_mov_b32 s7, 46598144\n" \
  "s_mov_b32 s8, 46862336\n" \
  "s_mov_b32 s9, 47126528\n" \
  "s_waitcnt vmcnt(0)\n" \
  "buffer_load_dwordx4 v[172:175], v245, s[60:63], s6 offen sc1 \n" \
  "buffer_load_dwordx4 v[176:179], v245, s[60:63], s7 offen sc1 \n" \
  "buffer_load_dwordx4 v[180:183], v245, s[60:63], s8 offen sc1 \n" \
  "buffer_load_dwordx4 v[184:187], v245, s[60:63], s9 offen sc1 \n" \
  "ds_read_u16_d16_hi v72, v248 offset:4\n" \
  "ds_read_u16_d16_hi v73, v248 offset:516\n" \
  "ds_read_u16_d16_hi v74, v248 offset:1028\n" \
  "ds_read_u16_d16_hi v75, v248 offset:1540\n" \
  "ds_read_u16_d16_hi v76, v248 offset:8196\n" \
  "ds_read_u16_d16_hi v77, v248 offset:8708\n" \
  "ds_read_u16_d16_hi v78, v248 offset:9220\n" \
  "ds_read_u16_d16_hi v79, v248 offset:9732\n" \
  "v_bfe_u32 v32, v11, 1, 3\n" \
  "v_mul_u32_u24 v226, 1168, v32\n" \
  "v_lshl_add_u32 v226, v255, 5, v226\n" \
  "v_and_b32 v32, 1, v11\n" \
  "v_lshl_add_u32 v226, v32, 4, v226\n" \
  "v_lshlrev_b32 v225, 4, v254\n" \
  "v_lshl_add_u32 v225, v255, 11, v225\n" \
  "v_and_b32 v32, 7, v11\n" \
  "v_lshrrev_b32 v33, 3, v11\n" \
  "v_mul_u32_u24 v224, 1168, v32\n" \
  "v_lshl_add_u32 v224, v33, 4, v224\n" \
  "v_lshlrev_b32 v223, 4, v32\n" \
  "v_and_b32 v32, 3, v33\n" \
  "v_lshl_add_u32 v223, v32, 8, v223\n" \
  "v_lshrrev_b32 v32, 5, v11\n" \
  "v_lshl_add_u32 v223, v32, 12, v223\n" \
  "s_and_b32 s81, s4, 7\n" \
  "s_lshl_b32 s81, s81, 8\n" \
  "s_add_u32 s81, s81, 2359296\n" \
  "v_add_u32 v225, s81, v225\n" \
  "s_bfe_u32 s81, s4, 0x20001\n" \
  "s_lshl_b32 s81, s81, 10\n" \
  "s_and_b32 s82, s4, 1\n" \
  "s_lshl_b32 s82, s82, 7\n" \
  "s_add_u32 s81, s81, s82\n" \
  "v_add_u32 v223, s81, v223\n" \
  "s_lshl_b32 s81, s80, 7\n" \
  "s_add_u32 s81, s81, 122880\n" \
  "v_add_u32 v226, s81, v226\n" \
  "v_add_u32 v224, s81, v224\n" \
  "s_cmp_eq_u32 s80, 0\n" \
  "s_cselect_b32 s83, 0, 1073741824\n" \
  "v_add_u32 v222, s83, v223\n" \
  "s_lshr_b32 s81, s4, 3\n" \
  "s_mul_i32 s88, s81, 73728\n" \
  "s_add_u32 s88, s88, 2097152\n" \
  "s_lshl_b32 s86, s80, 13\n" \
  "s_add_u32 s86, s86, s88\n" \
  "s_add_u32 s87, s86, 32768\n" \
  "s_add_u32 s88, s88, 65536\n" \
  "s_and_b32 s81, s4, 31\n" \
  "s_cselect_b32 s82, 2.0, 1.0\n" \
  "s_cmp_eq_u32 s80, 0\n" \
  "s_cselect_b32 s82, s82, 1.0\n" \
  "s_waitcnt lgkmcnt(0)\n" \
  "v_pk_mul_f32 v[56:57], v[56:57], s[82:83] op_sel_hi:[1,0]\n" \
  "v_pk_mul_f32 v[58:59], v[58:59], s[82:83] op_sel_hi:[1,0]\n" \
  "v_pk_mul_f32 v[60:61], v[60:61], s[82:83] op_sel_hi:[1,0]\n" \
  "v_pk_mul_f32 v[62:63], v[62:63], s[82:83] op_sel_hi:[1,0]\n" \
  "s_mul_i32 s81, s80, 3360\n" \
  "s_add_u32 s81, s81, 122880\n" \
  "v_add_u32 v220, s81, v24\n" \
  "s_lshl_b32 s10, s80, 4\n" \
  "s_cmp_eq_u32 s80, 3\n" \
  "s_cselect_b32 s81, 12288, s10\n" \
  "s_cselect_b32 s82, 16, 48\n" \
  "s_and_b32 s85, s4, 31\n" \
  "s_cmp_eq_u32 s85, 31\n" \
  "s_cselect_b32 s81, s10, s81\n" \
  "s_cselect_b32 s82, 64, s82\n" \
  "s_add_u32 s81, s81, 137728\n" \
  "s_and_b32 s85, s85, 7\n" \
  "v_lshl_add_u32 v32, v255, 6, v254\n" \
  "v_add_u32 v32, s85, v32\n" \
  "v_mov_b32 v34, s81\n" \
  "v_add_u32 v33, 48, v32\n" \
  "v_and_b32 v33, 255, v33\n" \
  "v_mad_u32_u24 v191, v33, s82, v34\n" \
  "v_mad_u32_u24 v188, v32, s82, v34\n" \
  "s_lshl_b32 s82, s82, 4\n" \
  "v_add_u32 v189, s82, v188\n" \
  "v_add_u32 v190, s82, v189\n" \
  "v_mov_b64 v[52:53], -1\n" \
  "v_mov_b64 v[54:55], -1\n" \
  "buffer_store_dwordx4 v[52:55], v222, s[60:63], s86 offen sc1\n" \
  "s_waitcnt vmcnt(1)\n" \
  "v_xor_b32 v27, v227, v175\n" \
  "v_bitop3_b32 v27, v27, v179, v227 bitop3:0xf6\n" \
  "v_bitop3_b32 v27, v27, v183, v227 bitop3:0xf6\n" \
  "v_bitop3_b32 v27, v27, v187, v227 bitop3:0xf6\n" \
  "v_cmp_ne_u32 vcc, 0, v27\n" \
  "s_cbranch_vccnz SLOW_ZA_%=\n" \
  "BACK_ZA_%=:\n" \
  "v_pk_mov_b32 v[40:41], v[172:173], v[176:177] op_sel:[0,0]\n" \
  "v_pk_mov_b32 v[42:43], v[180:181], v[184:185] op_sel:[0,0]\n" \
  "ds_write_b128 v244, v[40:43] offset:0\n" \
  "v_pk_mov_b32 v[44:45], v[172:173], v[176:177] op_sel:[1,1]\n" \
  "v_pk_mov_b32 v[46:47], v[180:181], v[184:185] op_sel:[1,1]\n" \
  "ds_write_b128 v244, v[44:47] offset:1120\n" \
  "v_pk_mov_b32 v[48:49], v[174:175], v[178:179] op_sel:[0,0]\n" \
  "v_pk_mov_b32 v[50:51], v[182:183], v[186:187] op_sel:[0,0]\n" \
  "ds_write_b128 v244, v[48:51] offset:2240\n" \
  "s_mul_i32 s6, s4, 16512\n" \
  "s_add_u32 s6, s6, 50561024\n" \
  "s_add_u32 s7, s6, 4096\n" \
  "s_add_u32 s8, s6, 8192\n" \
  "s_add_u32 s9, s6, 12288\n" \
  "s_waitcnt lgkmcnt(0)\n" \
  "s_barrier\n" \
  "ds_read_b128 v[172:175], v220 offset:0\n" \
  "ds_read_b128 v[184:187], v220 offset:1120\n" \
  "ds_read_b128 v[200:203], v220 offset:2240\n" \
  "buffer_load_dwordx3 v[16:18], v250, s[60:63], s21 offen offset:0 sc1\n" \
  "buffer_load_dwordx3 v[20:22], v250, s[60:63], s21 offen offset:512 sc1\n" \
  "buffer_load_dwordx3 v[80:82], v250, s[60:63], s22 offen offset:0 sc1\n" \
  "buffer_load_dwordx3 v[84:86], v250, s[60:63], s22 offen offset:512 sc1\n" \
  "buffer_load_dwordx3 v[88:90], v250, s[60:63], s23 offen offset:0 sc1\n" \
  "buffer_load_dwordx3 v[92:94], v250, s[60:63], s23 offen offset:512 sc1\n" \
  "s_waitcnt lgkmcnt(2)\n" \
  "v_mfma_f32_16x16x32_f16 v[52:55], v[172:175], v[124:127], 0\n" \
  "v_mfma_f32_16x16x32_f16 v[48:51], v[172:175], v[120:123], 0\n" \
  "s_waitcnt lgkmcnt(1)\n" \
  "v_mfma_f32_16x16x32_f16 v[180:183], v[184:187], v[124:127], 0\n" \
  "v_mfma_f32_16x16x32_f16 v[176:179], v[184:187], v[120:123], 0\n" \
  "s_waitcnt lgkmcnt(0)\n" \
  "v_mfma_f32_16x16x32_f16 v[196:199], v[200:203], v[116:119], 0\n" \
  "v_mfma_f32_16x16x32_f16 v[192:195], v[200:203], v[112:115], 0\n" \
  "v_mov_b32 v123, v28\n" \
  "v_mov_b32 v127, v28\n" \
  "v_mov_b32 v115, v28\n" \
  "v_mov_b32 v119, v28\n" \
  "buffer_load_dwordx3 v[96:98], v250, s[60:63], s5 offen offset:0 sc1\n" \
  "buffer_load_dwordx3 v[100:102], v250, s[60:63], s5 offen offset:512 sc1\n" \
  "buffer_load_dwordx3 v[104:106], v249, s[60:63], s11 offen offset:0 sc1\n" \
  "buffer_load_dwordx3 v[108:110], v249, s[60:63], s11 offen offset:512 sc1\n" \
  "v_pk_mul_f32 v[40:41], v[52:53], v[128:129]\n" \
  "v_pk_mul_f32 v[44:45], v[52:53], v[132:133]\n" \
  "v_pk_mul_f32 v[42:43], v[54:55], v[130:131]\n" \
  "v_pk_mul_f32 v[46:47], v[54:55], v[134:135]\n" \
  "v_pk_fma_f32 v[40:41], v[48:49], v[132:133], v[40:41] neg_lo:[0,0,1] neg_hi:[0,0,1]\n" \
  "v_pk_fma_f32 v[44:45], v[48:49], v[128:129], v[44:45]\n" \
  "v_pk_fma_f32 v[42:43], v[50:51], v[134:135], v[42:43] neg_lo:[0,0,1] neg_hi:[0,0,1]\n" \
  "v_pk_fma_f32 v[46:47], v[50:51], v[130:131], v[46:47]\n" \
  "v_cvt_pk_f16_f32 v172, v40, v44\n" \
  "v_cvt_pk_f16_f32 v173, v41, v45\n" \
  "v_cvt_pk_f16_f32 v174, v42, v46\n" \
  "v_cvt_pk_f16_f32 v175, v43, v47\n" \
  "v_pk_mul_f32 v[40:41], v[180:181], v[128:129]\n" \
  "v_pk_mul_f32 v[44:45], v[180:181], v[132:133]\n" \
  "v_pk_mul_f32 v[42:43], v[182:183], v[130:131]\n" \
  "v_pk_mul_f32 v[46:47], v[182:183], v[134:135]\n" \
  "v_pk_fma_f32 v[40:41], v[176:177], v[132:133], v[40:41] neg_lo:[0,0,1] neg_hi:[0,0,1]\n" \
  "v_pk_fma_f32 v[44:45], v[176:177], v[128:129], v[44:45]\n" \
  "v_pk_fma_f32 v[42:43], v[178:179], v[134:135], v[42:43] neg_lo:[0,0,1] neg_hi:[0,0,1]\n" \
  "v_pk_fma_f32 v[46:47], v[178:179], v[130:131], v[46:47]\n" \
  "v_mfma_f32_16x16x32_f16 v[52:55], v[148:151], v[172:175], 0\n" \
  "v_cvt_pk_f16_f32 v184, v40, v44\n" \
  "v_cvt_pk_f16_f32 v185, v41, v45\n" \
  "v_mfma_f32_16x16x32_f16 v[48:51], v[144:147], v[172:175], 0\n" \
  "v_cvt_pk_f16_f32 v186, v42, v46\n" \
  "v_cvt_pk_f16_f32 v187, v43, v47\n" \
  "v_pk_mul_f32 v[40:41], v[196:197], v[128:129]\n" \
  "v_pk_mul_f32 v[44:45], v[196:197], v[132:133]\n" \
  "v_pk_mul_f32 v[42:43], v[198:199], v[130:131]\n" \
  "v_pk_mul_f32 v[46:47], v[198:199], v[134:135]\n" \
  "v_pk_fma_f32 v[40:41], v[192:193], v[132:133], v[40:41] neg_lo:[0,0,1] neg_hi:[0,0,1]\n" \
  "v_pk_fma_f32 v[44:45], v[192:193], v[128:129], v[44:45]\n" \
  "v_pk_fma_f32 v[42:43], v[194:195], v[134:135], v[42:43] neg_lo:[0,0,1] neg_hi:[0,0,1]\n" \
  "v_pk_fma_f32 v[46:47], v[194:195], v[130:131], v[46:47]\n" \
  "v_mfma_f32_16x16x32_f16 v[180:183], v[148:151], v[184:187], 0\n" \
  "v_cvt_pk_f16_f32 v200, v40, v44\n" \
  "v_cvt_pk_f16_f32 v201, v41, v45\n" \
  "v_mfma_f32_16x16x32_f16 v[176:179], v[144:147], v[184:187], 0\n" \
  "v_cvt_pk_f16_f32 v202, v42, v46\n" \
  "v_cvt_pk_f16_f32 v203, v43, v47\n" \
  "s_mov_b32 s73, 44564480\n" \
  "s_add_u32 m0, s72, 16384\n" \
  "s_nop 0\n" \
  "buffer_load_dwordx4 v14, s[60:63], s73 offen sc1 lds\n" \
  "s_mov_b32 s73, 0\n" \
  "s_add_u32 m0, s72, 0\n" \
  "s_nop 0\n" \
  "buffer_load_dwordx4 v12, s[36:39], s73 offen  lds\n" \
  "s_add_u32 m0, s72, 4096\n" \
  "s_nop 0\n" \
  "buffer_load_dwordx4 v13, s[36:39], s73 offen  lds\n" \
  "s_add_u32 m0, s72, 8192\n" \
  "s_nop 0\n" \
  "buffer_load_dwordx4 v12, s[40:43], s73 offen  lds\n" \
  "s_add_u32 m0, s72, 12288\n" \
  "s_nop 0\n" \
  "buffer_load_dwordx4 v13, s[40:43], s73 offen  lds\n" \
  "v_pk_mul_f32 v[40:41], v[52:53], v[60:61]\n" \
  "v_pk_mul_f32 v[44:45], v[52:53], v[56:57]\n" \
  "v_pk_mul_f32 v[42:43], v[54:55], v[62:63]\n" \
  "v_pk_mul_f32 v[46:47], v[54:55], v[58:59]\n" \
  "v_pk_fma_f32 v[40:41], v[48:49], v[56:57], v[40:41] neg_lo:[0,0,1] neg_hi:[0,0,1]\n" \
  "v_pk_fma_f32 v[44:45], v[48:49], v[60:61], v[44:45]\n" \
  "v_pk_fma_f32 v[42:43], v[50:51], v[58:59], v[42:43] neg_lo:[0,0,1] neg_hi:[0,0,1]\n" \
  "v_pk_fma_f32 v[46:47], v[50:51], v[62:63], v[46:47]\n" \
  "v_mfma_f32_16x16x32_f16 v[196:199], v[148:151], v[200:203], 0\n" \
  "v_cvt_pk_f16_f32 v172, v40, v44\n" \
  "v_cvt_pk_f16_f32 v173, v41, v45\n" \
  "v_mfma_f32_16x16x32_f16 v[192:195], v[144:147], v[200:203], 0\n" \
  "v_cvt_pk_f16_f32 v174, v42, v46\n" \
  "v_cvt_pk_f16_f32 v175, v43, v47\n" \
  "v_pk_mul_f32 v[40:41], v[180:181], v[68:69]\n" \
  "v_pk_mul_f32 v[44:45], v[180:181], v[64:65]\n" \
  "v_pk_mul_f32 v[42:43], v[182:183], v[70:71]\n" \
  "v_pk_mul_f32 v[46:47], v[182:183], v[66:67]\n" \
  "v_pk_fma_f32 v[40:41], v[176:177], v[64:65], v[40:41] neg_lo:[0,0,1] neg_hi:[0,0,1]\n" \
  "v_pk_fma_f32 v[44:45], v[176:177], v[68:69], v[44:45]\n" \
  "v_pk_fma_f32 v[42:43], v[178:179], v[66:67], v[42:43] neg_lo:[0,0,1] neg_hi:[0,0,1]\n" \
  "v_pk_fma_f32 v[46:47], v[178:179], v[70:71], v[46:47]\n" \
  "v_mfma_f32_16x16x32_f16 v[52:55], v[172:175], v[140:143], 0\n" \
  "v_cvt_pk_f16_f32 v184, v40, v44\n" \
  "v_cvt_pk_f16_f32 v185, v41, v45\n" \
  "v_mfma_f32_16x16x32_f16 v[48:51], v[172:175], v[136:139], 0\n" \
  "v_cvt_pk_f16_f32 v186, v42, v46\n" \
  "v_cvt_pk_f16_f32 v187, v43, v47\n" \
  "v_pk_mul_f32 v[40:41], v[196:197], v[76:77]\n" \
  "v_pk_mul_f32 v[44:45], v[196:197], v[72:73]\n" \
  "v_pk_mul_f32 v[42:43], v[198:199], v[78:79]\n" \
  "v_pk_mul_f32 v[46:47], v[198:199], v[74:75]\n" \
  "v_pk_fma_f32 v[40:41], v[192:193], v[72:73], v[40:41] neg_lo:[0,0,1] neg_hi:[0,0,1]\n" \
  "v_pk_fma_f32 v[44:45], v[192:193], v[76:77], v[44:45]\n" \
  "v_pk_fma_f32 v[42:43], v[194:195], v[74:75], v[42:43] neg_lo:[0,0,1] neg_hi:[0,0,1]\n" \
  "v_pk_fma_f32 v[46:47], v[194:195], v[78:79], v[46:47]\n" \
  "v_mfma_f32_16x16x32_f16 v[180:183], v[184:187], v[140:143], 0\n" \
  "v_cvt_pk_f16_f32 v200, v40, v44\n" \
  "v_cvt_pk_f16_f32 v201, v41, v45\n" \
  "v_mfma_f32_16x16x32_f16 v[176:179], v[184:187], v[136:139], 0\n" \
  "v_cvt_pk_f16_f32 v202, v42, v46\n" \
  "v_cvt_pk_f16_f32 v203, v43, v47\n" \
  "v_pk_mul_f32 v[40:41], v[52:53], v[156:157]\n" \
  "v_pk_mul_f32 v[44:45], v[52:53], v[168:169]\n" \
  "v_pk_mul_f32 v[42:43], v[54:55], v[158:159]\n" \
  "v_pk_mul_f32 v[46:47], v[54:55], v[170:171]\n" \
  "v_pk_fma_f32 v[40:41], v[48:49], v[168:169], v[40:41] neg_lo:[0,0,1] neg_hi:[0,0,1]\n" \
  "v_pk_fma_f32 v[44:45], v[48:49], v[156:157], v[44:45]\n" \
  "v_pk_fma_f32 v[42:43], v[50:51], v[170:171], v[42:43] neg_lo:[0,0,1] neg_hi:[0,0,1]\n" \
  "v_pk_fma_f32 v[46:47], v[50:51], v[158:159], v[46:47]\n" \
  "v_mfma_f32_16x16x32_f16 v[196:199], v[200:203], v[140:143], 0\n" \
  "v_cvt_pk_f16_f32 v172, v40, v44\n" \
  "v_cvt_pk_f16_f32 v173, v41, v45\n" \
  "v_mfma_f32_16x16x32_f16 v[192:195], v[200:203], v[136:139], 0\n" \
  "v_cvt_pk_f16_f32 v174, v42, v46\n" \
  "v_cvt_pk_f16_f32 v175, v43, v47\n" \
  "v_pk_mul_f32 v[40:41], v[180:181], v[156:157]\n" \
  "v_pk_mul_f32 v[44:45], v[180:181], v[168:169]\n" \
  "v_pk_mul_f32 v[42:43], v[182:183], v[158:159]\n" \
  "v_pk_mul_f32 v[46:47], v[182:183], v[170:171]\n" \
  "v_pk_fma_f32 v[40:41], v[176:177], v[168:169], v[40:41] neg_lo:[0,0,1] neg_hi:[0,0,1]\n" \
  "v_pk_fma_f32 v[44:45], v[176:177], v[156:157], v[44:45]\n" \
  "v_pk_fma_f32 v[42:43], v[178:179], v[170:171], v[42:43] neg_lo:[0,0,1] neg_hi:[0,0,1]\n" \
  "v_pk_fma_f32 v[46:47], v[178:179], v[158:159], v[46:47]\n" \
  "v_mfma_f32_16x16x32_f16 v[52:55], v[164:167], v[172:175], 0\n" \
  "v_cvt_pk_f16_f32 v184, v40, v44\n" \
  "v_cvt_pk_f16_f32 v185, v41, v45\n" \
  "v_mfma_f32_16x16x32_f16 v[48:51], v[160:163], v[172:175], 0\n" \
  "v_cvt_pk_f16_f32 v186, v42, v46\n" \
  "v_cvt_pk_f16_f32 v187, v43, v47\n" \
  "v_pk_mul_f32 v[40:41], v[196:197], v[156:157]\n" \
  "v_pk_mul_f32 v[44:45], v[196:197], v[168:169]\n" \
  "v_pk_mul_f32 v[42:43], v[198:199], v[158:159]\n" \
  "v_pk_mul_f32 v[46:47], v[198:199], v[170:171]\n" \
  "v_pk_fma_f32 v[40:41], v[192:193], v[168:169], v[40:41] neg_lo:[0,0,1] neg_hi:[0,0,1]\n" \
  "v_pk_fma_f32 v[44:45], v[192:193], v[156:157], v[44:45]\n" \
  "v_pk_fma_f32 v[42:43], v[194:195], v[170:171], v[42:43] neg_lo:[0,0,1] neg_hi:[0,0,1]\n" \
  "v_pk_fma_f32 v[46:47], v[194:195], v[158:159], v[46:47]\n" \
  "v_mfma_f32_16x16x32_f16 v[180:183], v[164:167], v[184:187], 0\n" \
  "v_cvt_pk_f16_f32 v200, v40, v44\n" \
  "v_cvt_pk_f16_f32 v201, v41, v45\n" \
  "v_mfma_f32_16x16x32_f16 v[176:179], v[160:163], v[184:187], 0\n" \
  "v_cvt_pk_f16_f32 v202, v42, v46\n" \
  "v_cvt_pk_f16_f32 v203, v43, v47\n" \
  "v_cvt_pk_f16_f32 v120, v48, v52\n" \
  "v_cvt_pk_f16_f32 v124, v49, v53\n" \
  "v_mfma_f32_16x16x32_f16 v[196:199], v[152:155], v[200:203], 0\n" \
  "v_cvt_pk_f16_f32 v112, v50, v54\n" \
  "v_cvt_pk_f16_f32 v116, v51, v55\n" \
  "v_mfma_f32_16x16x32_f16 v[192:195], v[160:163], v[200:203], 0\n" \
  "v_cvt_pk_f16_f32 v121, v176, v180\n" \
  "v_cvt_pk_f16_f32 v125, v177, v181\n" \
  "v_cvt_pk_f16_f32 v113, v178, v182\n" \
  "v_cvt_pk_f16_f32 v117, v179, v183\n" \
  "s_nop 3\n" \
  "v_cvt_pk_f16_f32 v122, v192, v196\n" \
  "ds_write_b128 v188, v[120:123]\n" \
  "v_cvt_pk_f16_f32 v126, v193, v197\n" \
  "ds_write_b128 v189, v[124:127]\n" \
  "v_cvt_pk_f16_f32 v114, v194, v198\n" \
  "ds_write_b128 v190, v[112:115]\n" \
  "v_cvt_pk_f16_f32 v118, v195, v199\n" \
  "ds_write_b128 v191, v[116:119]\n" \
  "s_waitcnt vmcnt(15)\n" \
  "s_waitcnt lgkmcnt(0)\n" \
  "s_barrier\n" \
  "ds_read_b128 v[40:43], v213 offset:0\n" \
  "ds_read_b128 v[44:47], v213 offset:4096\n" \
  "ds_read_b128 v[48:51], v213 offset:8192\n" \
  "s_and_b32 s85, s4, 31\n" \
  "s_cmp_lg_u32 s85, 31\n" \
  "s_cbranch_scc1 ON15_%=\n" \
  "ds_read_b128 v[52:55], v213 offset:12288\n" \
  "s_waitcnt lgkmcnt(0)\n" \
  "buffer_store_dwordx4 v[52:55], v253, s[60:63], s9 offen sc1\n" \
  "ON15_%=:\n" \
  "s_waitcnt lgkmcnt(2)\n" \
  "buffer_store_dwordx4 v[40:43], v253, s[60:63], s6 offen sc1\n" \
  "s_waitcnt lgkmcnt(1)\n" \
  "buffer_store_dwordx4 v[44:47], v253, s[60:63], s7 offen sc1\n" \
  "s_waitcnt lgkmcnt(0)\n" \
  "buffer_store_dwordx4 v[48:51], v253, s[60:63], s8 offen sc1\n" \
  "s_or_b32 s81, s20, s80\n" \
  "s_cselect_b32 s81, 0x20000000, 0\n" \
  "v_add_u32 v32, s81, v1\n" \
  "s_mov_b32 s81, 8192\n" \
  "buffer_store_dword v28, v32, s[60:63], s81 offen sc1\n" \
  "s_waitcnt vmcnt(9)\n" \
  "v_xor_b32 v27, s84, v18\n" \
  "v_bitop3_b32 v27, v27, v22, s84 bitop3:0xf6\n" \
  "v_bitop3_b32 v27, v27, v82, s84 bitop3:0xf6\n" \
  "v_bitop3_b32 v27, v27, v86, s84 bitop3:0xf6\n" \
  "v_bitop3_b32 v27, v27, v90, s84 bitop3:0xf6\n" \
  "v_bitop3_b32 v27, v27, v94, s84 bitop3:0xf6\n" \
  "v_bitop3_b32 v27, v27, v98, s84 bitop3:0xf6\n" \
  "v_bitop3_b32 v27, v27, v102, s84 bitop3:0xf6\n" \
  "v_bitop3_b32 v27, v27, v106, s84 bitop3:0xf6\n" \
  "v_bitop3_b32 v27, v27, v110, s84 bitop3:0xf6\n" \
  "v_cmp_ne_u32 vcc, 0, v27\n" \
  "s_cbranch_vccnz SLOW_M1_%=\n" \
  "BACK_M1_%=:\n" \
  "v_lshlrev_b32 v247, 3, %2\n" \
  "v_and_b32 v35, 1023, v247\n" \
  "v_add_u32 v247, 154112, v247\n" \
  "v_add_u32 v35, 154112, v35\n" \
  "v_pk_add_f32 v[16:17], v[16:17], v[20:21]\n" \
  "ds_write_b64 v247, v[16:17] offset:0\n" \
  "v_pk_add_f32 v[80:81], v[80:81], v[84:85]\n" \
  "ds_write_b64 v247, v[80:81] offset:2048\n" \
  "v_pk_add_f32 v[88:89], v[88:89], v[92:93]\n" \
  "ds_write_b64 v247, v[88:89] offset:4096\n" \
  "v_pk_add_f32 v[96:97], v[96:97], v[100:101]\n" \
  "ds_write_b64 v247, v[96:97] offset:6144\n" \
  "v_pk_add_f32 v[104:105], v[104:105], v[108:109]\n" \
  "ds_write_b64 v35, v[104:105] offset:8192\n" \
  "v_lshrrev_b32 v32, 2, v11\n" \
  "v_and_b32 v33, 3, v11\n" \
  "s_lshl_b32 s81, s80, 5\n" \
  "v_add_u32 v34, s81, v32\n" \
  "v_mul_u32_u24 v34, 16512, v34\n" \
  "v_and_b32 v32, 7, v32\n" \
  "v_add_u32 v32, s4, v32\n" \
  "v_and_b32 v32, 255, v32\n" \
  "v_mul_u32_u24 v253, 3, v32\n" \
  "v_cmp_eq_u32 vcc, 3, v33\n" \
  "v_add_lshl_u32 v253, v253, v33, 4\n" \
  "v_mov_b32 v35, 1073741824\n" \
  "v_cndmask_b32 v253, v253, v35, vcc\n" \
  "v_cndmask_b32 v227, v28, 0, vcc\n" \
  "v_cmp_le_u32 vcc, 60, v11\n" \
  "v_lshl_add_u32 v35, v32, 2, v33\n" \
  "v_lshlrev_b32 v35, 4, v35\n" \
  "v_cndmask_b32 v250, v253, v35, vcc\n" \
  "v_cndmask_b32 v249, v227, v28, vcc\n" \
  "v_add_u32 v253, v253, v34\n" \
  "v_add_u32 v250, v250, v34\n" \
  "s_and_b32 s81, s4, 31\n" \
  "s_lshl_b32 s81, s81, 1\n" \
  "s_lshr_b32 s82, s80, 1\n" \
  "s_add_u32 s81, s81, s82\n" \
  "s_mul_i32 s81, s81, 28\n" \
  "v_add_u32 v32, s81, v11\n" \
  "v_mul_u32_u24 v33, 16384, v32\n" \
  "v_lshrrev_b32 v33, 16, v33\n" \
  "v_mul_u32_u24 v33, 8192, v33\n" \
  "v_lshl_add_u32 v32, v32, 8, v33\n" \
  "v_add_u32 v33, 36, v11\n" \
  "v_lshrrev_b32 v33, 6, v33\n" \
  "v_lshl_add_u32 v32, v33, 30, v32\n" \
  "s_mul_i32 s81, s78, 1152\n" \
  "s_add_u32 s81, s81, 1179648\n" \
  "v_add_u32 v248, s81, v32\n" \
  "v_mul_f32 v32, v246, v242\n" \
  "v_mul_f32 v32, 0x3d000000, v32\n" \
  "v_mul_f32 v34, 0x3e800000, v243\n" \
  "v_add_f32 v17, v32, v34\n" \
  "v_add_f32 v18, v17, v34\n" \
  "v_add_f32 v19, v18, v34\n" \
  "v_cos_f32 v20, v32\n" \
  "v_sin_f32 v16, v32\n" \
  "v_cos_f32 v21, v17\n" \
  "v_sin_f32 v17, v17\n" \
  "v_cos_f32 v22, v18\n" \
  "v_sin_f32 v18, v18\n" \
  "v_cos_f32 v23, v19\n" \
  "v_sin_f32 v19, v19\n" \
  "v_cvt_pk_f16_f32 v40, v20, -v16\n" \
  "v_cvt_pk_f16_f32 v44, v16, v20\n" \
  "v_cvt_pk_f16_f32 v41, v21, -v17\n" \
  "v_cvt_pk_f16_f32 v45, v17, v21\n" \
  "v_cvt_pk_f16_f32 v43, v22, -v18\n" \
  "v_cvt_pk_f16_f32 v47, v18, v22\n" \
  "v_cvt_pk_f16_f32 v42, v23, -v19\n" \
  "v_cvt_pk_f16_f32 v46, v19, v23\n" \
  "v_xor_b32 v36, v230, v229\n" \
  "v_xor_b32 v16, v229, v40\n" \
  "v_xor_b32 v17, v229, v41\n" \
  "v_xor_b32 v18, v229, v42\n" \
  "v_xor_b32 v19, v229, v43\n" \
  "v_xor_b32 v20, v229, v44\n" \
  "v_xor_b32 v21, v229, v45\n" \
  "v_xor_b32 v22, v229, v46\n" \
  "v_xor_b32 v23, v229, v47\n" \
  "v_xor_b32 v48, v230, v40\n" \
  "v_xor_b32 v49, v230, v41\n" \
  "v_xor_b32 v50, v230, v42\n" \
  "v_xor_b32 v51, v230, v43\n" \
  "v_xor_b32 v52, v230, v44\n" \
  "v_xor_b32 v53, v230, v45\n" \
  "v_xor_b32 v54, v230, v46\n" \
  "v_xor_b32 v55, v230, v47\n" \
  "v_xor_b32 v56, v36, v40\n" \
  "v_xor_b32 v57, v36, v41\n" \
  "v_xor_b32 v58, v36, v42\n" \
  "v_xor_b32 v59, v36, v43\n" \
  "v_xor_b32 v60, v36, v44\n" \
  "v_xor_b32 v61, v36, v45\n" \
  "v_xor_b32 v62, v36, v46\n" \
  "v_xor_b32 v63, v36, v47\n" \
  "s_bitcmp1_b32 s80, 0\n" \
  "s_cselect_b32 s81, -1, 0\n" \
  "v_and_b32 v36, s81, v230\n" \
  "v_xor_b32 v64, v36, v40\n" \
  "v_xor_b32 v65, v36, v41\n" \
  "v_xor_b32 v66, v36, v42\n" \
  "v_xor_b32 v67, v36, v43\n" \
  "v_xor_b32 v68, v36, v44\n" \
  "v_xor_b32 v69, v36, v45\n" \
  "v_xor_b32 v70, v36, v46\n" \
  "v_xor_b32 v71, v36, v47\n" \
  "v_xor_b32 v72, v229, v64\n" \
  "v_xor_b32 v73, v229, v65\n" \
  "v_xor_b32 v74, v229, v66\n" \
  "v_xor_b32 v75, v229, v67\n" \
  "v_xor_b32 v76, v229, v68\n" \
  "v_xor_b32 v77, v229, v69\n" \
  "v_xor_b32 v78, v229, v70\n" \
  "v_xor_b32 v79, v229, v71\n" \
  "v_mul_f32 v33, 0x3d800000, v231\n" \
  "v_mul_f32 v32, v246, v33\n" \
  "v_mul_f32 v34, v231, v228\n" \
  "v_fmamk_f32 v32, v34, 0x3be38e39, v32\n" \
  "v_add_f32 v81, v32, v33\n" \
  "v_add_f32 v82, v81, v33\n" \
  "v_add_f32 v83, v82, v33\n" \
  "v_cos_f32 v84, v32\n" \
  "v_sin_f32 v80, v32\n" \
  "v_cos_f32 v85, v81\n" \
  "v_sin_f32 v81, v81\n" \
  "v_cos_f32 v86, v82\n" \
  "v_sin_f32 v82, v82\n" \
  "v_cos_f32 v87, v83\n" \
  "v_sin_f32 v83, v83\n" \
  "v_cvt_pk_f16_f32 v88, v84, -v80\n" \
  "v_cvt_pk_f16_f32 v92, v80, v84\n" \
  "v_cvt_pk_f16_f32 v89, v85, -v81\n" \
  "v_cvt_pk_f16_f32 v93, v81, v85\n" \
  "v_cvt_pk_f16_f32 v90, v86, -v82\n" \
  "v_cvt_pk_f16_f32 v94, v82, v86\n" \
  "v_cvt_pk_f16_f32 v91, v87, -v83\n" \
  "v_cvt_pk_f16_f32 v95, v83, v87\n" \
  "v_mul_f32 v33, 0x3c800000, v243\n" \
  "v_mul_f32 v32, v246, v33\n" \
  "v_add_f32 v81, v32, v33\n" \
  "v_add_f32 v82, v81, v33\n" \
  "v_add_f32 v83, v82, v33\n" \
  "v_cos_f32 v84, v32\n" \
  "v_sin_f32 v80, v32\n" \
  "v_cos_f32 v85, v81\n" \
  "v_sin_f32 v81, v81\n" \
  "v_cos_f32 v86, v82\n" \
  "v_sin_f32 v82, v82\n" \
  "v_cos_f32 v87, v83\n" \
  "v_sin_f32 v83, v83\n" \
  "s_mov_b32 s82, 0x3c715bef\n" \
  "v_pk_mul_f32 v[84:85], v[84:85], s[82:83] op_sel_hi:[1,0]\n" \
  "v_pk_mul_f32 v[86:87], v[86:87], s[82:83] op_sel_hi:[1,0]\n" \
  "v_pk_mul_f32 v[80:81], v[80:81], s[82:83] op_sel_hi:[1,0]\n" \
  "v_pk_mul_f32 v[82:83], v[82:83], s[82:83] op_sel_hi:[1,0]\n" \
  "v_mul_f32 v34, v243, v228\n" \
  "v_fmamk_f32 v32, v34, 0x3ae38e39, v32\n" \
  "v_add_f32 v97, v32, v33\n" \
  "v_add_f32 v98, v97, v33\n" \
  "v_add_f32 v99, v98, v33\n" \
  "v_cos_f32 v100, v32\n" \
  "v_sin_f32 v96, v32\n" \
  "v_cos_f32 v101, v97\n" \
  "v_sin_f32 v97, v97\n" \
  "v_cos_f32 v102, v98\n" \
  "v_sin_f32 v98, v98\n" \
  "v_cos_f32 v103, v99\n" \
  "v_sin_f32 v99, v99\n" \
  "s_mov_b32 s82, 0x3cf15bef\n" \
  "v_pk_mul_f32 v[100:101], v[100:101], s[82:83] op_sel_hi:[1,0]\n" \
  "v_pk_mul_f32 v[102:103], v[102:103], s[82:83] op_sel_hi:[1,0]\n" \
  "v_pk_mul_f32 v[96:97], v[96:97], s[82:83] op_sel_hi:[1,0]\n" \
  "v_pk_mul_f32 v[98:99], v[98:99], s[82:83] op_sel_hi:[1,0]\n" \
  "s_mov_b32 s6, 50561024\n" \
  "s_mov_b32 s7, 50825216\n" \
  "s_mov_b32 s8, 52674560\n" \
  "s_mov_b32 s9, 52938752\n" \
  "s_waitcnt vmcnt(1)\n" \
  "buffer_load_dwordx4 v[104:107], v253, s[60:63], s6 offen sc1 \n" \
  "buffer_load_dwordx4 v[108:111], v250, s[60:63], s7 offen sc1 \n" \
  "buffer_load_dwordx4 v[112:115], v253, s[60:63], s8 offen sc1 \n" \
  "buffer_load_dwordx4 v[116:119], v250, s[60:63], s9 offen sc1 \n" \
  "v_lshrrev_b32 v32, 2, v11\n" \
  "v_and_b32 v33, 3, v11\n" \
  "v_lshlrev_b32 v247, 4, v32\n" \
  "v_sub_u32 v245, 123128, v247\n" \
  "v_add_u32 v247, 122880, v247\n" \
  "v_lshlrev_b32 v34, 2, v33\n" \
  "s_sub_u32 s82, 8, s80\n" \
  "s_and_b32 s82, s82, 7\n" \
  "v_mov_b32 v32, s80\n" \
  "v_mov_b32 v35, s82\n" \
  "v_cmp_le_u32 vcc, 2, v33\n" \
  "s_mov_b32 s81, 0x330\n" \
  "v_bfe_u32 v36, s81, v34, 4\n" \
  "v_cndmask_b32 v208, v247, v245, vcc\n" \
  "v_cmp_eq_u32 vcc, 2, v33\n" \
  "v_lshl_add_u32 v208, v36, 11, v208\n" \
  "s_nop 0\n" \
  "v_cndmask_b32 v36, v32, v35, vcc\n" \
  "v_lshl_add_u32 v208, v36, 8, v208\n" \
  "v_xor_b32 v211, 1024, v208\n" \
  "v_cmp_le_u32 vcc, 2, v33\n" \
  "s_mov_b32 s81, 0x5241\n" \
  "v_bfe_u32 v36, s81, v34, 4\n" \
  "v_cndmask_b32 v209, v247, v245, vcc\n" \
  "v_lshl_add_u32 v209, v36, 11, v209\n" \
  "v_cndmask_b32 v36, v32, v35, vcc\n" \
  "v_lshl_add_u32 v209, v36, 8, v209\n" \
  "v_xor_b32 v212, 1024, v209\n" \
  "v_cmp_le_u32 vcc, 1, v33\n" \
  "s_mov_b32 s81, 0x5142\n" \
  "v_bfe_u32 v36, s81, v34, 4\n" \
  "v_cndmask_b32 v210, v247, v245, vcc\n" \
  "v_lshl_add_u32 v210, v36, 11, v210\n" \
  "v_cndmask_b32 v36, v32, v35, vcc\n" \
  "v_lshl_add_u32 v210, v36, 8, v210\n" \
  "v_xor_b32 v213, 1024, v210\n" \
  "v_add_u32 v214, 4294962688, v214\n" \
  "v_add_u32 v215, 4294962688, v215\n" \
  "v_lshlrev_b32 v221, 3, v251\n" \
  "v_add_u32 v221, 154112, v221\n" \
  "s_lshl_b32 s81, s72, 2\n" \
  "s_add_u32 s81, s81, 104448\n" \
  "v_add_u32 v247, s81, v24\n" \
  "s_and_b32 s81, s72, 1024\n" \
  "s_add_u32 s81, s81, 102400\n" \
  "v_add_u32 v245, s81, v24\n" \
  "v_lshlrev_b32 v244, 3, %2\n" \
  "v_add_u32 v244, 102400, v244\n" \
  "v_lshrrev_b32 v32, 5, %2\n" \
  "v_mul_u32_u24 v32, 1168, v32\n" \
  "v_lshl_add_u32 v32, v251, 2, v32\n" \
  "v_add_u32 v220, 122880, v32\n" \
  "s_waitcnt vmcnt(0)\n" \
  "v_xor_b32 v27, v227, v107\n" \
  "v_bitop3_b32 v27, v27, v111, v249 bitop3:0xf6\n" \
  "v_bitop3_b32 v27, v27, v115, v227 bitop3:0xf6\n" \
  "v_bitop3_b32 v27, v27, v119, v249 bitop3:0xf6\n" \
  "v_cmp_ne_u32 vcc, 0, v27\n" \
  "s_cbranch_vccnz SLOW_ZB_%=\n" \
  "BACK_ZB_%=:\n" \
  "ds_write2_b32 v208, v104, v108 offset0:0 offset1:1\n" \
  "ds_write2_b32 v209, v105, v109 offset0:0 offset1:1\n" \
  "ds_write2_b32 v210, v106, v110 offset0:0 offset1:1\n" \
  "ds_write2_b32 v211, v112, v116 offset0:0 offset1:1\n" \
  "ds_write2_b32 v212, v113, v117 offset0:0 offset1:1\n" \
  "ds_write2_b32 v213, v114, v118 offset0:0 offset1:1\n" \
  "s_waitcnt lgkmcnt(0)\n" \
  "s_barrier\n" \
  "ds_read_b128 v[104:107], v215 offset:2048\n" \
  "ds_read_b128 v[108:111], v215 offset:3072\n" \
  "ds_read_b128 v[112:115], v214 offset:0\n" \
  "ds_read_b128 v[116:119], v214 offset:1024\n" \
  "ds_read_b64 v[196:197], v221 offset:0\n" \
  "ds_read_b64 v[198:199], v221 offset:256\n" \
  "ds_read_b64 v[200:201], v221 offset:512\n" \
  "ds_read_b64 v[202:203], v221 offset:768\n" \
  "ds_read_b64 v[204:205], v221 offset:1024\n" \
  "ds_read_b64 v[206:207], v221 offset:1280\n" \
  "ds_read_b64 v[208:209], v221 offset:1536\n" \
  "ds_read_b64 v[210:211], v221 offset:1792\n" \
  "ds_read_b64 v[212:213], v221 offset:2048\n" \
  "buffer_load_dword v252, v248, s[44:47], 0 offen sc1\n" \
  "s_waitcnt lgkmcnt(12)\n" \
  "v_mfma_f32_16x16x32_f16 v[144:147], v[104:107], v[44:47], 0\n" \
  "v_mfma_f32_16x16x32_f16 v[132:135], v[104:107], v[40:43], 0\n" \
  "s_waitcnt lgkmcnt(11)\n" \
  "v_mfma_f32_16x16x32_f16 v[144:147], v[108:111], v[20:23], v[144:147]\n" \
  "v_mfma_f32_16x16x32_f16 v[132:135], v[108:111], v[16:19], v[132:135]\n" \
  "v_mfma_f32_16x16x32_f16 v[152:155], v[104:107], v[52:55], 0\n" \
  "v_mfma_f32_16x16x32_f16 v[148:151], v[104:107], v[48:51], 0\n" \
  "v_mfma_f32_16x16x32_f16 v[152:155], v[108:111], v[60:63], v[152:155]\n" \
  "PG1_%=:\n" \
  "HOP1_%=:\n" \
  "s_cbranch_execz PG5_%=\n" \
  "v_mfma_f32_16x16x32_f16 v[148:151], v[108:111], v[56:59], v[148:151]\n" \
  "s_waitcnt lgkmcnt(9)\n" \
  "v_mfma_f32_16x16x32_f16 v[160:163], v[112:115], v[68:71], 0\n" \
  "v_mfma_f32_16x16x32_f16 v[156:159], v[112:115], v[64:67], 0\n" \
  "v_pk_mul_f32 v[120:121], v[144:145], v[96:97]\n" \
  "v_pk_mul_f32 v[124:125], v[144:145], v[100:101]\n" \
  "v_pk_mul_f32 v[122:123], v[146:147], v[98:99]\n" \
  "v_pk_mul_f32 v[126:127], v[146:147], v[102:103]\n" \
  "v_pk_fma_f32 v[120:121], v[132:133], v[100:101], v[120:121] neg_lo:[0,0,1] neg_hi:[0,0,1]\n" \
  "v_pk_fma_f32 v[124:125], v[132:133], v[96:97], v[124:125]\n" \
  "v_pk_fma_f32 v[122:123], v[134:135], v[102:103], v[122:123] neg_lo:[0,0,1] neg_hi:[0,0,1]\n" \
  "v_pk_fma_f32 v[126:127], v[134:135], v[98:99], v[126:127]\n" \
  "v_mfma_f32_16x16x32_f16 v[160:163], v[116:119], v[76:79], v[160:163]\n" \
  "v_cvt_pk_f16_f32 v128, v120, v124\n" \
  "v_cvt_pk_f16_f32 v129, v121, v125\n" \
  "v_mfma_f32_16x16x32_f16 v[156:159], v[116:119], v[72:75], v[156:159]\n" \
  "v_cvt_pk_f16_f32 v130, v122, v126\n" \
  "v_cvt_pk_f16_f32 v131, v123, v127\n" \
  "v_pk_mul_f32 v[120:121], v[152:153], v[96:97]\n" \
  "v_pk_mul_f32 v[124:125], v[152:153], v[100:101]\n" \
  "v_pk_mul_f32 v[122:123], v[154:155], v[98:99]\n" \
  "v_pk_mul_f32 v[126:127], v[154:155], v[102:103]\n" \
  "v_pk_fma_f32 v[120:121], v[148:149], v[100:101], v[120:121] neg_lo:[0,0,1] neg_hi:[0,0,1]\n" \
  "v_pk_fma_f32 v[124:125], v[148:149], v[96:97], v[124:125]\n" \
  "v_pk_fma_f32 v[122:123], v[150:151], v[102:103], v[122:123] neg_lo:[0,0,1] neg_hi:[0,0,1]\n" \
  "v_pk_fma_f32 v[126:127], v[150:151], v[98:99], v[126:127]\n" \
  "v_mfma_f32_16x16x32_f16 v[132:135], v[128:131], v[88:91], 0\n" \
  "v_cvt_pk_f16_f32 v104, v120, v124\n" \
  "v_cvt_pk_f16_f32 v105, v121, v125\n" \
  "v_mfma_f32_16x16x32_f16 v[144:147], v[128:131], v[92:95], 0\n" \
  "v_cvt_pk_f16_f32 v106, v122, v126\n" \
  "v_cvt_pk_f16_f32 v107, v123, v127\n" \
  "v_pk_mul_f32 v[120:121], v[160:161], v[80:81]\n" \
  "v_pk_mul_f32 v[124:125], v[160:161], v[84:85]\n" \
  "v_pk_mul_f32 v[122:123], v[162:163], v[82:83]\n" \
  "v_pk_mul_f32 v[126:127], v[162:163], v[86:87]\n" \
  "v_pk_fma_f32 v[120:121], v[156:157], v[84:85], v[120:121] neg_lo:[0,0,1] neg_hi:[0,0,1]\n" \
  "v_pk_fma_f32 v[124:125], v[156:157], v[80:81], v[124:125]\n" \
  "v_pk_fma_f32 v[122:123], v[158:159], v[86:87], v[122:123] neg_lo:[0,0,1] neg_hi:[0,0,1]\n" \
  "v_pk_fma_f32 v[126:127], v[158:159], v[82:83], v[126:127]\n" \
  "v_mfma_f32_16x16x32_f16 v[148:151], v[104:107], v[88:91], 0\n" \
  "v_cvt_pk_f16_f32 v108, v120, v124\n" \
  "v_cvt_pk_f16_f32 v109, v121, v125\n" \
  "v_mfma_f32_16x16x32_f16 v[152:155], v[104:107], v[92:95], 0\n" \
  "v_cvt_pk_f16_f32 v110, v122, v126\n" \
  "v_cvt_pk_f16_f32 v111, v123, v127\n" \
  "ds_write_b128 v247, v[132:135] offset:0\n" \
  "ds_write_b128 v247, v[144:147] offset:2048\n" \
  "v_mfma_f32_16x16x32_f16 v[156:159], v[108:111], v[136:139], 0\n" \
  "ds_write_b128 v247, v[148:151] offset:1024\n" \
  "s_nop 1\n" \
  "ds_write_b128 v247, v[152:155] offset:3072\n" \
  "s_nop 3\n" \
  "ds_write_b128 v245, v[156:159]\n" \
  "v_lshlrev_b32 v176, 16, v232\n" \
  "v_and_b32 v177, 0xffff0000, v232\n" \
  "v_lshlrev_b32 v178, 16, v233\n" \
  "v_and_b32 v179, 0xffff0000, v233\n" \
  "v_lshlrev_b32 v180, 16, v234\n" \
  "v_and_b32 v181, 0xffff0000, v234\n" \
  "v_lshlrev_b32 v182, 16, v235\n" \
  "v_and_b32 v183, 0xffff0000, v235\n" \
  "v_lshlrev_b32 v184, 16, v236\n" \
  "v_and_b32 v185, 0xffff0000, v236\n" \
  "v_lshlrev_b32 v186, 16, v237\n" \
  "v_and_b32 v187, 0xffff0000, v237\n" \
  "v_lshlrev_b32 v188, 16, v238\n" \
  "v_and_b32 v189, 0xffff0000, v238\n" \
  "v_lshlrev_b32 v190, 16, v239\n" \
  "v_and_b32 v191, 0xffff0000, v239\n" \
  "v_lshlrev_b32 v192, 16, v240\n" \
  "v_and_b32 v193, 0xffff0000, v240\n" \
  "s_waitcnt lgkmcnt(0)\n" \
  "s_barrier\n" \
  "ds_read_b64 v[156:157], v244 offset:0\n" \
  "ds_read_b64 v[166:167], v244 offset:10240\n" \
  "ds_read_b64 v[168:169], v244 offset:12288\n" \
  "ds_read_b64 v[160:161], v244 offset:4096\n" \
  "ds_read_b64 v[158:159], v244 offset:2048\n" \
  "ds_read_b64 v[164:165], v244 offset:8192\n" \
  "ds_read_b64 v[162:163], v244 offset:6144\n" \
  "ds_read_b64 v[172:173], v244 offset:16384\n" \
  "ds_read_b64 v[170:171], v244 offset:14336\n" \
  "s_mov_b32 s73, 44566528\n" \
  "s_add_u32 m0, s72, 36864\n" \
  "s_nop 0\n" \
  "buffer_load_dwordx4 v14, s[60:63], s73 offen sc1 lds\n" \
  "s_mov_b32 s73, 589824\n" \
  "s_add_u32 m0, s72, 20480\n" \
  "s_nop 0\n" \
  "buffer_load_dwordx4 v12, s[36:39], s73 offen  lds\n" \
  "s_add_u32 m0, s72, 24576\n" \
  "s_nop 0\n" \
  "buffer_load_dwordx4 v13, s[36:39], s73 offen  lds\n" \
  "s_add_u32 m0, s72, 28672\n" \
  "s_nop 0\n" \
  "buffer_load_dwordx4 v12, s[40:43], s73 offen  lds\n" \
  "s_add_u32 m0, s72, 32768\n" \
  "s_nop 0\n" \
  "buffer_load_dwordx4 v13, s[40:43], s73 offen  lds\n" \
  "s_waitcnt lgkmcnt(7)\n" \
  "v_pk_fma_f32 v[132:133], v[166:167], -0.5, v[156:157] op_sel_hi:[1,0,1]\n" \
  "v_pk_add_f32 v[130:131], v[156:157], v[166:167]\n" \
  "s_waitcnt lgkmcnt(6)\n" \
  "v_pk_mul_f32 v[134:135], v[168:169], s[16:17] op_sel:[0,1] op_sel_hi:[1,1]\n" \
  "s_waitcnt lgkmcnt(4)\n" \
  "v_pk_fma_f32 v[138:139], v[160:161], s[14:15], v[134:135] op_sel:[0,1,0] op_sel_hi:[1,1,1]\n" \
  "v_pk_fma_f32 v[142:143], v[160:161], s[16:17], v[134:135] op_sel_hi:[1,0,1] neg_lo:[0,0,1] neg_hi:[0,0,1]\n" \
  "v_pk_fma_f32 v[146:147], v[160:161], s[18:19], v[134:135] op_sel_hi:[1,0,1]\n" \
  "v_pk_fma_f32 v[136:137], v[158:159], s[12:13], v[132:133] op_sel_hi:[1,0,1]\n" \
  "v_pk_fma_f32 v[140:141], v[158:159], s[12:13], v[132:133] op_sel:[0,1,0] op_sel_hi:[1,1,1]\n" \
  "v_pk_fma_f32 v[144:145], v[158:159], s[14:15], v[132:133] op_sel_hi:[1,0,1]\n" \
  "s_waitcnt lgkmcnt(0)\n" \
  "ds_read_b64 v[58:59], v221 offset:4608\n" \
  "ds_read_b64 v[60:61], v221 offset:4864\n" \
  "ds_read_b64 v[62:63], v221 offset:5120\n" \
  "ds_read_b64 v[64:65], v221 offset:5376\n" \
  "ds_read_b64 v[66:67], v221 offset:5632\n" \
  "ds_read_b64 v[68:69], v221 offset:5888\n" \
  "v_pk_add_f32 v[128:129], v[158:159], v[162:163]\n" \
  "v_pk_add_f32 v[150:151], v[160:161], v[164:165] neg_lo:[0,1] neg_hi:[0,1]\n" \
  "v_pk_fma_f32 v[138:139], v[164:165], s[16:17], v[138:139] op_sel_hi:[1,0,1]\n" \
  "v_pk_fma_f32 v[142:143], v[164:165], s[18:19], v[142:143] op_sel_hi:[1,0,1]\n" \
  "v_pk_fma_f32 v[146:147], v[164:165], s[14:15], v[146:147] op_sel:[0,1,0] op_sel_hi:[1,1,1] neg_lo:[1,0,0] neg_hi:[1,0,0]\n" \
  "v_pk_fma_f32 v[136:137], v[162:163], s[12:13], v[136:137] op_sel:[0,1,0] op_sel_hi:[1,1,1]\n" \
  "v_pk_fma_f32 v[140:141], v[162:163], s[14:15], v[140:141] op_sel_hi:[1,0,1]\n" \
  "v_pk_fma_f32 v[144:145], v[162:163], s[12:13], v[144:145] op_sel_hi:[1,0,1]\n" \
  "v_pk_add_f32 v[128:129], v[128:129], v[170:171]\n" \
  "v_pk_add_f32 v[150:151], v[150:151], v[172:173]\n" \
  "ds_read_b64 v[70:71], v221 offset:6144\n" \
  "ds_read_b64 v[72:73], v221 offset:6400\n" \
  "ds_read_b64 v[74:75], v221 offset:6656\n" \
  "ds_read_b64 v[40:41], v221 offset:2304\n" \
  "ds_read_b64 v[42:43], v221 offset:2560\n" \
  "ds_read_b64 v[44:45], v221 offset:2816\n" \
  "v_pk_fma_f32 v[138:139], v[172:173], s[18:19], v[138:139] op_sel_hi:[1,0,1]\n" \
  "v_pk_fma_f32 v[142:143], v[172:173], s[14:15], v[142:143] op_sel:[0,1,0] op_sel_hi:[1,1,1] neg_lo:[1,0,0] neg_hi:[1,0,0]\n" \
  "v_pk_fma_f32 v[146:147], v[172:173], s[16:17], v[146:147] op_sel_hi:[1,0,1] neg_lo:[1,0,0] neg_hi:[1,0,0]\n" \
  "v_pk_fma_f32 v[136:137], v[170:171], s[14:15], v[136:137] op_sel_hi:[1,0,1]\n" \
  "v_pk_fma_f32 v[140:141], v[170:171], s[12:13], v[140:141] op_sel_hi:[1,0,1]\n" \
  "v_pk_fma_f32 v[144:145], v[170:171], s[12:13], v[144:145] op_sel:[0,1,0] op_sel_hi:[1,1,1]\n" \
  "v_pk_add_f32 v[152:153], v[130:131], v[128:129]\n" \
  "v_pk_fma_f32 v[148:149], v[128:129], -0.5, v[130:131] op_sel_hi:[1,0,1]\n" \
  "v_pk_mul_f32 v[150:151], v[150:151], s[16:17] op_sel:[0,1] op_sel_hi:[1,1]\n" \
  "ds_read_b64 v[46:47], v221 offset:3072\n" \
  "ds_read_b64 v[48:49], v221 offset:3328\n" \
  "ds_read_b64 v[50:51], v221 offset:3584\n" \
  "ds_read_b64 v[52:53], v221 offset:3840\n" \
  "ds_read_b64 v[54:55], v221 offset:4096\n" \
  "ds_read_b64 v[56:57], v221 offset:4352\n" \
  "s_mov_b32 s73, 44568576\n" \
  "s_add_u32 m0, s72, 57344\n" \
  "s_nop 0\n" \
  "buffer_load_dwordx4 v14, s[60:63], s73 offen sc1 lds\n" \
  "s_mov_b32 s73, 1179648\n" \
  "s_add_u32 m0, s72, 40960\n" \
  "s_nop 0\n" \
  "buffer_load_dwordx4 v12, s[36:39], s73 offen  lds\n" \
  "s_add_u32 m0, s72, 45056\n" \
  "s_nop 0\n" \
  "buffer_load_dwordx4 v13, s[36:39], s73 offen  lds\n" \
  "s_add_u32 m0, s72, 49152\n" \
  "s_nop 0\n" \
  "buffer_load_dwordx4 v12, s[40:43], s73 offen  lds\n" \
  "s_add_u32 m0, s72, 53248\n" \
  "s_nop 0\n" \
  "buffer_load_dwordx4 v13, s[40:43], s73 offen  lds\n" \
  "v_pk_fma_f32 v[176:177], v[152:153], v[196:197], v[176:177]\n" \
  "v_cvt_pk_bf16_f32 v232, v176, v177\n" \
  "v_pk_add_f32 v[156:157], v[136:137], v[138:139] neg_lo:[0,1] neg_hi:[0,1]\n" \
  "v_pk_add_f32 v[158:159], v[136:137], v[138:139]\n" \
  "v_pk_fma_f32 v[178:179], v[156:157], v[198:199], v[178:179]\n" \
  "v_cvt_pk_bf16_f32 v233, v178, v179\n" \
  "v_pk_fma_f32 v[192:193], v[158:159], v[212:213], v[192:193]\n" \
  "v_cvt_pk_bf16_f32 v240, v192, v193\n" \
  "ds_write2_b32 v220, v232, v233 offset0:0 offset1:32\n" \
  "ds_write_b32 v220, v240 offset:1024\n" \
  "v_pk_add_f32 v[156:157], v[140:141], v[142:143] neg_lo:[0,1] neg_hi:[0,1]\n" \
  "v_pk_add_f32 v[158:159], v[140:141], v[142:143]\n" \
  "v_pk_fma_f32 v[180:181], v[156:157], v[200:201], v[180:181]\n" \
  "v_cvt_pk_bf16_f32 v234, v180, v181\n" \
  "v_pk_fma_f32 v[190:191], v[158:159], v[210:211], v[190:191]\n" \
  "v_cvt_pk_bf16_f32 v239, v190, v191\n" \
  "ds_write2_b32 v220, v234, v239 offset0:64 offset1:224\n" \
  "v_pk_add_f32 v[156:157], v[144:145], v[146:147] neg_lo:[0,1] neg_hi:[0,1]\n" \
  "v_pk_add_f32 v[158:159], v[144:145], v[146:147]\n" \
  "v_pk_fma_f32 v[184:185], v[156:157], v[204:205], v[184:185]\n" \
  "v_cvt_pk_bf16_f32 v236, v184, v185\n" \
  "v_pk_fma_f32 v[186:187], v[158:159], v[206:207], v[186:187]\n" \
  "v_cvt_pk_bf16_f32 v237, v186, v187\n" \
  "ds_write2_b32 v220, v236, v237 offset0:128 offset1:160\n" \
  "v_pk_add_f32 v[156:157], v[148:149], v[150:151] neg_lo:[0,1] neg_hi:[0,1]\n" \
  "v_pk_add_f32 v[158:159], v[148:149], v[150:151]\n" \
  "v_pk_fma_f32 v[182:183], v[156:157], v[202:203], v[182:183]\n" \
  "v_cvt_pk_bf16_f32 v235, v182, v183\n" \
  "v_pk_fma_f32 v[188:189], v[158:159], v[208:209], v[188:189]\n" \
  "v_cvt_pk_bf16_f32 v238, v188, v189\n" \
  "ds_write2_b32 v220, v235, v238 offset0:96 offset1:192\n" \
  "s_waitcnt lgkmcnt(5)\n" \
  "v_readlane_b32 s81, v176, 0\n" \
  "v_readlane_b32 s82, v176, 32\n" \
  "s_mov_b32 vcc_lo, 0\n" \
  "s_mov_b32 vcc_hi, -1\n" \
  "v_mov_b32 v16, s81\n" \
  "v_mov_b32 v17, s82\n" \
  "v_cndmask_b32 v16, v16, v17, vcc\n" \
  "v_pk_add_f32 v[176:177], v[176:177], v[16:17] op_sel_hi:[1,0] neg_lo:[0,1] neg_hi:[0,1]\n" \
  "v_pk_add_f32 v[178:179], v[178:179], v[16:17] op_sel_hi:[1,0] neg_lo:[0,1] neg_hi:[0,1]\n" \
  "v_pk_add_f32 v[180:181], v[180:181], v[16:17] op_sel_hi:[1,0] neg_lo:[0,1] neg_hi:[0,1]\n" \
  "v_pk_add_f32 v[182:183], v[182:183], v[16:17] op_sel_hi:[1,0] neg_lo:[0,1] neg_hi:[0,1]\n" \
  "v_pk_add_f32 v[184:185], v[184:185], v[16:17] op_sel_hi:[1,0] neg_lo:[0,1] neg_hi:[0,1]\n" \
  "v_pk_add_f32 v[186:187], v[186:187], v[16:17] op_sel_hi:[1,0] neg_lo:[0,1] neg_hi:[0,1]\n" \
  "v_pk_add_f32 v[188:189], v[188:189], v[16:17] op_sel_hi:[1,0] neg_lo:[0,1] neg_hi:[0,1]\n" \
  "v_pk_add_f32 v[190:191], v[190:191], v[16:17] op_sel_hi:[1,0] neg_lo:[0,1] neg_hi:[0,1]\n" \
  "v_pk_add_f32 v[192:193], v[192:193], v[16:17] op_sel_hi:[1,0] neg_lo:[0,1] neg_hi:[0,1]\n" \
  "v_pk_add_f32 v[20:21], v[176:177], v[178:179]\n" \
  "v_pk_mul_f32 v[22:23], v[176:177], v[176:177]\n" \
  "v_pk_fma_f32 v[22:23], v[178:179], v[178:179], v[22:23]\n" \
  "v_pk_add_f32 v[20:21], v[20:21], v[180:181]\n" \
  "v_pk_fma_f32 v[22:23], v[180:181], v[180:181], v[22:23]\n" \
  "v_pk_add_f32 v[20:21], v[20:21], v[182:183]\n" \
  "v_pk_fma_f32 v[22:23], v[182:183], v[182:183], v[22:23]\n" \
  "v_pk_add_f32 v[20:21], v[20:21], v[184:185]\n" \
  "v_pk_fma_f32 v[22:23], v[184:185], v[184:185], v[22:23]\n" \
  "v_pk_add_f32 v[20:21], v[20:21], v[186:187]\n" \
  "v_pk_fma_f32 v[22:23], v[186:187], v[186:187], v[22:23]\n" \
  "v_pk_add_f32 v[20:21], v[20:21], v[188:189]\n" \
  "v_pk_fma_f32 v[22:23], v[188:189], v[188:189], v[22:23]\n" \
  "v_pk_add_f32 v[20:21], v[20:21], v[190:191]\n" \
  "v_pk_fma_f32 v[22:23], v[190:191], v[190:191], v[22:23]\n" \
  "v_pk_add_f32 v[20:21], v[20:21], v[192:193]\n" \
  "v_pk_fma_f32 v[22:23], v[192:193], v[192:193], v[22:23]\n" \
  "v_add_f32 v16, v20, v21\n" \
  "v_add_f32 v17, v22, v23\n" \
  "s_mov_b32 s82, 0x3ae38e39\n" \
  "v_add_f32_dpp v16, v16, v16 quad_perm:[1,0,3,2] row_mask:0xf bank_mask:0xf bound_ctrl:1\n" \
  "v_add_f32_dpp v17, v17, v17 quad_perm:[1,0,3,2] row_mask:0xf bank_mask:0xf bound_ctrl:1\n" \
  "v_pk_mul_f32 v[176:177], v[176:177], v[58:59]\n" \
  "v_add_f32_dpp v16, v16, v16 quad_perm:[2,3,0,1] row_mask:0xf bank_mask:0xf bound_ctrl:1\n" \
  "v_add_f32_dpp v17, v17, v17 quad_perm:[2,3,0,1] row_mask:0xf bank_mask:0xf bound_ctrl:1\n" \
  "v_pk_mul_f32 v[178:179], v[178:179], v[60:61]\n" \
  "v_add_f32_dpp v16, v16, v16 row_half_mirror row_mask:0xf bank_mask:0xf bound_ctrl:1\n" \
  "v_add_f32_dpp v17, v17, v17 row_half_mirror row_mask:0xf bank_mask:0xf bound_ctrl:1\n" \
  "v_pk_mul_f32 v[180:181], v[180:181], v[62:63]\n" \
  "v_add_f32_dpp v16, v16, v16 row_mirror row_mask:0xf bank_mask:0xf bound_ctrl:1\n" \
  "v_add_f32_dpp v17, v17, v17 row_mirror row_mask:0xf bank_mask:0xf bound_ctrl:1\n" \
  "v_pk_mov_b32 v[18:19], v[16:17], v[16:17] op_sel:[0,1]\n" \
  "v_pk_mul_f32 v[182:183], v[182:183], v[64:65]\n" \
  "v_pk_mul_f32 v[184:185], v[184:185], v[66:67]\n" \
  "v_permlane16_swap_b32 v16, v18\n" \
  "v_permlane16_swap_b32 v17, v19\n" \
  "v_pk_add_f32 v[16:17], v[16:17], v[18:19]\n" \
  "v_add_u32 v220, 9344, v220\n" \
  "s_waitcnt lgkmcnt(0)\n" \
  "s_barrier\n" \
  "ds_read_b128 v[96:99], v226 offset:0\n" \
  "ds_read_b128 v[100:103], v226 offset:512\n" \
  "ds_read_b128 v[104:107], v226 offset:1024\n" \
  "v_pk_mul_f32 v[16:17], v[16:17], s[82:83] op_sel_hi:[1,0]\n" \
  "v_fma_f32 v17, -v16, v16, v17\n" \
  "v_max_f32 v17, 0, v17\n" \
  "v_add_f32 v17, 0x3727c5ac, v17\n" \
  "v_rsq_f32 v17, v17\n" \
  "v_pk_mul_f32 v[186:187], v[186:187], v[68:69]\n" \
  "v_pk_mul_f32 v[188:189], v[188:189], v[70:71]\n" \
  "v_pk_mul_f32 v[190:191], v[190:191], v[72:73]\n" \
  "v_pk_mul_f32 v[192:193], v[192:193], v[74:75]\n" \
  "v_mul_f32 v16, v16, v17\n" \
  "v_pk_fma_f32 v[40:41], v[58:59], v[16:17], v[40:41] op_sel_hi:[1,0,1] neg_lo:[1,0,0] neg_hi:[1,0,0]\n" \
  "s_waitcnt lgkmcnt(2)\n" \
  "buffer_store_dwordx4 v[96:99], v225, s[60:63], s86 offen sc1\n" \
  "v_pk_fma_f32 v[42:43], v[60:61], v[16:17], v[42:43] op_sel_hi:[1,0,1] neg_lo:[1,0,0] neg_hi:[1,0,0]\n" \
  "v_pk_fma_f32 v[44:45], v[62:63], v[16:17], v[44:45] op_sel_hi:[1,0,1] neg_lo:[1,0,0] neg_hi:[1,0,0]\n" \
  "v_pk_fma_f32 v[46:47], v[64:65], v[16:17], v[46:47] op_sel_hi:[1,0,1] neg_lo:[1,0,0] neg_hi:[1,0,0]\n" \
  "v_pk_fma_f32 v[48:49], v[66:67], v[16:17], v[48:49] op_sel_hi:[1,0,1] neg_lo:[1,0,0] neg_hi:[1,0,0]\n" \
  "s_waitcnt lgkmcnt(1)\n" \
  "buffer_store_dwordx4 v[100:103], v225, s[60:63], s87 offen sc1\n" \
  "v_pk_fma_f32 v[50:51], v[68:69], v[16:17], v[50:51] op_sel_hi:[1,0,1] neg_lo:[1,0,0] neg_hi:[1,0,0]\n" \
  "v_pk_fma_f32 v[52:53], v[70:71], v[16:17], v[52:53] op_sel_hi:[1,0,1] neg_lo:[1,0,0] neg_hi:[1,0,0]\n" \
  "v_pk_fma_f32 v[54:55], v[72:73], v[16:17], v[54:55] op_sel_hi:[1,0,1] neg_lo:[1,0,0] neg_hi:[1,0,0]\n" \
  "v_pk_fma_f32 v[56:57], v[74:75], v[16:17], v[56:57] op_sel_hi:[1,0,1] neg_lo:[1,0,0] neg_hi:[1,0,0]\n" \
  "s_waitcnt lgkmcnt(0)\n" \
  "s_cmp_lg_u32 s80, 0\n" \
  "s_cbranch_scc1 ON16_%=\n" \
  "buffer_store_dwordx4 v[104:107], v225, s[60:63], s88 offen sc1\n" \
  "ON16_%=:\n" \
  "v_pk_fma_f32 v[176:177], v[176:177], v[16:17], v[40:41] op_sel:[0,1,0] op_sel_hi:[1,1,1]\n" \
  "v_cvt_pk_bf16_f32 v232, v176, v177\n" \
  "v_pk_fma_f32 v[178:179], v[178:179], v[16:17], v[42:43] op_sel:[0,1,0] op_sel_hi:[1,1,1]\n" \
  "v_cvt_pk_bf16_f32 v233, v178, v179\n" \
  "ds_write2_b32 v220, v232, v233 offset0:0 offset1:32\n" \
  "v_pk_fma_f32 v[180:181], v[180:181], v[16:17], v[44:45] op_sel:[0,1,0] op_sel_hi:[1,1,1]\n" \
  "v_cvt_pk_bf16_f32 v234, v180, v181\n" \
  "v_pk_fma_f32 v[182:183], v[182:183], v[16:17], v[46:47] op_sel:[0,1,0] op_sel_hi:[1,1,1]\n" \
  "v_cvt_pk_bf16_f32 v235, v182, v183\n" \
  "ds_write2_b32 v220, v234, v235 offset0:64 offset1:96\n" \
  "v_pk_fma_f32 v[184:185], v[184:185], v[16:17], v[48:49] op_sel:[0,1,0] op_sel_hi:[1,1,1]\n" \
  "v_cvt_pk_bf16_f32 v236, v184, v185\n" \
  "v_pk_fma_f32 v[186:187], v[186:187], v[16:17], v[50:51] op_sel:[0,1,0] op_sel_hi:[1,1,1]\n" \
  "v_cvt_pk_bf16_f32 v237, v186, v187\n" \
  "ds_write2_b32 v220, v236, v237 offset0:128 offset1:160\n" \
  "v_pk_fma_f32 v[188:189], v[188:189], v[16:17], v[52:53] op_sel:[0,1,0] op_sel_hi:[1,1,1]\n" \
  "v_cvt_pk_bf16_f32 v238, v188, v189\n" \
  "v_pk_fma_f32 v[190:191], v[190:191], v[16:17], v[54:55] op_sel:[0,1,0] op_sel_hi:[1,1,1]\n" \
  "v_cvt_pk_bf16_f32 v239, v190, v191\n" \
  "ds_write2_b32 v220, v238, v239 offset0:192 offset1:224\n" \
  "v_pk_fma_f32 v[192:193], v[192:193], v[16:17], v[56:57] op_sel:[0,1,0] op_sel_hi:[1,1,1]\n" \
  "v_cvt_pk_bf16_f32 v240, v192, v193\n" \
  "ds_write_b32 v220, v240 offset:1024\n" \
  "s_waitcnt lgkmcnt(0)\n" \
  "s_barrier\n" \
  "ds_read_b128 v[96:99], v224 offset:9344\n" \
  "ds_read_b128 v[100:103], v224 offset:9856\n" \
  "ds_read_b128 v[104:107], v224 offset:10368\n" \
  "s_waitcnt lgkmcnt(2)\n" \
  "buffer_store_dwordx4 v[96:99], v223, s[60:63], s86 offen sc1\n" \
  "s_waitcnt lgkmcnt(1)\n" \
  "buffer_store_dwordx4 v[100:103], v223, s[60:63], s87 offen sc1\n" \
  "s_waitcnt lgkmcnt(0)\n" \
  "s_cmp_lg_u32 s80, 0\n" \
  "s_cbranch_scc1 ON17_%=\n" \
  "buffer_store_dwordx4 v[104:107], v223, s[60:63], s88 offen sc1\n" \
  "ON17_%=:\n" \
  "s_nop 0\n" \
  "s_lshl_b32 s7, s77, 3\n" \
  "s_add_u32 s7, s7, s78\n" \
  "s_lshl_b32 s7, s7, 6\n" \
  "s_add_u32 s7, s7, 161024\n" \
  "v_lshl_add_u32 v34, v255, 4, s7\n" \
  "ds_read_b128 v[76:79], v34\n" \
  "s_lshl_b32 s7, s77, 2\n" \
  "s_add_u32 s7, s7, s80\n" \
  "s_lshl_b32 s7, s7, 4\n" \
  "s_add_u32 s7, s7, 163072\n" \
  "v_mov_b32 v34, s7\n" \
  "ds_read_b128 v[80:83], v34\n" \
  "s_lshl_b32 s81, s4, 7\n" \
  "s_add_u32 s81, s81, 262144\n" \
  "s_lshr_b32 s6, s83, 1\n" \
  "v_add_u32 v32, s6, v1\n" \
  "v_lshrrev_b32 v35, 3, v11\n" \
  "v_lshlrev_b32 v37, 7, v11\n" \
  "v_lshl_add_u32 v37, v35, 27, v37\n" \
  "s_waitcnt lgkmcnt(0)\n" \
  "v_cvt_pk_bf16_f32 v38, v76, v77\n" \
  "v_cvt_pk_bf16_f32 v76, v80, v81\n" \
  "v_cvt_pk_bf16_f32 v39, v78, v79\n" \
  "v_cvt_pk_bf16_f32 v77, v82, v83\n" \
  "v_readfirstlane_b32 s2, v76\n" \
  "v_readfirstlane_b32 s3, v77\n" \
  "s_waitcnt vmcnt(0)\n" \
  "s_barrier\n" \
  "buffer_store_dword v28, v32, s[60:63], s81 offen sc1\n" \
  "s_sleep 5\n" \
  "s_mov_b32 s74, 0\n" \
  "buffer_load_dwordx4 v[64:67], v24, s[52:55], s74 offen offset:0 sc1\n" \
  "buffer_load_dwordx4 v[68:71], v24, s[52:55], s74 offen offset:1024 sc1\n" \
  "buffer_load_dwordx4 v[72:75], v24, s[52:55], s74 offen offset:2048 sc1\n" \
  "buffer_load_dwordx4 v[76:79], v24, s[52:55], s74 offen offset:3072 sc1\n" \
  "buffer_load_dwordx4 v[80:83], v25, s[52:55], s74 offen offset:0 sc1\n" \
  "buffer_load_dwordx4 v[84:87], v25, s[52:55], s74 offen offset:1024 sc1\n" \
  "buffer_load_dwordx4 v[88:91], v25, s[52:55], s74 offen offset:2048 sc1\n" \
  "buffer_load_dwordx4 v[92:95], v25, s[52:55], s74 offen offset:3072 sc1\n" \
  "s_waitcnt vmcnt(0)\n" \
  "v_max3_u32 v27, v64, v65, v66\n" \
  "v_max3_u32 v27, v27, v67, v68\n" \
  "v_max3_u32 v27, v27, v69, v70\n" \
  "v_max3_u32 v27, v27, v71, v72\n" \
  "v_max3_u32 v27, v27, v73, v74\n" \
  "v_max3_u32 v27, v27, v75, v76\n" \
  "v_max3_u32 v27, v27, v77, v78\n" \
  "v_max3_u32 v27, v27, v79, v80\n" \
  "v_max3_u32 v27, v27, v81, v82\n" \
  "v_max3_u32 v27, v27, v83, v84\n" \
  "v_max3_u32 v27, v27, v85, v86\n" \
  "v_max3_u32 v27, v27, v87, v88\n" \
  "v_max3_u32 v27, v27, v89, v90\n" \
  "v_max3_u32 v27, v27, v91, v92\n" \
  "v_max3_u32 v27, v27, v93, v94\n" \
  "v_max3_u32 v27, v27, v95, v95\n" \
  "v_cmp_eq_u32 vcc, -1, v27\n" \
  "s_cbranch_vccnz SLOW_HS_%=\n" \
  "BACK_HS_%=:\n" \
  "ds_read_b64_tr_b16 v[40:41], v8 offset:16384\n" \
  "ds_read_b64_tr_b16 v[42:43], v8 offset:16896\n" \
  "ds_read_b64_tr_b16 v[44:45], v8 offset:18432\n" \
  "ds_read_b64_tr_b16 v[46:47], v8 offset:18944\n" \
  "ds_read_b64_tr_b16 v[48:49], v8 offset:17408\n" \
  "ds_read_b64_tr_b16 v[50:51], v8 offset:17920\n" \
  "ds_read_b64_tr_b16 v[52:53], v8 offset:19456\n" \
  "ds_read_b64_tr_b16 v[54:55], v8 offset:19968\n" \
  "s_waitcnt lgkmcnt(4)\n" \
  "v_mfma_f32_16x16x32_bf16 v[224:227], v[40:43], v[64:67], 0\n" \
  "buffer_load_dword v27, v37, s[60:63], s75 offen offset:0 sc1\n" \
  "v_mfma_f32_16x16x32_bf16 v[224:227], v[44:47], v[80:83], v[224:227]\n" \
  "ds_read_b64_tr_b16 v[56:57], v2 offset:0\n" \
  "v_add_u32 v4, 61440, v2\n" \
  "v_mfma_f32_16x16x32_bf16 v[228:231], v[44:47], v[84:87], 0\n" \
  "v_add_u32 v5, 61440, v3\n" \
  "v_add_u32 v9, 61440, v8\n" \
  "v_mfma_f32_16x16x32_bf16 v[228:231], v[40:43], v[68:71], v[228:231]\n" \
  "ds_read_b64_tr_b16 v[58:59], v2 offset:2048\n" \
  "v_add_u32 v6, 122880, v2\n" \
  "v_mfma_f32_16x16x32_bf16 v[232:235], v[40:43], v[72:75], 0\n" \
  "v_add_u32 v7, 122880, v3\n" \
  "v_add_u32 v10, 122880, v8\n" \
  "v_mfma_f32_16x16x32_bf16 v[232:235], v[44:47], v[88:91], v[232:235]\n" \
  "ds_read_b64_tr_b16 v[60:61], v2 offset:4096\n" \
  "s_lshl_b32 s89, s79, 2\n" \
  "v_mfma_f32_16x16x32_bf16 v[236:239], v[44:47], v[92:95], 0\n" \
  "ds_read_b64_tr_b16 v[62:63], v2 offset:6144\n" \
  "s_add_u32 s89, s89, s77\n" \
  "v_mfma_f32_16x16x32_bf16 v[236:239], v[40:43], v[76:79], v[236:239]\n" \
  "s_lshl_b32 s87, s89, 15\n" \
  "s_waitcnt lgkmcnt(4)\n" \
  "v_mfma_f32_16x16x32_bf16 v[240:243], v[48:51], v[64:67], 0\n" \
  "s_add_u32 s87, s87, 1048576\n" \
  "s_lshl_b32 s88, s89, 16\n" \
  "v_mfma_f32_16x16x32_bf16 v[240:243], v[52:55], v[80:83], v[240:243]\n" \
  "ds_read_b64_tr_b16 v[40:41], v2 offset:8192\n" \
  "s_add_u32 s88, s88, 42467328\n" \
  "v_mfma_f32_16x16x32_bf16 v[244:247], v[52:55], v[84:87], 0\n" \
  "s_lshl_b32 s89, s89, 2\n" \
  "s_add_u32 s89, s89, s80\n" \
  "v_mfma_f32_16x16x32_bf16 v[244:247], v[48:51], v[68:71], v[244:247]\n" \
  "ds_read_b64_tr_b16 v[42:43], v2 offset:10240\n" \
  "s_lshl_b32 s89, s89, 17\n" \
  "v_mfma_f32_16x16x32_bf16 v[248:251], v[48:51], v[72:75], 0\n" \
  "s_add_u32 s64, s60, 25690112\n" \
  "s_addc_u32 s65, s61, 0\n" \
  "v_mfma_f32_16x16x32_bf16 v[248:251], v[52:55], v[88:91], v[248:251]\n" \
  "ds_read_b64_tr_b16 v[44:45], v2 offset:12288\n" \
  "v_mfma_f32_16x16x32_bf16 v[252:255], v[52:55], v[92:95], 0\n" \
  "ds_read_b64_tr_b16 v[46:47], v2 offset:14336\n" \
  "v_mfma_f32_16x16x32_bf16 v[252:255], v[48:51], v[76:79], v[252:255]\n" \
  "s_waitcnt lgkmcnt(4)\n" \
  "v_mfma_f32_16x16x32_bf16 v[96:99], v[56:59], v[64:67], 0\n" \
  "s_add_u32 s64, s64, s89\n" \
  "s_addc_u32 s65, s65, 0\n" \
  "v_mfma_f32_16x16x32_bf16 v[96:99], v[60:63], v[80:83], v[96:99]\n" \
  "ds_read_b64_tr_b16 v[48:49], v3 offset:0\n" \
  "s_mov_b32 s66, 131072\n" \
  "v_mfma_f32_16x16x32_bf16 v[100:103], v[60:63], v[84:87], 0\n" \
  "s_mov_b32 s67, 0x20000\n" \
  "s_mul_i32 s95, s78, 18432\n" \
  "v_mfma_f32_16x16x32_bf16 v[100:103], v[56:59], v[68:71], v[100:103]\n" \
  "ds_read_b64_tr_b16 v[50:51], v3 offset:2048\n" \
  "s_lshl_b32 s97, s80, 14\n" \
  "v_mfma_f32_16x16x32_bf16 v[104:107], v[56:59], v[72:75], 0\n" \
  "s_add_u32 s97, s97, s88\n" \
  "s_lshl_b32 s98, s78, 10\n" \
  "v_mfma_f32_16x16x32_bf16 v[104:107], v[60:63], v[88:91], v[104:107]\n" \
  "ds_read_b64_tr_b16 v[52:53], v3 offset:4096\n" \
  "s_add_u32 s97, s97, s98\n" \
  "v_mfma_f32_16x16x32_bf16 v[108:111], v[60:63], v[92:95], 0\n" \
  "ds_read_b64_tr_b16 v[54:55], v3 offset:6144\n" \
  "s_add_u32 s98, s97, 8192\n" \
  "v_mfma_f32_16x16x32_bf16 v[108:111], v[56:59], v[76:79], v[108:111]\n" \
  "s_waitcnt lgkmcnt(4)\n" \
  "v_mfma_f32_16x16x32_bf16 v[112:115], v[40:43], v[64:67], 0\n" \
  "s_waitcnt vmcnt(0)\n" \
  "v_mfma_f32_16x16x32_bf16 v[112:115], v[44:47], v[80:83], v[112:115]\n" \
  "ds_read_b64_tr_b16 v[56:57], v3 offset:8192\n" \
  "v_mfma_f32_16x16x32_bf16 v[116:119], v[44:47], v[84:87], 0\n" \
  "v_cmp_ne_u32 vcc, s84, v27\n" \
  "s_and_b32 vcc_lo, vcc_lo, 0xff\n" \
  "s_cbranch_scc1 SLOW_H2_%=\n" \
  "BACK_H2_%=:\n" \
  "v_mfma_f32_16x16x32_bf16 v[116:119], v[40:43], v[68:71], v[116:119]\n" \
  "ds_read_b64_tr_b16 v[58:59], v3 offset:10240\n" \
  "s_mov_b32 s74, 8192\n" \
  "v_mfma_f32_16x16x32_bf16 v[120:123], v[40:43], v[72:75], 0\n" \
  "buffer_load_dwordx4 a[0:3], v24, s[52:55], s74 offen offset:0 sc1\n" \
  "v_mfma_f32_16x16x32_bf16 v[120:123], v[44:47], v[88:91], v[120:123]\n" \
  "ds_read_b64_tr_b16 v[60:61], v3 offset:12288\n" \
  "v_mfma_f32_16x16x32_bf16 v[124:127], v[44:47], v[92:95], 0\n" \
  "ds_read_b64_tr_b16 v[62:63], v3 offset:14336\n" \
  "v_mfma_f32_16x16x32_bf16 v[124:127], v[40:43], v[76:79], v[124:127]\n" \
  "s_waitcnt lgkmcnt(4)\n" \
  "v_mfma_f32_16x16x32_bf16 v[128:131], v[48:51], v[64:67], 0\n" \
  "buffer_load_dwordx4 a[4:7], v24, s[52:55], s74 offen offset:1024 sc1\n" \
  "v_mfma_f32_16x16x32_bf16 v[128:131], v[52:55], v[80:83], v[128:131]\n" \
  "ds_read_b64_tr_b16 v[40:41], v2 offset:512\n" \
  "v_mfma_f32_16x16x32_bf16 v[132:135], v[52:55], v[84:87], 0\n" \
  "buffer_load_dwordx4 a[8:11], v24, s[52:55], s74 offen offset:2048 sc1\n" \
  "v_mfma_f32_16x16x32_bf16 v[132:135], v[48:51], v[68:71], v[132:135]\n" \
  "ds_read_b64_tr_b16 v[42:43], v2 offset:2560\n" \
  "v_mfma_f32_16x16x32_bf16 v[136:139], v[48:51], v[72:75], 0\n" \
  "buffer_load_dwordx4 a[12:15], v24, s[52:55], s74 offen offset:3072 sc1\n" \
  "v_mfma_f32_16x16x32_bf16 v[136:139], v[52:55], v[88:91], v[136:139]\n" \
  "ds_read_b64_tr_b16 v[44:45], v2 offset:4608\n" \
  "v_mfma_f32_16x16x32_bf16 v[140:143], v[52:55], v[92:95], 0\n" \
  "ds_read_b64_tr_b16 v[46:47], v2 offset:6656\n" \
  "v_mfma_f32_16x16x32_bf16 v[140:143], v[48:51], v[76:79], v[140:143]\n" \
  "s_waitcnt lgkmcnt(4)\n" \
  "v_mfma_f32_16x16x32_bf16 v[144:147], v[56:59], v[64:67], 0\n" \
  "buffer_load_dwordx4 a[16:19], v25, s[52:55], s74 offen offset:0 sc1\n" \
  "v_mfma_f32_16x16x32_bf16 v[144:147], v[60:63], v[80:83], v[144:147]\n" \
  "ds_read_b64_tr_b16 v[48:49], v2 offset:8704\n" \
  "v_mfma_f32_16x16x32_bf16 v[148:151], v[60:63], v[84:87], 0\n" \
  "buffer_load_dwordx4 a[20:23], v25, s[52:55], s74 offen offset:1024 sc1\n" \
  "v_mfma_f32_16x16x32_bf16 v[148:151], v[56:59], v[68:71], v[148:151]\n" \
  "ds_read_b64_tr_b16 v[50:51], v2 offset:10752\n" \
  "v_mfma_f32_16x16x32_bf16 v[152:155], v[56:59], v[72:75], 0\n" \
  "buffer_load_dwordx4 a[24:27], v25, s[52:55], s74 offen offset:2048 sc1\n" \
  "v_mfma_f32_16x16x32_bf16 v[152:155], v[60:63], v[88:91], v[152:155]\n" \
  "ds_read_b64_tr_b16 v[52:53], v2 offset:12800\n" \
  "v_mfma_f32_16x16x32_bf16 v[156:159], v[60:63], v[92:95], 0\n" \
  "ds_read_b64_tr_b16 v[54:55], v2 offset:14848\n" \
  "v_mfma_f32_16x16x32_bf16 v[156:159], v[56:59], v[76:79], v[156:159]\n" \
  "s_waitcnt lgkmcnt(4)\n" \
  "v_mfma_f32_16x16x32_bf16 v[160:163], v[40:43], v[64:67], 0\n" \
  "buffer_load_dwordx4 a[28:31], v25, s[52:55], s74 offen offset:3072 sc1\n" \
  "v_mfma_f32_16x16x32_bf16 v[160:163], v[44:47], v[80:83], v[160:163]\n" \
  "ds_read_b64_tr_b16 v[56:57], v3 offset:512\n" \
  "s_mov_b32 s74, 16384\n" \
  "v_mfma_f32_16x16x32_bf16 v[164:167], v[44:47], v[84:87], 0\n" \
  "buffer_load_dwordx4 a[32:35], v24, s[52:55], s74 offen offset:0 sc1\n" \
  "v_mfma_f32_16x16x32_bf16 v[164:167], v[40:43], v[68:71], v[164:167]\n" \
  "ds_read_b64_tr_b16 v[58:59], v3 offset:2560\n" \
  "v_mfma_f32_16x16x32_bf16 v[168:171], v[40:43], v[72:75], 0\n" \
  "buffer_load_dwordx4 a[36:39], v24, s[52:55], s74 offen offset:1024 sc1\n" \
  "v_mfma_f32_16x16x32_bf16 v[168:171], v[44:47], v[88:91], v[168:171]\n" \
  "ds_read_b64_tr_b16 v[60:61], v3 offset:4608\n" \
  "v_mfma_f32_16x16x32_bf16 v[172:175], v[44:47], v[92:95], 0\n" \
  "ds_read_b64_tr_b16 v[62:63], v3 offset:6656\n" \
  "v_mfma_f32_16x16x32_bf16 v[172:175], v[40:43], v[76:79], v[172:175]\n" \
  "s_waitcnt lgkmcnt(4)\n" \
  "v_mfma_f32_16x16x32_bf16 v[176:179], v[48:51], v[64:67], 0\n" \
  "buffer_load_dwordx4 a[40:43], v24, s[52:55], s74 offen offset:2048 sc1\n" \
  "v_mfma_f32_16x16x32_bf16 v[176:179], v[52:55], v[80:83], v[176:179]\n" \
  "ds_read_b64_tr_b16 v[40:41], v3 offset:8704\n" \
  "v_mfma_f32_16x16x32_bf16 v[180:183], v[52:55], v[84:87], 0\n" \
  "buffer_load_dwordx4 a[44:47], v24, s[52:55], s74 offen offset:3072 sc1\n" \
  "v_mfma_f32_16x16x32_bf16 v[180:183], v[48:51], v[68:71], v[180:183]\n" \
  "ds_read_b64_tr_b16 v[42:43], v3 offset:10752\n" \
  "v_mfma_f32_16x16x32_bf16 v[184:187], v[48:51], v[72:75], 0\n" \
  "v_mfma_f32_16x16x32_bf16 v[184:187], v[52:55], v[88:91], v[184:187]\n" \
  "ds_read_b64_tr_b16 v[44:45], v3 offset:12800\n" \
  "v_mfma_f32_16x16x32_bf16 v[188:191], v[52:55], v[92:95], 0\n" \
  "ds_read_b64_tr_b16 v[46:47], v3 offset:14848\n" \
  "v_mfma_f32_16x16x32_bf16 v[188:191], v[48:51], v[76:79], v[188:191]\n" \
  "s_barrier\n" \
  "s_waitcnt lgkmcnt(4)\n" \
  "v_mfma_f32_16x16x32_bf16 v[192:195], v[56:59], v[64:67], 0\n" \
  "buffer_load_dwordx4 a[48:51], v25, s[52:55], s74 offen offset:0 sc1\n" \
  "v_mfma_f32_16x16x32_bf16 v[192:195], v[60:63], v[80:83], v[192:195]\n" \
  "ds_read_b64_tr_b16 v[48:49], v8 offset:36864\n" \
  "v_mfma_f32_16x16x32_bf16 v[196:199], v[60:63], v[84:87], 0\n" \
  "v_mfma_f32_16x16x32_bf16 v[196:199], v[56:59], v[68:71], v[196:199]\n" \
  "ds_read_b64_tr_b16 v[50:51], v8 offset:37376\n" \
  "v_mfma_f32_16x16x32_bf16 v[200:203], v[56:59], v[72:75], 0\n" \
  "v_mfma_f32_16x16x32_bf16 v[200:203], v[60:63], v[88:91], v[200:203]\n" \
  "ds_read_b64_tr_b16 v[52:53], v8 offset:38912\n" \
  "v_mfma_f32_16x16x32_bf16 v[204:207], v[60:63], v[92:95], 0\n" \
  "ds_read_b64_tr_b16 v[54:55], v8 offset:39424\n" \
  "v_mfma_f32_16x16x32_bf16 v[204:207], v[56:59], v[76:79], v[204:207]\n" \
  "s_waitcnt lgkmcnt(4)\n" \
  "v_mfma_f32_16x16x32_bf16 v[208:211], v[40:43], v[64:67], 0\n" \
  "buffer_load_dwordx4 a[52:55], v25, s[52:55], s74 offen offset:1024 sc1\n" \
  "v_mfma_f32_16x16x32_bf16 v[208:211], v[44:47], v[80:83], v[208:211]\n" \
  "ds_read_b64_tr_b16 v[56:57], v8 offset:37888\n" \
  "v_mfma_f32_16x16x32_bf16 v[212:215], v[44:47], v[84:87], 0\n" \
  "buffer_load_dwordx4 a[56:59], v25, s[52:55], s74 offen offset:2048 sc1\n" \
  "v_mfma_f32_16x16x32_bf16 v[212:215], v[40:43], v[68:71], v[212:215]\n" \
  "ds_read_b64_tr_b16 v[58:59], v8 offset:38400\n" \
  "v_mfma_f32_16x16x32_bf16 v[216:219], v[40:43], v[72:75], 0\n" \
  "v_mfma_f32_16x16x32_bf16 v[216:219], v[44:47], v[88:91], v[216:219]\n" \
  "ds_read_b64_tr_b16 v[60:61], v8 offset:39936\n" \
  "PG2_%=:\n" \
  "HOP2_%=:\n" \
  "s_cbranch_execz PG6_%=\n" \
  "v_mfma_f32_16x16x32_bf16 v[220:223], v[44:47], v[92:95], 0\n" \
  "ds_read_b64_tr_b16 v[62:63], v8 offset:40448\n" \
  "v_mfma_f32_16x16x32_bf16 v[220:223], v[40:43], v[76:79], v[220:223]\n" \
  "s_waitcnt vmcnt(7) lgkmcnt(4)\n" \
  "v_mfma_f32_16x16x32_bf16 v[224:227], v[48:51], a[0:3], v[224:227]\n" \
  "s_mov_b32 s73, 44570624\n" \
  "s_add_u32 m0, s72, 77824\n" \
  "v_mfma_f32_16x16x32_bf16 v[224:227], v[52:55], a[16:19], v[224:227]\n" \
  "ds_read_b64_tr_b16 v[40:41], v2 offset:20480\n" \
  "v_mfma_f32_16x16x32_bf16 v[228:231], v[52:55], a[20:23], v[228:231]\n" \
  "buffer_load_dwordx4 v14, s[60:63], s73 offen sc1 lds\n" \
  "v_mfma_f32_16x16x32_bf16 v[228:231], v[48:51], a[4:7], v[228:231]\n" \
  "ds_read_b64_tr_b16 v[42:43], v2 offset:22528\n" \
  "v_mfma_f32_16x16x32_bf16 v[232:235], v[48:51], a[8:11], v[232:235]\n" \
  "buffer_load_dwordx4 a[60:63], v25, s[52:55], s74 offen offset:3072 sc1\n" \
  "v_mfma_f32_16x16x32_bf16 v[232:235], v[52:55], a[24:27], v[232:235]\n" \
  "ds_read_b64_tr_b16 v[44:45], v2 offset:24576\n" \
  "v_mfma_f32_16x16x32_bf16 v[236:239], v[52:55], a[28:31], v[236:239]\n" \
  "ds_read_b64_tr_b16 v[46:47], v2 offset:26624\n" \
  "v_mfma_f32_16x16x32_bf16 v[236:239], v[48:51], a[12:15], v[236:239]\n" \
  "s_waitcnt lgkmcnt(4)\n" \
  "v_mfma_f32_16x16x32_bf16 v[240:243], v[56:59], a[0:3], v[240:243]\n" \
  "s_mov_b32 s73, 1769472\n" \
  "s_add_u32 m0, s72, 61440\n" \
  "v_mfma_f32_16x16x32_bf16 v[240:243], v[60:63], a[16:19], v[240:243]\n" \
  "ds_read_b64_tr_b16 v[48:49], v2 offset:28672\n" \
  "v_mfma_f32_16x16x32_bf16 v[244:247], v[60:63], a[20:23], v[244:247]\n" \
  "buffer_load_dwordx4 v12, s[36:39], s73 offen  lds\n" \
  "v_mfma_f32_16x16x32_bf16 v[244:247], v[56:59], a[4:7], v[244:247]\n" \
  "ds_read_b64_tr_b16 v[50:51], v2 offset:30720\n" \
  "s_mov_b32 s74, 24576\n" \
  "v_mfma_f32_16x16x32_bf16 v[248:251], v[56:59], a[8:11], v[248:251]\n" \
  "buffer_load_dwordx4 a[64:67], v24, s[52:55], s74 offen offset:0 sc1\n" \
  "v_mfma_f32_16x16x32_bf16 v[248:251], v[60:63], a[24:27], v[248:251]\n" \
  "ds_read_b64_tr_b16 v[52:53], v2 offset:32768\n" \
  "v_mfma_f32_16x16x32_bf16 v[252:255], v[60:63], a[28:31], v[252:255]\n" \
  "ds_read_b64_tr_b16 v[54:55], v2 offset:34816\n" \
  "v_mfma_f32_16x16x32_bf16 v[252:255], v[56:59], a[12:15], v[252:255]\n" \
  "s_waitcnt lgkmcnt(4)\n" \
  "v_mfma_f32_16x16x32_bf16 v[96:99], v[40:43], a[0:3], v[96:99]\n" \
  "buffer_load_dwordx4 a[68:71], v24, s[52:55], s74 offen offset:1024 sc1\n" \
  "v_mfma_f32_16x16x32_bf16 v[96:99], v[44:47], a[16:19], v[96:99]\n" \
  "ds_read_b64_tr_b16 v[56:57], v3 offset:20480\n" \
  "s_add_u32 m0, s72, 65536\n" \
  "v_mfma_f32_16x16x32_bf16 v[100:103], v[44:47], a[20:23], v[100:103]\n" \
  "buffer_load_dwordx4 v13, s[36:39], s73 offen  lds\n" \
  "v_mfma_f32_16x16x32_bf16 v[100:103], v[40:43], a[4:7], v[100:103]\n" \
  "ds_read_b64_tr_b16 v[58:59], v3 offset:22528\n" \
  "s_lshl_b32 s89, s79, 2\n" \
  "v_mfma_f32_16x16x32_bf16 v[104:107], v[40:43], a[8:11], v[104:107]\n" \
  "buffer_load_dwordx4 a[72:75], v24, s[52:55], s74 offen offset:2048 sc1\n" \
  "v_mfma_f32_16x16x32_bf16 v[104:107], v[44:47], a[24:27], v[104:107]\n" \
  "ds_read_b64_tr_b16 v[60:61], v3 offset:24576\n" \
  "s_add_u32 s89, s89, s80\n" \
  "v_mfma_f32_16x16x32_bf16 v[108:111], v[44:47], a[28:31], v[108:111]\n" \
  "ds_read_b64_tr_b16 v[62:63], v3 offset:26624\n" \
  "s_lshl_b32 s89, s89, 3\n" \
  "v_mfma_f32_16x16x32_bf16 v[108:111], v[40:43], a[12:15], v[108:111]\n" \
  "s_add_u32 s89, s89, s78\n" \
  "s_waitcnt lgkmcnt(4)\n" \
  "v_mfma_f32_16x16x32_bf16 v[112:115], v[48:51], a[0:3], v[112:115]\n" \
  "s_add_u32 m0, s72, 69632\n" \
  "s_mul_i32 s90, s89, 73728\n" \
  "v_mfma_f32_16x16x32_bf16 v[112:115], v[52:55], a[16:19], v[112:115]\n" \
  "ds_read_b64_tr_b16 v[40:41], v3 offset:28672\n" \
  "v_mfma_f32_16x16x32_bf16 v[116:119], v[52:55], a[20:23], v[116:119]\n" \
  "buffer_load_dwordx4 v12, s[40:43], s73 offen  lds\n" \
  "v_mfma_f32_16x16x32_bf16 v[116:119], v[48:51], a[4:7], v[116:119]\n" \
  "ds_read_b64_tr_b16 v[42:43], v3 offset:30720\n" \
  "v_mfma_f32_16x16x32_bf16 v[120:123], v[48:51], a[8:11], v[120:123]\n" \
  "buffer_load_dwordx4 a[76:79], v24, s[52:55], s74 offen offset:3072 sc1\n" \
  "v_mfma_f32_16x16x32_bf16 v[120:123], v[52:55], a[24:27], v[120:123]\n" \
  "ds_read_b64_tr_b16 v[44:45], v3 offset:32768\n" \
  "v_mfma_f32_16x16x32_bf16 v[124:127], v[52:55], a[28:31], v[124:127]\n" \
  "ds_read_b64_tr_b16 v[46:47], v3 offset:34816\n" \
  "v_mfma_f32_16x16x32_bf16 v[124:127], v[48:51], a[12:15], v[124:127]\n" \
  "s_waitcnt lgkmcnt(4)\n" \
  "v_mfma_f32_16x16x32_bf16 v[128:131], v[56:59], a[0:3], v[128:131]\n" \
  "s_add_u32 m0, s72, 73728\n" \
  "v_mfma_f32_16x16x32_bf16 v[128:131], v[60:63], a[16:19], v[128:131]\n" \
  "ds_read_b64_tr_b16 v[48:49], v2 offset:20992\n" \
  "v_mfma_f32_16x16x32_bf16 v[132:135], v[60:63], a[20:23], v[132:135]\n" \
  "buffer_load_dwordx4 v13, s[40:43], s73 offen  lds\n" \
  "v_mfma_f32_16x16x32_bf16 v[132:135], v[56:59], a[4:7], v[132:135]\n" \
  "ds_read_b64_tr_b16 v[50:51], v2 offset:23040\n" \
  "s_mov_b32 s73, 44572672\n" \
  "v_mfma_f32_16x16x32_bf16 v[136:139], v[56:59], a[8:11], v[136:139]\n" \
  "s_add_u32 m0, s72, 98304\n" \
  "v_mfma_f32_16x16x32_bf16 v[136:139], v[60:63], a[24:27], v[136:139]\n" \
  "ds_read_b64_tr_b16 v[52:53], v2 offset:25088\n" \
  "v_mfma_f32_16x16x32_bf16 v[140:143], v[60:63], a[28:31], v[140:143]\n" \
  "ds_read_b64_tr_b16 v[54:55], v2 offset:27136\n" \
  "v_mfma_f32_16x16x32_bf16 v[140:143], v[56:59], a[12:15], v[140:143]\n" \
  "s_waitcnt lgkmcnt(4)\n" \
  "v_mfma_f32_16x16x32_bf16 v[144:147], v[40:43], a[0:3], v[144:147]\n" \
  "buffer_load_dwordx4 v14, s[60:63], s73 offen sc1 lds\n" \
  "v_mfma_f32_16x16x32_bf16 v[144:147], v[44:47], a[16:19], v[144:147]\n" \
  "ds_read_b64_tr_b16 v[56:57], v2 offset:29184\n" \
  "v_mfma_f32_16x16x32_bf16 v[148:151], v[44:47], a[20:23], v[148:151]\n" \
  "buffer_load_dwordx4 a[80:83], v25, s[52:55], s74 offen offset:0 sc1\n" \
  "v_mfma_f32_16x16x32_bf16 v[148:151], v[40:43], a[4:7], v[148:151]\n" \
  "ds_read_b64_tr_b16 v[58:59], v2 offset:31232\n" \
  "v_mfma_f32_16x16x32_bf16 v[152:155], v[40:43], a[8:11], v[152:155]\n" \
  "buffer_load_dwordx4 a[84:87], v25, s[52:55], s74 offen offset:1024 sc1\n" \
  "v_mfma_f32_16x16x32_bf16 v[152:155], v[44:47], a[24:27], v[152:155]\n" \
  "ds_read_b64_tr_b16 v[60:61], v2 offset:33280\n" \
  "s_mov_b32 s73, 2359296\n" \
  "v_mfma_f32_16x16x32_bf16 v[156:159], v[44:47], a[28:31], v[156:159]\n" \
  "ds_read_b64_tr_b16 v[62:63], v2 offset:35328\n" \
  "s_add_u32 m0, s72, 81920\n" \
  "v_mfma_f32_16x16x32_bf16 v[156:159], v[40:43], a[12:15], v[156:159]\n" \
  "s_waitcnt lgkmcnt(4)\n" \
  "v_mfma_f32_16x16x32_bf16 v[160:163], v[48:51], a[0:3], v[160:163]\n" \
  "buffer_load_dwordx4 v12, s[36:39], s73 offen  lds\n" \
  "v_mfma_f32_16x16x32_bf16 v[160:163], v[52:55], a[16:19], v[160:163]\n" \
  "ds_read_b64_tr_b16 v[40:41], v3 offset:20992\n" \
  "v_mfma_f32_16x16x32_bf16 v[164:167], v[52:55], a[20:23], v[164:167]\n" \
  "buffer_load_dwordx4 a[88:91], v25, s[52:55], s74 offen offset:2048 sc1\n" \
  "v_mfma_f32_16x16x32_bf16 v[164:167], v[48:51], a[4:7], v[164:167]\n" \
  "ds_read_b64_tr_b16 v[42:43], v3 offset:23040\n" \
  "v_mfma_f32_16x16x32_bf16 v[168:171], v[48:51], a[8:11], v[168:171]\n" \
  "buffer_load_dwordx4 a[92:95], v25, s[52:55], s74 offen offset:3072 sc1\n" \
  "v_mfma_f32_16x16x32_bf16 v[168:171], v[52:55], a[24:27], v[168:171]\n" \
  "ds_read_b64_tr_b16 v[44:45], v3 offset:25088\n" \
  "v_mfma_f32_16x16x32_bf16 v[172:175], v[52:55], a[28:31], v[172:175]\n" \
  "ds_read_b64_tr_b16 v[46:47], v3 offset:27136\n" \
  "v_mfma_f32_16x16x32_bf16 v[172:175], v[48:51], a[12:15], v[172:175]\n" \
  "s_waitcnt lgkmcnt(4)\n" \
  "v_mfma_f32_16x16x32_bf16 v[176:179], v[56:59], a[0:3], v[176:179]\n" \
  "s_add_u32 m0, s72, 86016\n" \
  "v_mfma_f32_16x16x32_bf16 v[176:179], v[60:63], a[16:19], v[176:179]\n" \
  "ds_read_b64_tr_b16 v[48:49], v3 offset:29184\n" \
  "v_mfma_f32_16x16x32_bf16 v[180:183], v[60:63], a[20:23], v[180:183]\n" \
  "buffer_load_dwordx4 v13, s[36:39], s73 offen  lds\n" \
  "v_mfma_f32_16x16x32_bf16 v[180:183], v[56:59], a[4:7], v[180:183]\n" \
  "ds_read_b64_tr_b16 v[50:51], v3 offset:31232\n" \
  "s_mov_b32 s74, 32768\n" \
  "v_mfma_f32_16x16x32_bf16 v[184:187], v[56:59], a[8:11], v[184:187]\n" \
  "buffer_load_dwordx4 a[96:99], v24, s[52:55], s74 offen offset:0 sc1\n" \
  "v_mfma_f32_16x16x32_bf16 v[184:187], v[60:63], a[24:27], v[184:187]\n" \
  "ds_read_b64_tr_b16 v[52:53], v3 offset:33280\n" \
  "v_mfma_f32_16x16x32_bf16 v[188:191], v[60:63], a[28:31], v[188:191]\n" \
  "ds_read_b64_tr_b16 v[54:55], v3 offset:35328\n" \
  "v_mfma_f32_16x16x32_bf16 v[188:191], v[56:59], a[12:15], v[188:191]\n" \
  "s_barrier\n" \
  "s_waitcnt lgkmcnt(4)\n" \
  "v_mfma_f32_16x16x32_bf16 v[192:195], v[40:43], a[0:3], v[192:195]\n" \
  "buffer_load_dwordx4 a[100:103], v24, s[52:55], s74 offen offset:1024 sc1\n" \
  "v_mfma_f32_16x16x32_bf16 v[192:195], v[44:47], a[16:19], v[192:195]\n" \
  "ds_read_b64_tr_b16 v[56:57], v8 offset:57344\n" \
  "s_add_u32 m0, s72, 90112\n" \
  "v_mfma_f32_16x16x32_bf16 v[196:199], v[44:47], a[20:23], v[196:199]\n" \
  "buffer_load_dwordx4 v12, s[40:43], s73 offen  lds\n" \
  "v_mfma_f32_16x16x32_bf16 v[196:199], v[40:43], a[4:7], v[196:199]\n" \
  "ds_read_b64_tr_b16 v[58:59], v8 offset:57856\n" \
  "v_mfma_f32_16x16x32_bf16 v[200:203], v[40:43], a[8:11], v[200:203]\n" \
  "buffer_load_dwordx4 a[104:107], v24, s[52:55], s74 offen offset:2048 sc1\n" \
  "v_mfma_f32_16x16x32_bf16 v[200:203], v[44:47], a[24:27], v[200:203]\n" \
  "ds_read_b64_tr_b16 v[60:61], v8 offset:59392\n" \
  "v_mfma_f32_16x16x32_bf16 v[204:207], v[44:47], a[28:31], v[204:207]\n" \
  "ds_read_b64_tr_b16 v[62:63], v8 offset:59904\n" \
  "v_mfma_f32_16x16x32_bf16 v[204:207], v[40:43], a[12:15], v[204:207]\n" \
  "s_waitcnt lgkmcnt(4)\n" \
  "v_mfma_f32_16x16x32_bf16 v[208:211], v[48:51], a[0:3], v[208:211]\n" \
  "buffer_load_dwordx4 a[108:111], v24, s[52:55], s74 offen offset:3072 sc1\n" \
  "v_mfma_f32_16x16x32_bf16 v[208:211], v[52:55], a[16:19], v[208:211]\n" \
  "ds_read_b64_tr_b16 v[40:41], v8 offset:58368\n" \
  "v_mfma_f32_16x16x32_bf16 v[212:215], v[52:55], a[20:23], v[212:215]\n" \
  "s_add_u32 s56, s60, 6815744\n" \
  "s_addc_u32 s57, s61, 0\n" \
  "v_mfma_f32_16x16x32_bf16 v[212:215], v[48:51], a[4:7], v[212:215]\n" \
  "ds_read_b64_tr_b16 v[42:43], v8 offset:58880\n" \
  "v_mfma_f32_16x16x32_bf16 v[216:219], v[48:51], a[8:11], v[216:219]\n" \
  "s_add_u32 s56, s56, s90\n" \
  "s_addc_u32 s57, s57, 0\n" \
  "v_mfma_f32_16x16x32_bf16 v[216:219], v[52:55], a[24:27], v[216:219]\n" \
  "ds_read_b64_tr_b16 v[44:45], v8 offset:60416\n" \
  "s_mov_b32 s58, 73728\n" \
  "v_mfma_f32_16x16x32_bf16 v[220:223], v[52:55], a[28:31], v[220:223]\n" \
  "ds_read_b64_tr_b16 v[46:47], v8 offset:60928\n" \
  "s_mov_b32 s59, 0x20000\n" \
  "v_mfma_f32_16x16x32_bf16 v[220:223], v[48:51], a[12:15], v[220:223]\n" \
  "s_lshl_b32 s86, s89, 11\n" \
  "s_waitcnt vmcnt(20) lgkmcnt(4)\n" \
  "v_mfma_f32_16x16x32_bf16 v[224:227], v[56:59], a[32:35], v[224:227]\n" \
  "s_add_u32 m0, s72, 94208\n" \
  "s_add_u32 s86, s86, 524288\n" \
  "v_mfma_f32_16x16x32_bf16 v[224:227], v[60:63], a[48:51], v[224:227]\n" \
  "ds_read_b64_tr_b16 v[48:49], v2 offset:40960\n" \
  "s_lshl_b32 s83, s77, 14\n" \
  "v_mfma_f32_16x16x32_bf16 v[228:231], v[60:63], a[52:55], v[228:231]\n" \
  "buffer_load_dwordx4 v13, s[40:43], s73 offen  lds\n" \
  "v_mfma_f32_16x16x32_bf16 v[228:231], v[56:59], a[36:39], v[228:231]\n" \
  "ds_read_b64_tr_b16 v[50:51], v2 offset:43008\n" \
  "s_xor_b32 s4, s83, 0\n" \
  "v_mfma_f32_16x16x32_bf16 v[232:235], v[56:59], a[40:43], v[232:235]\n" \
  "buffer_load_dwordx4 a[112:115], v25, s[52:55], s74 offen offset:0 sc1\n" \
  "v_mfma_f32_16x16x32_bf16 v[232:235], v[60:63], a[56:59], v[232:235]\n" \
  "ds_read_b64_tr_b16 v[52:53], v2 offset:45056\n" \
  "s_mul_i32 s13, s4, 9\n" \
  "v_mfma_f32_16x16x32_bf16 v[236:239], v[60:63], a[60:63], v[236:239]\n" \
  "ds_read_b64_tr_b16 v[54:55], v2 offset:47104\n" \
  "s_mov_b32 s5, 0\n" \
  "v_mfma_f32_16x16x32_bf16 v[236:239], v[56:59], a[44:47], v[236:239]\n" \
  "s_waitcnt lgkmcnt(4)\n" \
  "v_mfma_f32_16x16x32_bf16 v[240:243], v[40:43], a[32:35], v[240:243]\n" \
  "buffer_load_dwordx4 a[116:119], v25, s[52:55], s74 offen offset:1024 sc1\n" \
  "v_mfma_f32_16x16x32_bf16 v[240:243], v[44:47], a[48:51], v[240:243]\n" \
  "ds_read_b64_tr_b16 v[56:57], v2 offset:49152\n" \
  "v_mfma_f32_16x16x32_bf16 v[244:247], v[44:47], a[52:55], v[244:247]\n" \
  "buffer_load_dwordx4 a[120:123], v25, s[52:55], s74 offen offset:2048 sc1\n" \
  "v_mfma_f32_16x16x32_bf16 v[244:247], v[40:43], a[36:39], v[244:247]\n" \
  "ds_read_b64_tr_b16 v[58:59], v2 offset:51200\n" \
  "v_mfma_f32_16x16x32_bf16 v[248:251], v[40:43], a[40:43], v[248:251]\n" \
  "s_cmp_gt_u32 s77, 0\n" \
  "s_cselect_b32 s5, s5, 16384\n" \
  "v_mfma_f32_16x16x32_bf16 v[248:251], v[44:47], a[56:59], v[248:251]\n" \
  "ds_read_b64_tr_b16 v[60:61], v2 offset:53248\n" \
  "s_mul_i32 s14, s5, 9\n" \
  "v_mfma_f32_16x16x32_bf16 v[252:255], v[44:47], a[60:63], v[252:255]\n" \
  "ds_read_b64_tr_b16 v[62:63], v2 offset:55296\n" \
  "s_mov_b32 s6, 16384\n" \
  "v_mfma_f32_16x16x32_bf16 v[252:255], v[40:43], a[44:47], v[252:255]\n" \
  "s_waitcnt lgkmcnt(4)\n" \
  "v_mfma_f32_16x16x32_bf16 v[96:99], v[48:51], a[32:35], v[96:99]\n" \
  "buffer_load_dwordx4 a[124:127], v25, s[52:55], s74 offen offset:3072 sc1\n" \
  "v_mfma_f32_16x16x32_bf16 v[96:99], v[52:55], a[48:51], v[96:99]\n" \
  "ds_read_b64_tr_b16 v[40:41], v3 offset:40960\n" \
  "v_mfma_f32_16x16x32_bf16 v[100:103], v[52:55], a[52:55], v[100:103]\n" \
  "s_cmp_gt_u32 s77, 1\n" \
  "s_cselect_b32 s6, s6, 32768\n" \
  "v_mfma_f32_16x16x32_bf16 v[100:103], v[48:51], a[36:39], v[100:103]\n" \
  "ds_read_b64_tr_b16 v[42:43], v3 offset:43008\n" \
  "s_mul_i32 s15, s6, 9\n" \
  "v_mfma_f32_16x16x32_bf16 v[104:107], v[48:51], a[40:43], v[104:107]\n" \
  "s_mov_b32 s7, 32768\n" \
  "v_mfma_f32_16x16x32_bf16 v[104:107], v[52:55], a[56:59], v[104:107]\n" \
  "ds_read_b64_tr_b16 v[44:45], v3 offset:45056\n" \
  "v_mfma_f32_16x16x32_bf16 v[108:111], v[52:55], a[60:63], v[108:111]\n" \
  "ds_read_b64_tr_b16 v[46:47], v3 offset:47104\n" \
  "v_mfma_f32_16x16x32_bf16 v[108:111], v[48:51], a[44:47], v[108:111]\n" \
  "s_waitcnt lgkmcnt(4)\n" \
  "v_mfma_f32_16x16x32_bf16 v[112:115], v[56:59], a[32:35], v[112:115]\n" \
  "s_mov_b32 s74, 40960\n" \
  "v_mfma_f32_16x16x32_bf16 v[112:115], v[60:63], a[48:51], v[112:115]\n" \
  "ds_read_b64_tr_b16 v[48:49], v3 offset:49152\n" \
  "v_mfma_f32_16x16x32_bf16 v[116:119], v[60:63], a[52:55], v[116:119]\n" \
  "buffer_load_dwordx4 a[128:131], v24, s[52:55], s74 offen offset:0 sc1\n" \
  "v_mfma_f32_16x16x32_bf16 v[116:119], v[56:59], a[36:39], v[116:119]\n" \
  "ds_read_b64_tr_b16 v[50:51], v3 offset:51200\n" \
  "v_mfma_f32_16x16x32_bf16 v[120:123], v[56:59], a[40:43], v[120:123]\n" \
  "buffer_load_dwordx4 a[132:135], v24, s[52:55], s74 offen offset:1024 sc1\n" \
  "v_mfma_f32_16x16x32_bf16 v[120:123], v[60:63], a[56:59], v[120:123]\n" \
  "ds_read_b64_tr_b16 v[52:53], v3 offset:53248\n" \
  "v_mfma_f32_16x16x32_bf16 v[124:127], v[60:63], a[60:63], v[124:127]\n" \
  "ds_read_b64_tr_b16 v[54:55], v3 offset:55296\n" \
  "v_mfma_f32_16x16x32_bf16 v[124:127], v[56:59], a[44:47], v[124:127]\n" \
  "s_waitcnt lgkmcnt(4)\n" \
  "v_mfma_f32_16x16x32_bf16 v[128:131], v[40:43], a[32:35], v[128:131]\n" \
  "s_mov_b32 s73, 44574720\n" \
  "s_add_u32 m0, s72, 118784\n" \
  "v_mfma_f32_16x16x32_bf16 v[128:131], v[44:47], a[48:51], v[128:131]\n" \
  "ds_read_b64_tr_b16 v[56:57], v2 offset:41472\n" \
  "v_mfma_f32_16x16x32_bf16 v[132:135], v[44:47], a[52:55], v[132:135]\n" \
  "buffer_load_dwordx4 v14, s[60:63], s73 offen sc1 lds\n" \
  "v_mfma_f32_16x16x32_bf16 v[132:135], v[40:43], a[36:39], v[132:135]\n" \
  "ds_read_b64_tr_b16 v[58:59], v2 offset:43520\n" \
  "v_mfma_f32_16x16x32_bf16 v[136:139], v[40:43], a[40:43], v[136:139]\n" \
  "buffer_load_dwordx4 a[136:139], v24, s[52:55], s74 offen offset:2048 sc1\n" \
  "v_mfma_f32_16x16x32_bf16 v[136:139], v[44:47], a[56:59], v[136:139]\n" \
  "ds_read_b64_tr_b16 v[60:61], v2 offset:45568\n" \
  "v_mfma_f32_16x16x32_bf16 v[140:143], v[44:47], a[60:63], v[140:143]\n" \
  "ds_read_b64_tr_b16 v[62:63], v2 offset:47616\n" \
  "v_mfma_f32_16x16x32_bf16 v[140:143], v[40:43], a[44:47], v[140:143]\n" \
  "s_waitcnt lgkmcnt(4)\n" \
  "v_mfma_f32_16x16x32_bf16 v[144:147], v[48:51], a[32:35], v[144:147]\n" \
  "s_mov_b32 s73, 2949120\n" \
  "s_add_u32 m0, s72, 102400\n" \
  "v_mfma_f32_16x16x32_bf16 v[144:147], v[52:55], a[48:51], v[144:147]\n" \
  "ds_read_b64_tr_b16 v[40:41], v2 offset:49664\n" \
  "v_mfma_f32_16x16x32_bf16 v[148:151], v[52:55], a[52:55], v[148:151]\n" \
  "buffer_load_dwordx4 v12, s[36:39], s73 offen  lds\n" \
  "v_mfma_f32_16x16x32_bf16 v[148:151], v[48:51], a[36:39], v[148:151]\n" \
  "ds_read_b64_tr_b16 v[42:43], v2 offset:51712\n" \
  "v_mfma_f32_16x16x32_bf16 v[152:155], v[48:51], a[40:43], v[152:155]\n" \
  "buffer_load_dwordx4 a[140:143], v24, s[52:55], s74 offen offset:3072 sc1\n" \
  "v_mfma_f32_16x16x32_bf16 v[152:155], v[52:55], a[56:59], v[152:155]\n" \
  "ds_read_b64_tr_b16 v[44:45], v2 offset:53760\n" \
  "v_mfma_f32_16x16x32_bf16 v[156:159], v[52:55], a[60:63], v[156:159]\n" \
  "ds_read_b64_tr_b16 v[46:47], v2 offset:55808\n" \
  "v_mfma_f32_16x16x32_bf16 v[156:159], v[48:51], a[44:47], v[156:159]\n" \
  "s_waitcnt lgkmcnt(4)\n" \
  "v_mfma_f32_16x16x32_bf16 v[160:163], v[56:59], a[32:35], v[160:163]\n" \
  "buffer_load_dwordx4 a[144:147], v25, s[52:55], s74 offen offset:0 sc1\n" \
  "v_mfma_f32_16x16x32_bf16 v[160:163], v[60:63], a[48:51], v[160:163]\n" \
  "ds_read_b64_tr_b16 v[48:49], v3 offset:41472\n" \
  "v_mfma_f32_16x16x32_bf16 v[164:167], v[60:63], a[52:55], v[164:167]\n" \
  "buffer_load_dwordx4 a[148:151], v25, s[52:55], s74 offen offset:1024 sc1\n" \
  "v_mfma_f32_16x16x32_bf16 v[164:167], v[56:59], a[36:39], v[164:167]\n" \
  "ds_read_b64_tr_b16 v[50:51], v3 offset:43520\n" \
  "v_mfma_f32_16x16x32_bf16 v[168:171], v[56:59], a[40:43], v[168:171]\n" \
  "s_cmp_gt_u32 s77, 2\n" \
  "s_cselect_b32 s7, s7, 49152\n" \
  "v_mfma_f32_16x16x32_bf16 v[168:171], v[60:63], a[56:59], v[168:171]\n" \
  "ds_read_b64_tr_b16 v[52:53], v3 offset:45568\n" \
  "s_mul_i32 s16, s7, 9\n" \
  "v_mfma_f32_16x16x32_bf16 v[172:175], v[60:63], a[60:63], v[172:175]\n" \
  "ds_read_b64_tr_b16 v[54:55], v3 offset:47616\n" \
  "s_mov_b32 s8, 65536\n" \
  "v_mfma_f32_16x16x32_bf16 v[172:175], v[56:59], a[44:47], v[172:175]\n" \
  "s_mul_i32 s17, s8, 9\n" \
  "s_waitcnt lgkmcnt(4)\n" \
  "v_mfma_f32_16x16x32_bf16 v[176:179], v[40:43], a[32:35], v[176:179]\n" \
  "s_add_u32 m0, s72, 106496\n" \
  "s_xor_b32 s9, s83, 8192\n" \
  "v_mfma_f32_16x16x32_bf16 v[176:179], v[44:47], a[48:51], v[176:179]\n" \
  "ds_read_b64_tr_b16 v[56:57], v3 offset:49664\n" \
  "s_mul_i32 s18, s9, 9\n" \
  "v_mfma_f32_16x16x32_bf16 v[180:183], v[44:47], a[52:55], v[180:183]\n" \
  "buffer_load_dwordx4 v13, s[36:39], s73 offen  lds\n" \
  "v_mfma_f32_16x16x32_bf16 v[180:183], v[40:43], a[36:39], v[180:183]\n" \
  "ds_read_b64_tr_b16 v[58:59], v3 offset:51712\n" \
  "s_mov_b32 s10, 8192\n" \
  "v_mfma_f32_16x16x32_bf16 v[184:187], v[40:43], a[40:43], v[184:187]\n" \
  "buffer_load_dwordx4 a[152:155], v25, s[52:55], s74 offen offset:2048 sc1\n" \
  "v_mfma_f32_16x16x32_bf16 v[184:187], v[44:47], a[56:59], v[184:187]\n" \
  "ds_read_b64_tr_b16 v[60:61], v3 offset:53760\n" \
  "v_mfma_f32_16x16x32_bf16 v[188:191], v[44:47], a[60:63], v[188:191]\n" \
  "ds_read_b64_tr_b16 v[62:63], v3 offset:55808\n" \
  "v_mfma_f32_16x16x32_bf16 v[188:191], v[40:43], a[44:47], v[188:191]\n" \
  "s_waitcnt vmcnt(27)\n" \
  "s_barrier\n" \
  "s_waitcnt lgkmcnt(4)\n" \
  "v_mfma_f32_16x16x32_bf16 v[192:195], v[48:51], a[32:35], v[192:195]\n" \
  "s_add_u32 m0, s72, 110592\n" \
  "v_mfma_f32_16x16x32_bf16 v[192:195], v[52:55], a[48:51], v[192:195]\n" \
  "ds_read_b64_tr_b16 v[40:41], v9 offset:16384\n" \
  "v_mfma_f32_16x16x32_bf16 v[196:199], v[52:55], a[52:55], v[196:199]\n" \
  "buffer_load_dwordx4 v12, s[40:43], s73 offen  lds\n" \
  "v_mfma_f32_16x16x32_bf16 v[196:199], v[48:51], a[36:39], v[196:199]\n" \
  "ds_read_b64_tr_b16 v[42:43], v9 offset:16896\n" \
  "v_mfma_f32_16x16x32_bf16 v[200:203], v[48:51], a[40:43], v[200:203]\n" \
  "buffer_load_dwordx4 a[156:159], v25, s[52:55], s74 offen offset:3072 sc1\n" \
  "v_mfma_f32_16x16x32_bf16 v[200:203], v[52:55], a[56:59], v[200:203]\n" \
  "ds_read_b64_tr_b16 v[44:45], v9 offset:18432\n" \
  "v_mfma_f32_16x16x32_bf16 v[204:207], v[52:55], a[60:63], v[204:207]\n" \
  "ds_read_b64_tr_b16 v[46:47], v9 offset:18944\n" \
  "v_mfma_f32_16x16x32_bf16 v[204:207], v[48:51], a[44:47], v[204:207]\n" \
  "s_waitcnt lgkmcnt(4)\n" \
  "v_mfma_f32_16x16x32_bf16 v[208:211], v[56:59], a[32:35], v[208:211]\n" \
  "s_mov_b32 s74, 49152\n" \
  "v_mfma_f32_16x16x32_bf16 v[208:211], v[60:63], a[48:51], v[208:211]\n" \
  "ds_read_b64_tr_b16 v[48:49], v9 offset:17408\n" \
  "v_mfma_f32_16x16x32_bf16 v[212:215], v[60:63], a[52:55], v[212:215]\n" \
  "buffer_load_dwordx4 a[160:163], v24, s[52:55], s74 offen offset:0 sc1\n" \
  "v_mfma_f32_16x16x32_bf16 v[212:215], v[56:59], a[36:39], v[212:215]\n" \
  "ds_read_b64_tr_b16 v[50:51], v9 offset:17920\n" \
  "v_mfma_f32_16x16x32_bf16 v[216:219], v[56:59], a[40:43], v[216:219]\n" \
  "buffer_load_dwordx4 a[164:167], v24, s[52:55], s74 offen offset:1024 sc1\n" \
  "v_mfma_f32_16x16x32_bf16 v[216:219], v[60:63], a[56:59], v[216:219]\n" \
  "ds_read_b64_tr_b16 v[52:53], v9 offset:19456\n" \
  "v_mfma_f32_16x16x32_bf16 v[220:223], v[60:63], a[60:63], v[220:223]\n" \
  "ds_read_b64_tr_b16 v[54:55], v9 offset:19968\n" \
  "v_mfma_f32_16x16x32_bf16 v[220:223], v[56:59], a[44:47], v[220:223]\n" \
  "s_waitcnt vmcnt(25) lgkmcnt(4)\n" \
  "v_mfma_f32_16x16x32_bf16 v[224:227], v[40:43], a[64:67], v[224:227]\n" \
  "s_add_u32 m0, s72, 114688\n" \
  "v_mfma_f32_16x16x32_bf16 v[224:227], v[44:47], a[80:83], v[224:227]\n" \
  "ds_read_b64_tr_b16 v[56:57], v4 offset:0\n" \
  "v_mfma_f32_16x16x32_bf16 v[228:231], v[44:47], a[84:87], v[228:231]\n" \
  "buffer_load_dwordx4 v13, s[40:43], s73 offen  lds\n" \
  "v_mfma_f32_16x16x32_bf16 v[228:231], v[40:43], a[68:71], v[228:231]\n" \
  "ds_read_b64_tr_b16 v[58:59], v4 offset:2048\n" \
  "v_mfma_f32_16x16x32_bf16 v[232:235], v[40:43], a[72:75], v[232:235]\n" \
  "buffer_load_dwordx4 a[168:171], v24, s[52:55], s74 offen offset:2048 sc1\n" \
  "v_mfma_f32_16x16x32_bf16 v[232:235], v[44:47], a[88:91], v[232:235]\n" \
  "ds_read_b64_tr_b16 v[60:61], v4 offset:4096\n" \
  "v_mfma_f32_16x16x32_bf16 v[236:239], v[44:47], a[92:95], v[236:239]\n" \
  "ds_read_b64_tr_b16 v[62:63], v4 offset:6144\n" \
  "v_mfma_f32_16x16x32_bf16 v[236:239], v[40:43], a[76:79], v[236:239]\n" \
  "s_waitcnt lgkmcnt(4)\n" \
  "v_mfma_f32_16x16x32_bf16 v[240:243], v[48:51], a[64:67], v[240:243]\n" \
  "buffer_load_dwordx4 a[172:175], v24, s[52:55], s74 offen offset:3072 sc1\n" \
  "v_mfma_f32_16x16x32_bf16 v[240:243], v[52:55], a[80:83], v[240:243]\n" \
  "ds_read_b64_tr_b16 v[40:41], v4 offset:8192\n" \
  "v_mfma_f32_16x16x32_bf16 v[244:247], v[52:55], a[84:87], v[244:247]\n" \
  "s_cmp_gt_u32 s77, 0\n" \
  "s_cselect_b32 s10, s10, 24576\n" \
  "v_mfma_f32_16x16x32_bf16 v[244:247], v[48:51], a[68:71], v[244:247]\n" \
  "ds_read_b64_tr_b16 v[42:43], v4 offset:10240\n" \
  "s_mul_i32 s19, s10, 9\n" \
  "v_mfma_f32_16x16x32_bf16 v[248:251], v[48:51], a[72:75], v[248:251]\n" \
  "s_mov_b32 s11, 24576\n" \
  "v_mfma_f32_16x16x32_bf16 v[248:251], v[52:55], a[88:91], v[248:251]\n" \
  "ds_read_b64_tr_b16 v[44:45], v4 offset:12288\n" \
  "v_mfma_f32_16x16x32_bf16 v[252:255], v[52:55], a[92:95], v[252:255]\n" \
  "ds_read_b64_tr_b16 v[46:47], v4 offset:14336\n" \
  "v_mfma_f32_16x16x32_bf16 v[252:255], v[48:51], a[76:79], v[252:255]\n" \
  "s_waitcnt lgkmcnt(4)\n" \
  "v_mfma_f32_16x16x32_bf16 v[96:99], v[56:59], a[64:67], v[96:99]\n" \
  "buffer_load_dwordx4 a[176:179], v25, s[52:55], s74 offen offset:0 sc1\n" \
  "v_mfma_f32_16x16x32_bf16 v[96:99], v[60:63], a[80:83], v[96:99]\n" \
  "ds_read_b64_tr_b16 v[48:49], v5 offset:0\n" \
  "v_mfma_f32_16x16x32_bf16 v[100:103], v[60:63], a[84:87], v[100:103]\n" \
  "buffer_load_dwordx4 a[180:183], v25, s[52:55], s74 offen offset:1024 sc1\n" \
  "v_mfma_f32_16x16x32_bf16 v[100:103], v[56:59], a[68:71], v[100:103]\n" \
  "ds_read_b64_tr_b16 v[50:51], v5 offset:2048\n" \
  "v_mfma_f32_16x16x32_bf16 v[104:107], v[56:59], a[72:75], v[104:107]\n" \
  "s_cmp_gt_u32 s77, 1\n" \
  "s_cselect_b32 s11, s11, 40960\n" \
  "v_mfma_f32_16x16x32_bf16 v[104:107], v[60:63], a[88:91], v[104:107]\n" \
  "ds_read_b64_tr_b16 v[52:53], v5 offset:4096\n" \
  "s_mul_i32 s20, s11, 9\n" \
  "v_mfma_f32_16x16x32_bf16 v[108:111], v[60:63], a[92:95], v[108:111]\n" \
  "ds_read_b64_tr_b16 v[54:55], v5 offset:6144\n" \
  "s_mov_b32 s12, 40960\n" \
  "v_mfma_f32_16x16x32_bf16 v[108:111], v[56:59], a[76:79], v[108:111]\n" \
  "s_waitcnt lgkmcnt(4)\n" \
  "v_mfma_f32_16x16x32_bf16 v[112:115], v[40:43], a[64:67], v[112:115]\n" \
  "buffer_load_dwordx4 a[184:187], v25, s[52:55], s74 offen offset:2048 sc1\n" \
  "v_mfma_f32_16x16x32_bf16 v[112:115], v[44:47], a[80:83], v[112:115]\n" \
  "ds_read_b64_tr_b16 v[56:57], v5 offset:8192\n" \
  "v_mfma_f32_16x16x32_bf16 v[116:119], v[44:47], a[84:87], v[116:119]\n" \
  "s_cmp_gt_u32 s77, 2\n" \
  "s_cselect_b32 s12, s12, 57344\n" \
  "v_mfma_f32_16x16x32_bf16 v[116:119], v[40:43], a[68:71], v[116:119]\n" \
  "ds_read_b64_tr_b16 v[58:59], v5 offset:10240\n" \
  "s_mul_i32 s21, s12, 9\n" \
  "v_mfma_f32_16x16x32_bf16 v[120:123], v[40:43], a[72:75], v[120:123]\n" \
  "s_mov_b32 s76, 69632\n" \
  "v_mfma_f32_16x16x32_bf16 v[120:123], v[44:47], a[88:91], v[120:123]\n" \
  "ds_read_b64_tr_b16 v[60:61], v5 offset:12288\n" \
  "v_mfma_f32_16x16x32_bf16 v[124:127], v[44:47], a[92:95], v[124:127]\n" \
  "ds_read_b64_tr_b16 v[62:63], v5 offset:14336\n" \
  "v_mfma_f32_16x16x32_bf16 v[124:127], v[40:43], a[76:79], v[124:127]\n" \
  "s_waitcnt lgkmcnt(4)\n" \
  "v_mfma_f32_16x16x32_bf16 v[128:131], v[48:51], a[64:67], v[128:131]\n" \
  "s_mov_b32 s73, 44576768\n" \
  "s_add_u32 m0, s72, 139264\n" \
  "v_mfma_f32_16x16x32_bf16 v[128:131], v[52:55], a[80:83], v[128:131]\n" \
  "ds_read_b64_tr_b16 v[40:41], v4 offset:512\n" \
  "v_mfma_f32_16x16x32_bf16 v[132:135], v[52:55], a[84:87], v[132:135]\n" \
  "buffer_load_dwordx4 v14, s[60:63], s73 offen sc1 lds\n" \
  "v_mfma_f32_16x16x32_bf16 v[132:135], v[48:51], a[68:71], v[132:135]\n" \
  "ds_read_b64_tr_b16 v[42:43], v4 offset:2560\n" \
  "v_mfma_f32_16x16x32_bf16 v[136:139], v[48:51], a[72:75], v[136:139]\n" \
  "buffer_load_dwordx4 a[188:191], v25, s[52:55], s74 offen offset:3072 sc1\n" \
  "v_mfma_f32_16x16x32_bf16 v[136:139], v[52:55], a[88:91], v[136:139]\n" \
  "ds_read_b64_tr_b16 v[44:45], v4 offset:4608\n" \
  "v_mfma_f32_16x16x32_bf16 v[140:143], v[52:55], a[92:95], v[140:143]\n" \
  "ds_read_b64_tr_b16 v[46:47], v4 offset:6656\n" \
  "v_mfma_f32_16x16x32_bf16 v[140:143], v[48:51], a[76:79], v[140:143]\n" \
  "s_waitcnt lgkmcnt(4)\n" \
  "v_mfma_f32_16x16x32_bf16 v[144:147], v[56:59], a[64:67], v[144:147]\n" \
  "s_mov_b32 s73, 3538944\n" \
  "s_add_u32 m0, s72, 122880\n" \
  "v_mfma_f32_16x16x32_bf16 v[144:147], v[60:63], a[80:83], v[144:147]\n" \
  "ds_read_b64_tr_b16 v[48:49], v4 offset:8704\n" \
  "v_mfma_f32_16x16x32_bf16 v[148:151], v[60:63], a[84:87], v[148:151]\n" \
  "buffer_load_dwordx4 v12, s[36:39], s73 offen  lds\n" \
  "v_mfma_f32_16x16x32_bf16 v[148:151], v[56:59], a[68:71], v[148:151]\n" \
  "ds_read_b64_tr_b16 v[50:51], v4 offset:10752\n" \
  "s_mov_b32 s74, 57344\n" \
  "v_mfma_f32_16x16x32_bf16 v[152:155], v[56:59], a[72:75], v[152:155]\n" \
  "buffer_load_dwordx4 a[192:195], v24, s[52:55], s74 offen offset:0 sc1\n" \
  "v_mfma_f32_16x16x32_bf16 v[152:155], v[60:63], a[88:91], v[152:155]\n" \
  "ds_read_b64_tr_b16 v[52:53], v4 offset:12800\n" \
  "v_mfma_f32_16x16x32_bf16 v[156:159], v[60:63], a[92:95], v[156:159]\n" \
  "ds_read_b64_tr_b16 v[54:55], v4 offset:14848\n" \
  "v_mfma_f32_16x16x32_bf16 v[156:159], v[56:59], a[76:79], v[156:159]\n" \
  "s_waitcnt lgkmcnt(4)\n" \
  "v_mfma_f32_16x16x32_bf16 v[160:163], v[40:43], a[64:67], v[160:163]\n" \
  "buffer_load_dwordx4 a[196:199], v24, s[52:55], s74 offen offset:1024 sc1\n" \
  "v_mfma_f32_16x16x32_bf16 v[160:163], v[44:47], a[80:83], v[160:163]\n" \
  "ds_read_b64_tr_b16 v[56:57], v5 offset:512\n" \
  "v_mfma_f32_16x16x32_bf16 v[164:167], v[44:47], a[84:87], v[164:167]\n" \
  "buffer_load_dwordx4 a[200:203], v24, s[52:55], s74 offen offset:2048 sc1\n" \
  "v_mfma_f32_16x16x32_bf16 v[164:167], v[40:43], a[68:71], v[164:167]\n" \
  "ds_read_b64_tr_b16 v[58:59], v5 offset:2560\n" \
  "v_mfma_f32_16x16x32_bf16 v[168:171], v[40:43], a[72:75], v[168:171]\n" \
  "s_cmp_ge_u32 s80, 2\n" \
  "s_cselect_b32 s22, s5, s4\n" \
  "v_mfma_f32_16x16x32_bf16 v[168:171], v[44:47], a[88:91], v[168:171]\n" \
  "ds_read_b64_tr_b16 v[60:61], v5 offset:4608\n" \
  "s_lshr_b32 s22, s22, 2\n" \
  "v_mfma_f32_16x16x32_bf16 v[172:175], v[44:47], a[92:95], v[172:175]\n" \
  "ds_read_b64_tr_b16 v[62:63], v5 offset:6656\n" \
  "s_add_u32 s22, s22, 44601344\n" \
  "v_mfma_f32_16x16x32_bf16 v[172:175], v[40:43], a[76:79], v[172:175]\n" \
  "s_waitcnt lgkmcnt(4)\n" \
  "v_mfma_f32_16x16x32_bf16 v[176:179], v[48:51], a[64:67], v[176:179]\n" \
  "s_add_u32 m0, s72, 126976\n" \
  "v_mfma_f32_16x16x32_bf16 v[176:179], v[52:55], a[80:83], v[176:179]\n" \
  "ds_read_b64_tr_b16 v[40:41], v5 offset:8704\n" \
  "v_mfma_f32_16x16x32_bf16 v[180:183], v[52:55], a[84:87], v[180:183]\n" \
  "buffer_load_dwordx4 v13, s[36:39], s73 offen  lds\n" \
  "v_mfma_f32_16x16x32_bf16 v[180:183], v[48:51], a[68:71], v[180:183]\n" \
  "ds_read_b64_tr_b16 v[42:43], v5 offset:10752\n" \
  "v_mfma_f32_16x16x32_bf16 v[184:187], v[48:51], a[72:75], v[184:187]\n" \
  "buffer_load_dwordx4 a[204:207], v24, s[52:55], s74 offen offset:3072 sc1\n" \
  "v_mfma_f32_16x16x32_bf16 v[184:187], v[52:55], a[88:91], v[184:187]\n" \
  "ds_read_b64_tr_b16 v[44:45], v5 offset:12800\n" \
  "v_mfma_f32_16x16x32_bf16 v[188:191], v[52:55], a[92:95], v[188:191]\n" \
  "ds_read_b64_tr_b16 v[46:47], v5 offset:14848\n" \
  "v_mfma_f32_16x16x32_bf16 v[188:191], v[48:51], a[76:79], v[188:191]\n" \
  "s_waitcnt vmcnt(32)\n" \
  "s_barrier\n" \
  "s_waitcnt lgkmcnt(4)\n" \
  "v_mfma_f32_16x16x32_bf16 v[192:195], v[56:59], a[64:67], v[192:195]\n" \
  "s_add_u32 m0, s72, 131072\n" \
  "v_mfma_f32_16x16x32_bf16 v[192:195], v[60:63], a[80:83], v[192:195]\n" \
  "ds_read_b64_tr_b16 v[48:49], v9 offset:36864\n" \
  "v_mfma_f32_16x16x32_bf16 v[196:199], v[60:63], a[84:87], v[196:199]\n" \
  "buffer_load_dwordx4 v12, s[40:43], s73 offen  lds\n" \
  "v_mfma_f32_16x16x32_bf16 v[196:199], v[56:59], a[68:71], v[196:199]\n" \
  "ds_read_b64_tr_b16 v[50:51], v9 offset:37376\n" \
  "v_mfma_f32_16x16x32_bf16 v[200:203], v[56:59], a[72:75], v[200:203]\n" \
  "buffer_load_dwordx4 a[208:211], v25, s[52:55], s74 offen offset:0 sc1\n" \
  "v_mfma_f32_16x16x32_bf16 v[200:203], v[60:63], a[88:91], v[200:203]\n" \
  "ds_read_b64_tr_b16 v[52:53], v9 offset:38912\n" \
  "v_mfma_f32_16x16x32_bf16 v[204:207], v[60:63], a[92:95], v[204:207]\n" \
  "ds_read_b64_tr_b16 v[54:55], v9 offset:39424\n" \
  "v_mfma_f32_16x16x32_bf16 v[204:207], v[56:59], a[76:79], v[204:207]\n" \
  "s_waitcnt lgkmcnt(4)\n" \
  "v_mfma_f32_16x16x32_bf16 v[208:211], v[40:43], a[64:67], v[208:211]\n" \
  "buffer_load_dwordx4 a[212:215], v25, s[52:55], s74 offen offset:1024 sc1\n" \
  "v_mfma_f32_16x16x32_bf16 v[208:211], v[44:47], a[80:83], v[208:211]\n" \
  "ds_read_b64_tr_b16 v[56:57], v9 offset:37888\n" \
  "v_mfma_f32_16x16x32_bf16 v[212:215], v[44:47], a[84:87], v[212:215]\n" \
  "buffer_load_dwordx4 a[216:219], v25, s[52:55], s74 offen offset:2048 sc1\n" \
  "v_mfma_f32_16x16x32_bf16 v[212:215], v[40:43], a[68:71], v[212:215]\n" \
  "ds_read_b64_tr_b16 v[58:59], v9 offset:38400\n" \
  "v_mfma_f32_16x16x32_bf16 v[216:219], v[40:43], a[72:75], v[216:219]\n" \
  "s_cmp_ge_u32 s80, 2\n" \
  "s_cselect_b32 s23, s7, s6\n" \
  "v_mfma_f32_16x16x32_bf16 v[216:219], v[44:47], a[88:91], v[216:219]\n" \
  "ds_read_b64_tr_b16 v[60:61], v9 offset:39936\n" \
  "s_lshr_b32 s23, s23, 2\n" \
  "v_mfma_f32_16x16x32_bf16 v[220:223], v[44:47], a[92:95], v[220:223]\n" \
  "ds_read_b64_tr_b16 v[62:63], v9 offset:40448\n" \
  "s_add_u32 s23, s23, 44601344\n" \
  "v_mfma_f32_16x16x32_bf16 v[220:223], v[40:43], a[76:79], v[220:223]\n" \
  "s_waitcnt vmcnt(32) lgkmcnt(4)\n" \
  "v_mfma_f32_16x16x32_bf16 v[224:227], v[48:51], a[96:99], v[224:227]\n" \
  "s_add_u32 m0, s72, 135168\n" \
  "PG3_%=:\n" \
  "HOP3_%=:\n" \
  "s_cbranch_execz PG7_%=\n" \
  "v_mfma_f32_16x16x32_bf16 v[224:227], v[52:55], a[112:115], v[224:227]\n" \
  "ds_read_b64_tr_b16 v[40:41], v4 offset:20480\n" \
  "v_mfma_f32_16x16x32_bf16 v[228:231], v[52:55], a[116:119], v[228:231]\n" \
  "buffer_load_dwordx4 v13, s[40:43], s73 offen  lds\n" \
  "v_mfma_f32_16x16x32_bf16 v[228:231], v[48:51], a[100:103], v[228:231]\n" \
  "ds_read_b64_tr_b16 v[42:43], v4 offset:22528\n" \
  "v_mfma_f32_16x16x32_bf16 v[232:235], v[48:51], a[104:107], v[232:235]\n" \
  "buffer_load_dwordx4 a[220:223], v25, s[52:55], s74 offen offset:3072 sc1\n" \
  "v_mfma_f32_16x16x32_bf16 v[232:235], v[52:55], a[120:123], v[232:235]\n" \
  "ds_read_b64_tr_b16 v[44:45], v4 offset:24576\n" \
  "v_mfma_f32_16x16x32_bf16 v[236:239], v[52:55], a[124:127], v[236:239]\n" \
  "ds_read_b64_tr_b16 v[46:47], v4 offset:26624\n" \
  "v_mfma_f32_16x16x32_bf16 v[236:239], v[48:51], a[108:111], v[236:239]\n" \
  "s_waitcnt lgkmcnt(4)\n" \
  "v_mfma_f32_16x16x32_bf16 v[240:243], v[56:59], a[96:99], v[240:243]\n" \
  "s_mov_b32 s74, 65536\n" \
  "v_mfma_f32_16x16x32_bf16 v[240:243], v[60:63], a[112:115], v[240:243]\n" \
  "ds_read_b64_tr_b16 v[48:49], v4 offset:28672\n" \
  "v_mfma_f32_16x16x32_bf16 v[244:247], v[60:63], a[116:119], v[244:247]\n" \
  "buffer_load_dwordx4 a[224:227], v24, s[52:55], s74 offen offset:0 sc1\n" \
  "v_mfma_f32_16x16x32_bf16 v[244:247], v[56:59], a[100:103], v[244:247]\n" \
  "ds_read_b64_tr_b16 v[50:51], v4 offset:30720\n" \
  "v_mfma_f32_16x16x32_bf16 v[248:251], v[56:59], a[104:107], v[248:251]\n" \
  "buffer_load_dwordx4 a[228:231], v24, s[52:55], s74 offen offset:1024 sc1\n" \
  "v_mfma_f32_16x16x32_bf16 v[248:251], v[60:63], a[120:123], v[248:251]\n" \
  "ds_read_b64_tr_b16 v[52:53], v4 offset:32768\n" \
  "v_mfma_f32_16x16x32_bf16 v[252:255], v[60:63], a[124:127], v[252:255]\n" \
  "ds_read_b64_tr_b16 v[54:55], v4 offset:34816\n" \
  "v_mfma_f32_16x16x32_bf16 v[252:255], v[56:59], a[108:111], v[252:255]\n" \
  "s_waitcnt lgkmcnt(4)\n" \
  "v_mfma_f32_16x16x32_bf16 v[96:99], v[40:43], a[96:99], v[96:99]\n" \
  "buffer_load_dwordx4 a[232:235], v24, s[52:55], s74 offen offset:2048 sc1\n" \
  "v_mfma_f32_16x16x32_bf16 v[96:99], v[44:47], a[112:115], v[96:99]\n" \
  "ds_read_b64_tr_b16 v[56:57], v5 offset:20480\n" \
  "v_mfma_f32_16x16x32_bf16 v[100:103], v[44:47], a[116:119], v[100:103]\n" \
  "s_cmp_ge_u32 s80, 2\n" \
  "s_cselect_b32 s24, s9, s8\n" \
  "v_mfma_f32_16x16x32_bf16 v[100:103], v[40:43], a[100:103], v[100:103]\n" \
  "ds_read_b64_tr_b16 v[58:59], v5 offset:22528\n" \
  "s_lshr_b32 s24, s24, 2\n" \
  "v_mfma_f32_16x16x32_bf16 v[104:107], v[40:43], a[104:107], v[104:107]\n" \
  "s_add_u32 s24, s24, 44601344\n" \
  "v_mfma_f32_16x16x32_bf16 v[104:107], v[44:47], a[120:123], v[104:107]\n" \
  "ds_read_b64_tr_b16 v[60:61], v5 offset:24576\n" \
  "v_mfma_f32_16x16x32_bf16 v[108:111], v[44:47], a[124:127], v[108:111]\n" \
  "ds_read_b64_tr_b16 v[62:63], v5 offset:26624\n" \
  "v_mfma_f32_16x16x32_bf16 v[108:111], v[40:43], a[108:111], v[108:111]\n" \
  "s_waitcnt lgkmcnt(4)\n" \
  "v_mfma_f32_16x16x32_bf16 v[112:115], v[48:51], a[96:99], v[112:115]\n" \
  "buffer_load_dwordx4 a[236:239], v24, s[52:55], s74 offen offset:3072 sc1\n" \
  "v_mfma_f32_16x16x32_bf16 v[112:115], v[52:55], a[112:115], v[112:115]\n" \
  "ds_read_b64_tr_b16 v[40:41], v5 offset:28672\n" \
  "v_mfma_f32_16x16x32_bf16 v[116:119], v[52:55], a[116:119], v[116:119]\n" \
  "s_cmp_ge_u32 s80, 2\n" \
  "s_cselect_b32 s25, s11, s10\n" \
  "v_mfma_f32_16x16x32_bf16 v[116:119], v[48:51], a[100:103], v[116:119]\n" \
  "ds_read_b64_tr_b16 v[42:43], v5 offset:30720\n" \
  "s_lshr_b32 s25, s25, 2\n" \
  "v_mfma_f32_16x16x32_bf16 v[120:123], v[48:51], a[104:107], v[120:123]\n" \
  "s_add_u32 s25, s25, 44601344\n" \
  "v_mfma_f32_16x16x32_bf16 v[120:123], v[52:55], a[120:123], v[120:123]\n" \
  "ds_read_b64_tr_b16 v[44:45], v5 offset:32768\n" \
  "v_mfma_f32_16x16x32_bf16 v[124:127], v[52:55], a[124:127], v[124:127]\n" \
  "ds_read_b64_tr_b16 v[46:47], v5 offset:34816\n" \
  "v_mfma_f32_16x16x32_bf16 v[124:127], v[48:51], a[108:111], v[124:127]\n" \
  "s_waitcnt lgkmcnt(4)\n" \
  "v_mfma_f32_16x16x32_bf16 v[128:131], v[56:59], a[96:99], v[128:131]\n" \
  "s_mov_b32 s73, 44578816\n" \
  "s_add_u32 m0, s72, 159744\n" \
  "v_mfma_f32_16x16x32_bf16 v[128:131], v[60:63], a[112:115], v[128:131]\n" \
  "ds_read_b64_tr_b16 v[48:49], v4 offset:20992\n" \
  "v_mfma_f32_16x16x32_bf16 v[132:135], v[60:63], a[116:119], v[132:135]\n" \
  "buffer_load_dwordx4 v14, s[60:63], s73 offen sc1 lds\n" \
  "v_mfma_f32_16x16x32_bf16 v[132:135], v[56:59], a[100:103], v[132:135]\n" \
  "ds_read_b64_tr_b16 v[50:51], v4 offset:23040\n" \
  "v_mfma_f32_16x16x32_bf16 v[136:139], v[56:59], a[104:107], v[136:139]\n" \
  "buffer_load_dwordx4 a[240:243], v25, s[52:55], s74 offen offset:0 sc1\n" \
  "v_mfma_f32_16x16x32_bf16 v[136:139], v[60:63], a[120:123], v[136:139]\n" \
  "ds_read_b64_tr_b16 v[52:53], v4 offset:25088\n" \
  "v_mfma_f32_16x16x32_bf16 v[140:143], v[60:63], a[124:127], v[140:143]\n" \
  "ds_read_b64_tr_b16 v[54:55], v4 offset:27136\n" \
  "v_mfma_f32_16x16x32_bf16 v[140:143], v[56:59], a[108:111], v[140:143]\n" \
  "s_waitcnt lgkmcnt(4)\n" \
  "v_mfma_f32_16x16x32_bf16 v[144:147], v[40:43], a[96:99], v[144:147]\n" \
  "buffer_load_dwordx4 a[244:247], v25, s[52:55], s74 offen offset:1024 sc1\n" \
  "v_mfma_f32_16x16x32_bf16 v[144:147], v[44:47], a[112:115], v[144:147]\n" \
  "ds_read_b64_tr_b16 v[56:57], v4 offset:29184\n" \
  "s_mov_b32 s73, 4128768\n" \
  "v_mfma_f32_16x16x32_bf16 v[148:151], v[44:47], a[116:119], v[148:151]\n" \
  "s_add_u32 m0, s72, 143360\n" \
  "v_mfma_f32_16x16x32_bf16 v[148:151], v[40:43], a[100:103], v[148:151]\n" \
  "ds_read_b64_tr_b16 v[58:59], v4 offset:31232\n" \
  "v_mfma_f32_16x16x32_bf16 v[152:155], v[40:43], a[104:107], v[152:155]\n" \
  "buffer_load_dwordx4 v12, s[36:39], s73 offen  lds\n" \
  "v_mfma_f32_16x16x32_bf16 v[152:155], v[44:47], a[120:123], v[152:155]\n" \
  "ds_read_b64_tr_b16 v[60:61], v4 offset:33280\n" \
  "v_mfma_f32_16x16x32_bf16 v[156:159], v[44:47], a[124:127], v[156:159]\n" \
  "ds_read_b64_tr_b16 v[62:63], v4 offset:35328\n" \
  "v_mfma_f32_16x16x32_bf16 v[156:159], v[40:43], a[108:111], v[156:159]\n" \
  "s_waitcnt lgkmcnt(4)\n" \
  "v_mfma_f32_16x16x32_bf16 v[160:163], v[48:51], a[96:99], v[160:163]\n" \
  "buffer_load_dwordx4 a[248:251], v25, s[52:55], s74 offen offset:2048 sc1\n" \
  "v_mfma_f32_16x16x32_bf16 v[160:163], v[52:55], a[112:115], v[160:163]\n" \
  "ds_read_b64_tr_b16 v[40:41], v5 offset:20992\n" \
  "v_mfma_f32_16x16x32_bf16 v[164:167], v[52:55], a[116:119], v[164:167]\n" \
  "buffer_load_dwordx4 a[252:255], v25, s[52:55], s74 offen offset:3072 sc1\n" \
  "v_mfma_f32_16x16x32_bf16 v[164:167], v[48:51], a[100:103], v[164:167]\n" \
  "ds_read_b64_tr_b16 v[42:43], v5 offset:23040\n" \
  "v_mfma_f32_16x16x32_bf16 v[168:171], v[48:51], a[104:107], v[168:171]\n" \
  "s_cmp_ge_u32 s80, 2\n" \
  "s_cselect_b32 s26, s12, s12\n" \
  "v_mfma_f32_16x16x32_bf16 v[168:171], v[52:55], a[120:123], v[168:171]\n" \
  "ds_read_b64_tr_b16 v[44:45], v5 offset:25088\n" \
  "s_lshr_b32 s26, s26, 2\n" \
  "v_mfma_f32_16x16x32_bf16 v[172:175], v[52:55], a[124:127], v[172:175]\n" \
  "ds_read_b64_tr_b16 v[46:47], v5 offset:27136\n" \
  "s_add_u32 s26, s26, 44601344\n" \
  "v_mfma_f32_16x16x32_bf16 v[172:175], v[48:51], a[108:111], v[172:175]\n" \
  "v_mov_b32 v28, s84\n" \
  "s_waitcnt lgkmcnt(4)\n" \
  "v_mfma_f32_16x16x32_bf16 v[176:179], v[56:59], a[96:99], v[176:179]\n" \
  "s_add_u32 m0, s72, 147456\n" \
  "v_lshrrev_b32 v32, 2, v11\n" \
  "v_mfma_f32_16x16x32_bf16 v[176:179], v[60:63], a[112:115], v[176:179]\n" \
  "ds_read_b64_tr_b16 v[48:49], v5 offset:29184\n" \
  "v_xor_b32 v26, s77, v11\n" \
  "v_mfma_f32_16x16x32_bf16 v[180:183], v[60:63], a[116:119], v[180:183]\n" \
  "buffer_load_dwordx4 v13, s[36:39], s73 offen  lds\n" \
  "v_mfma_f32_16x16x32_bf16 v[180:183], v[56:59], a[100:103], v[180:183]\n" \
  "ds_read_b64_tr_b16 v[50:51], v5 offset:31232\n" \
  "v_lshlrev_b32 v26, 7, v26\n" \
  "v_mfma_f32_16x16x32_bf16 v[184:187], v[56:59], a[104:107], v[184:187]\n" \
  "v_lshl_add_u32 v26, v32, 27, v26\n" \
  "v_writelane_b32 v26, 2.0, 0\n" \
  "v_mfma_f32_16x16x32_bf16 v[184:187], v[60:63], a[120:123], v[184:187]\n" \
  "ds_read_b64_tr_b16 v[52:53], v5 offset:33280\n" \
  "s_lshl_b32 s94, s78, 5\n" \
  "v_mfma_f32_16x16x32_bf16 v[188:191], v[60:63], a[124:127], v[188:191]\n" \
  "ds_read_b64_tr_b16 v[54:55], v5 offset:35328\n" \
  "s_and_b32 s89, s94, 96\n" \
  "v_mfma_f32_16x16x32_bf16 v[188:191], v[56:59], a[108:111], v[188:191]\n" \
  "s_waitcnt vmcnt(30)\n" \
  "s_barrier\n" \
  "s_waitcnt lgkmcnt(4)\n" \
  "v_mfma_f32_16x16x32_bf16 v[192:195], v[40:43], a[96:99], v[192:195]\n" \
  "s_add_u32 m0, s72, 151552\n" \
  "v_xor_b32 v29, s89, v29\n" \
  "v_mfma_f32_16x16x32_bf16 v[192:195], v[44:47], a[112:115], v[192:195]\n" \
  "ds_read_b64_tr_b16 v[56:57], v9 offset:57344\n" \
  "s_mul_i32 s89, s78, 663552\n" \
  "v_mfma_f32_16x16x32_bf16 v[196:199], v[44:47], a[116:119], v[196:199]\n" \
  "buffer_load_dwordx4 v12, s[40:43], s73 offen  lds\n" \
  "v_mfma_f32_16x16x32_bf16 v[196:199], v[40:43], a[100:103], v[196:199]\n" \
  "ds_read_b64_tr_b16 v[58:59], v9 offset:57856\n" \
  "s_lshl_b32 s90, s77, 8\n" \
  "v_mfma_f32_16x16x32_bf16 v[200:203], v[40:43], a[104:107], v[200:203]\n" \
  "s_add_u32 s90, s90, s89\n" \
  "s_and_b32 s91, s94, 128\n" \
  "v_mfma_f32_16x16x32_bf16 v[200:203], v[44:47], a[120:123], v[200:203]\n" \
  "ds_read_b64_tr_b16 v[60:61], v9 offset:59392\n" \
  "s_add_u32 s90, s90, s91\n" \
  "v_mfma_f32_16x16x32_bf16 v[204:207], v[44:47], a[124:127], v[204:207]\n" \
  "ds_read_b64_tr_b16 v[62:63], v9 offset:59904\n" \
  "v_add_u32 v29, s90, v29\n" \
  "v_mfma_f32_16x16x32_bf16 v[204:207], v[40:43], a[108:111], v[204:207]\n" \
  "v_add_u32 v30, 36864, v29\n" \
  "s_waitcnt lgkmcnt(4)\n" \
  "v_mfma_f32_16x16x32_bf16 v[208:211], v[48:51], a[96:99], v[208:211]\n" \
  "s_lshl_b32 s101, s78, 13\n" \
  "s_add_u32 s101, s101, s88\n" \
  "v_mfma_f32_16x16x32_bf16 v[208:211], v[52:55], a[112:115], v[208:211]\n" \
  "ds_read_b64_tr_b16 v[40:41], v9 offset:58368\n" \
  "s_lshl_b32 s89, s80, 8\n" \
  "v_mfma_f32_16x16x32_bf16 v[212:215], v[52:55], a[116:119], v[212:215]\n" \
  "s_add_u32 s101, s101, s89\n" \
  "s_add_u32 s31, s101, 4096\n" \
  "v_mfma_f32_16x16x32_bf16 v[212:215], v[48:51], a[100:103], v[212:215]\n" \
  "ds_read_b64_tr_b16 v[42:43], v9 offset:58880\n" \
  "s_lshl_b32 s89, s79, 2\n" \
  "v_mfma_f32_16x16x32_bf16 v[216:219], v[48:51], a[104:107], v[216:219]\n" \
  "s_add_u32 s89, s89, s77\n" \
  "s_lshl_b32 s89, s89, 1\n" \
  "v_mfma_f32_16x16x32_bf16 v[216:219], v[52:55], a[120:123], v[216:219]\n" \
  "ds_read_b64_tr_b16 v[44:45], v9 offset:60416\n" \
  "s_lshr_b32 s90, s80, 1\n" \
  "v_mfma_f32_16x16x32_bf16 v[220:223], v[52:55], a[124:127], v[220:223]\n" \
  "ds_read_b64_tr_b16 v[46:47], v9 offset:60928\n" \
  "s_add_u32 s89, s89, s90\n" \
  "v_mfma_f32_16x16x32_bf16 v[220:223], v[48:51], a[108:111], v[220:223]\n" \
  "s_mul_i32 s89, s89, 82944\n" \
  "s_waitcnt lgkmcnt(4)\n" \
  "v_mfma_f32_16x16x32_bf16 v[224:227], v[56:59], a[128:131], v[224:227]\n" \
  "s_add_u32 m0, s72, 155648\n" \
  "s_mul_i32 s90, s78, 1152\n" \
  "v_mfma_f32_16x16x32_bf16 v[224:227], v[60:63], a[144:147], v[224:227]\n" \
  "ds_read_b64_tr_b16 v[48:49], v4 offset:40960\n" \
  "s_add_u32 s89, s89, s90\n" \
  "v_mfma_f32_16x16x32_bf16 v[228:231], v[60:63], a[148:151], v[228:231]\n" \
  "buffer_load_dwordx4 v13, s[40:43], s73 offen  lds\n" \
  "v_mfma_f32_16x16x32_bf16 v[228:231], v[56:59], a[132:135], v[228:231]\n" \
  "ds_read_b64_tr_b16 v[50:51], v4 offset:43008\n" \
  "v_lshrrev_b32 v33, 2, v11\n" \
  "v_mfma_f32_16x16x32_bf16 v[232:235], v[56:59], a[136:139], v[232:235]\n" \
  "v_and_b32 v32, 3, v11\n" \
  "v_mul_u32_u24 v33, 9216, v33\n" \
  "v_mfma_f32_16x16x32_bf16 v[232:235], v[60:63], a[152:155], v[232:235]\n" \
  "ds_read_b64_tr_b16 v[52:53], v4 offset:45056\n" \
  "v_lshl_add_u32 v32, v32, 8, v33\n" \
  "v_mfma_f32_16x16x32_bf16 v[236:239], v[60:63], a[156:159], v[236:239]\n" \
  "ds_read_b64_tr_b16 v[54:55], v4 offset:47104\n" \
  "v_add_u32 v33, 28, v11\n" \
  "v_mfma_f32_16x16x32_bf16 v[236:239], v[56:59], a[140:143], v[236:239]\n" \
  "v_lshrrev_b32 v33, 6, v33\n" \
  "s_waitcnt lgkmcnt(4)\n" \
  "v_mfma_f32_16x16x32_bf16 v[240:243], v[40:43], a[128:131], v[240:243]\n" \
  "v_lshl_add_u32 v32, v33, 30, v32\n" \
  "v_mfma_f32_16x16x32_bf16 v[240:243], v[44:47], a[144:147], v[240:243]\n" \
  "ds_read_b64_tr_b16 v[56:57], v4 offset:49152\n" \
  "v_mfma_f32_16x16x32_bf16 v[244:247], v[44:47], a[148:151], v[244:247]\n" \
  "buffer_load_dword v37, v32, s[44:47], s89 offen offset:128 sc1\n" \
  "v_mfma_f32_16x16x32_bf16 v[244:247], v[40:43], a[132:135], v[244:247]\n" \
  "ds_read_b64_tr_b16 v[58:59], v4 offset:51200\n" \
  "v_mfma_f32_16x16x32_bf16 v[248:251], v[40:43], a[136:139], v[248:251]\n" \
  "v_mfma_f32_16x16x32_bf16 v[248:251], v[44:47], a[152:155], v[248:251]\n" \
  "ds_read_b64_tr_b16 v[60:61], v4 offset:53248\n" \
  "v_mfma_f32_16x16x32_bf16 v[252:255], v[44:47], a[156:159], v[252:255]\n" \
  "ds_read_b64_tr_b16 v[62:63], v4 offset:55296\n" \
  "v_mfma_f32_16x16x32_bf16 v[252:255], v[40:43], a[140:143], v[252:255]\n" \
  "s_waitcnt lgkmcnt(4)\n" \
  "v_mfma_f32_16x16x32_bf16 v[96:99], v[48:51], a[128:131], v[96:99]\n" \
  "s_mov_b32 s73, 44580864\n" \
  "s_add_u32 m0, s72, 16384\n" \
  "v_mfma_f32_16x16x32_bf16 v[96:99], v[52:55], a[144:147], v[96:99]\n" \
  "ds_read_b64_tr_b16 v[40:41], v5 offset:40960\n" \
  "v_mfma_f32_16x16x32_bf16 v[100:103], v[52:55], a[148:151], v[100:103]\n" \
  "buffer_load_dwordx4 v14, s[60:63], s73 offen sc1 lds\n" \
  "v_mfma_f32_16x16x32_bf16 v[100:103], v[48:51], a[132:135], v[100:103]\n" \
  "ds_read_b64_tr_b16 v[42:43], v5 offset:43008\n" \
  "v_mfma_f32_16x16x32_bf16 v[104:107], v[48:51], a[136:139], v[104:107]\n" \
  "v_mfma_f32_16x16x32_bf16 v[104:107], v[52:55], a[152:155], v[104:107]\n" \
  "ds_read_b64_tr_b16 v[44:45], v5 offset:45056\n" \
  "v_mfma_f32_16x16x32_bf16 v[108:111], v[52:55], a[156:159], v[108:111]\n" \
  "ds_read_b64_tr_b16 v[46:47], v5 offset:47104\n" \
  "v_mfma_f32_16x16x32_bf16 v[108:111], v[48:51], a[140:143], v[108:111]\n" \
  "s_waitcnt lgkmcnt(4)\n" \
  "v_mfma_f32_16x16x32_bf16 v[112:115], v[56:59], a[128:131], v[112:115]\n" \
  "s_mov_b32 s73, 4718592\n" \
  "s_add_u32 m0, s72, 0\n" \
  "v_mfma_f32_16x16x32_bf16 v[112:115], v[60:63], a[144:147], v[112:115]\n" \
  "ds_read_b64_tr_b16 v[48:49], v5 offset:49152\n" \
  "v_mfma_f32_16x16x32_bf16 v[116:119], v[60:63], a[148:151], v[116:119]\n" \
  "buffer_load_dwordx4 v12, s[36:39], s73 offen  lds\n" \
  "v_mfma_f32_16x16x32_bf16 v[116:119], v[56:59], a[132:135], v[116:119]\n" \
  "ds_read_b64_tr_b16 v[50:51], v5 offset:51200\n" \
  "v_mfma_f32_16x16x32_bf16 v[120:123], v[56:59], a[136:139], v[120:123]\n" \
  "v_mfma_f32_16x16x32_bf16 v[120:123], v[60:63], a[152:155], v[120:123]\n" \
  "ds_read_b64_tr_b16 v[52:53], v5 offset:53248\n" \
  "v_mfma_f32_16x16x32_bf16 v[124:127], v[60:63], a[156:159], v[124:127]\n" \
  "ds_read_b64_tr_b16 v[54:55], v5 offset:55296\n" \
  "v_mfma_f32_16x16x32_bf16 v[124:127], v[56:59], a[140:143], v[124:127]\n" \
  "s_waitcnt lgkmcnt(4)\n" \
  "v_mfma_f32_16x16x32_bf16 v[128:131], v[40:43], a[128:131], v[128:131]\n" \
  "s_add_u32 m0, s72, 4096\n" \
  "v_mfma_f32_16x16x32_bf16 v[128:131], v[44:47], a[144:147], v[128:131]\n" \
  "ds_read_b64_tr_b16 v[56:57], v4 offset:41472\n" \
  "v_mfma_f32_16x16x32_bf16 v[132:135], v[44:47], a[148:151], v[132:135]\n" \
  "buffer_load_dwordx4 v13, s[36:39], s73 offen  lds\n" \
  "v_mfma_f32_16x16x32_bf16 v[132:135], v[40:43], a[132:135], v[132:135]\n" \
  "ds_read_b64_tr_b16 v[58:59], v4 offset:43520\n" \
  "v_mfma_f32_16x16x32_bf16 v[136:139], v[40:43], a[136:139], v[136:139]\n" \
  "v_mfma_f32_16x16x32_bf16 v[136:139], v[44:47], a[152:155], v[136:139]\n" \
  "ds_read_b64_tr_b16 v[60:61], v4 offset:45568\n" \
  "v_mfma_f32_16x16x32_bf16 v[140:143], v[44:47], a[156:159], v[140:143]\n" \
  "ds_read_b64_tr_b16 v[62:63], v4 offset:47616\n" \
  "v_mfma_f32_16x16x32_bf16 v[140:143], v[40:43], a[140:143], v[140:143]\n" \
  "s_waitcnt lgkmcnt(4)\n" \
  "v_mfma_f32_16x16x32_bf16 v[144:147], v[48:51], a[128:131], v[144:147]\n" \
  "s_add_u32 m0, s72, 8192\n" \
  "v_mfma_f32_16x16x32_bf16 v[144:147], v[52:55], a[144:147], v[144:147]\n" \
  "ds_read_b64_tr_b16 v[40:41], v4 offset:49664\n" \
  "v_mfma_f32_16x16x32_bf16 v[148:151], v[52:55], a[148:151], v[148:151]\n" \
  "buffer_load_dwordx4 v12, s[40:43], s73 offen  lds\n" \
  "v_mfma_f32_16x16x32_bf16 v[148:151], v[48:51], a[132:135], v[148:151]\n" \
  "ds_read_b64_tr_b16 v[42:43], v4 offset:51712\n" \
  "v_mfma_f32_16x16x32_bf16 v[152:155], v[48:51], a[136:139], v[152:155]\n" \
  "v_mfma_f32_16x16x32_bf16 v[152:155], v[52:55], a[152:155], v[152:155]\n" \
  "ds_read_b64_tr_b16 v[44:45], v4 offset:53760\n" \
  "v_mfma_f32_16x16x32_bf16 v[156:159], v[52:55], a[156:159], v[156:159]\n" \
  "ds_read_b64_tr_b16 v[46:47], v4 offset:55808\n" \
  "v_mfma_f32_16x16x32_bf16 v[156:159], v[48:51], a[140:143], v[156:159]\n" \
  "s_waitcnt lgkmcnt(4)\n" \
  "v_mfma_f32_16x16x32_bf16 v[160:163], v[56:59], a[128:131], v[160:163]\n" \
  "s_add_u32 m0, s72, 12288\n" \
  "v_mfma_f32_16x16x32_bf16 v[160:163], v[60:63], a[144:147], v[160:163]\n" \
  "ds_read_b64_tr_b16 v[48:49], v5 offset:41472\n" \
  "v_mfma_f32_16x16x32_bf16 v[164:167], v[60:63], a[148:151], v[164:167]\n" \
  "buffer_load_dwordx4 v13, s[40:43], s73 offen  lds\n" \
  "v_mfma_f32_16x16x32_bf16 v[164:167], v[56:59], a[132:135], v[164:167]\n" \
  "ds_read_b64_tr_b16 v[50:51], v5 offset:43520\n" \
  "v_mfma_f32_16x16x32_bf16 v[168:171], v[56:59], a[136:139], v[168:171]\n" \
  "v_mfma_f32_16x16x32_bf16 v[168:171], v[60:63], a[152:155], v[168:171]\n" \
  "ds_read_b64_tr_b16 v[52:53], v5 offset:45568\n" \
  "v_mfma_f32_16x16x32_bf16 v[172:175], v[60:63], a[156:159], v[172:175]\n" \
  "ds_read_b64_tr_b16 v[54:55], v5 offset:47616\n" \
  "v_mfma_f32_16x16x32_bf16 v[172:175], v[56:59], a[140:143], v[172:175]\n" \
  "s_waitcnt lgkmcnt(4)\n" \
  "v_mfma_f32_16x16x32_bf16 v[176:179], v[40:43], a[128:131], v[176:179]\n" \
  "v_mfma_f32_16x16x32_bf16 v[176:179], v[44:47], a[144:147], v[176:179]\n" \
  "ds_read_b64_tr_b16 v[56:57], v5 offset:49664\n" \
  "v_mfma_f32_16x16x32_bf16 v[180:183], v[44:47], a[148:151], v[180:183]\n" \
  "v_mfma_f32_16x16x32_bf16 v[180:183], v[40:43], a[132:135], v[180:183]\n" \
  "ds_read_b64_tr_b16 v[58:59], v5 offset:51712\n" \
  "v_mfma_f32_16x16x32_bf16 v[184:187], v[40:43], a[136:139], v[184:187]\n" \
  "v_mfma_f32_16x16x32_bf16 v[184:187], v[44:47], a[152:155], v[184:187]\n" \
  "ds_read_b64_tr_b16 v[60:61], v5 offset:53760\n" \
  "v_mfma_f32_16x16x32_bf16 v[188:191], v[44:47], a[156:159], v[188:191]\n" \
  "ds_read_b64_tr_b16 v[62:63], v5 offset:55808\n" \
  "v_mfma_f32_16x16x32_bf16 v[188:191], v[40:43], a[140:143], v[188:191]\n" \
  "s_waitcnt vmcnt(20)\n" \
  "s_barrier\n" \
  "s_waitcnt lgkmcnt(4)\n" \
  "v_mfma_f32_16x16x32_bf16 v[192:195], v[48:51], a[128:131], v[192:195]\n" \
  "v_mfma_f32_16x16x32_bf16 v[192:195], v[52:55], a[144:147], v[192:195]\n" \
  "ds_read_b64_tr_b16 v[40:41], v10 offset:16384\n" \
  "v_mfma_f32_16x16x32_bf16 v[196:199], v[52:55], a[148:151], v[196:199]\n" \
  "v_mfma_f32_16x16x32_bf16 v[196:199], v[48:51], a[132:135], v[196:199]\n" \
  "ds_read_b64_tr_b16 v[42:43], v10 offset:16896\n" \
  "v_mfma_f32_16x16x32_bf16 v[200:203], v[48:51], a[136:139], v[200:203]\n" \
  "v_mfma_f32_16x16x32_bf16 v[200:203], v[52:55], a[152:155], v[200:203]\n" \
  "ds_read_b64_tr_b16 v[44:45], v10 offset:18432\n" \
  "v_mfma_f32_16x16x32_bf16 v[204:207], v[52:55], a[156:159], v[204:207]\n" \
  "ds_read_b64_tr_b16 v[46:47], v10 offset:18944\n" \
  "v_mfma_f32_16x16x32_bf16 v[204:207], v[48:51], a[140:143], v[204:207]\n" \
  "s_waitcnt lgkmcnt(4)\n" \
  "v_mfma_f32_16x16x32_bf16 v[208:211], v[56:59], a[128:131], v[208:211]\n" \
  "v_mfma_f32_16x16x32_bf16 v[208:211], v[60:63], a[144:147], v[208:211]\n" \
  "ds_read_b64_tr_b16 v[48:49], v10 offset:17408\n" \
  "v_mfma_f32_16x16x32_bf16 v[212:215], v[60:63], a[148:151], v[212:215]\n" \
  "v_mfma_f32_16x16x32_bf16 v[212:215], v[56:59], a[132:135], v[212:215]\n" \
  "ds_read_b64_tr_b16 v[50:51], v10 offset:17920\n" \
  "v_mfma_f32_16x16x32_bf16 v[216:219], v[56:59], a[136:139], v[216:219]\n" \
  "v_mfma_f32_16x16x32_bf16 v[216:219], v[60:63], a[152:155], v[216:219]\n" \
  "ds_read_b64_tr_b16 v[52:53], v10 offset:19456\n" \
  "v_mfma_f32_16x16x32_bf16 v[220:223], v[60:63], a[156:159], v[220:223]\n" \
  "ds_read_b64_tr_b16 v[54:55], v10 offset:19968\n" \
  "v_mfma_f32_16x16x32_bf16 v[220:223], v[56:59], a[140:143], v[220:223]\n" \
  "s_waitcnt lgkmcnt(4)\n" \
  "v_mfma_f32_16x16x32_bf16 v[224:227], v[40:43], a[160:163], v[224:227]\n" \
  "s_mov_b32 s73, 128\n" \
  "s_add_u32 m0, s72, 20480\n" \
  "v_mfma_f32_16x16x32_bf16 v[224:227], v[44:47], a[176:179], v[224:227]\n" \
  "ds_read_b64_tr_b16 v[56:57], v6 offset:0\n" \
  "v_mfma_f32_16x16x32_bf16 v[228:231], v[44:47], a[180:183], v[228:231]\n" \
  "buffer_load_dwordx4 v12, s[36:39], s73 offen  lds\n" \
  "v_mfma_f32_16x16x32_bf16 v[228:231], v[40:43], a[164:167], v[228:231]\n" \
  "ds_read_b64_tr_b16 v[58:59], v6 offset:2048\n" \
  "v_mfma_f32_16x16x32_bf16 v[232:235], v[40:43], a[168:171], v[232:235]\n" \
  "v_mfma_f32_16x16x32_bf16 v[232:235], v[44:47], a[184:187], v[232:235]\n" \
  "ds_read_b64_tr_b16 v[60:61], v6 offset:4096\n" \
  "v_mfma_f32_16x16x32_bf16 v[236:239], v[44:47], a[188:191], v[236:239]\n" \
  "ds_read_b64_tr_b16 v[62:63], v6 offset:6144\n" \
  "v_mfma_f32_16x16x32_bf16 v[236:239], v[40:43], a[172:175], v[236:239]\n" \
  "s_waitcnt lgkmcnt(4)\n" \
  "v_mfma_f32_16x16x32_bf16 v[240:243], v[48:51], a[160:163], v[240:243]\n" \
  "s_add_u32 m0, s72, 24576\n" \
  "v_mfma_f32_16x16x32_bf16 v[240:243], v[52:55], a[176:179], v[240:243]\n" \
  "ds_read_b64_tr_b16 v[40:41], v6 offset:8192\n" \
  "v_mfma_f32_16x16x32_bf16 v[244:247], v[52:55], a[180:183], v[244:247]\n" \
  "buffer_load_dwordx4 v13, s[36:39], s73 offen  lds\n" \
  "v_mfma_f32_16x16x32_bf16 v[244:247], v[48:51], a[164:167], v[244:247]\n" \
  "ds_read_b64_tr_b16 v[42:43], v6 offset:10240\n" \
  "v_mfma_f32_16x16x32_bf16 v[248:251], v[48:51], a[168:171], v[248:251]\n" \
  "v_mfma_f32_16x16x32_bf16 v[248:251], v[52:55], a[184:187], v[248:251]\n" \
  "ds_read_b64_tr_b16 v[44:45], v6 offset:12288\n" \
  "v_mfma_f32_16x16x32_bf16 v[252:255], v[52:55], a[188:191], v[252:255]\n" \
  "ds_read_b64_tr_b16 v[46:47], v6 offset:14336\n" \
  "v_mfma_f32_16x16x32_bf16 v[252:255], v[48:51], a[172:175], v[252:255]\n" \
  "s_waitcnt lgkmcnt(4)\n" \
  "v_mfma_f32_16x16x32_bf16 v[96:99], v[56:59], a[160:163], v[96:99]\n" \
  "s_add_u32 m0, s72, 28672\n" \
  "v_mfma_f32_16x16x32_bf16 v[96:99], v[60:63], a[176:179], v[96:99]\n" \
  "ds_read_b64_tr_b16 v[48:49], v7 offset:0\n" \
  "v_mfma_f32_16x16x32_bf16 v[100:103], v[60:63], a[180:183], v[100:103]\n" \
  "buffer_load_dwordx4 v12, s[40:43], s73 offen  lds\n" \
  "v_mfma_f32_16x16x32_bf16 v[100:103], v[56:59], a[164:167], v[100:103]\n" \
  "ds_read_b64_tr_b16 v[50:51], v7 offset:2048\n" \
  "v_mfma_f32_16x16x32_bf16 v[104:107], v[56:59], a[168:171], v[104:107]\n" \
  "v_mfma_f32_16x16x32_bf16 v[104:107], v[60:63], a[184:187], v[104:107]\n" \
  "ds_read_b64_tr_b16 v[52:53], v7 offset:4096\n" \
  "v_mfma_f32_16x16x32_bf16 v[108:111], v[60:63], a[188:191], v[108:111]\n" \
  "ds_read_b64_tr_b16 v[54:55], v7 offset:6144\n" \
  "v_mfma_f32_16x16x32_bf16 v[108:111], v[56:59], a[172:175], v[108:111]\n" \
  "s_waitcnt lgkmcnt(4)\n" \
  "v_mfma_f32_16x16x32_bf16 v[112:115], v[40:43], a[160:163], v[112:115]\n" \
  "s_add_u32 m0, s72, 32768\n" \
  "v_mfma_f32_16x16x32_bf16 v[112:115], v[44:47], a[176:179], v[112:115]\n" \
  "ds_read_b64_tr_b16 v[56:57], v7 offset:8192\n" \
  "v_mfma_f32_16x16x32_bf16 v[116:119], v[44:47], a[180:183], v[116:119]\n" \
  "buffer_load_dwordx4 v13, s[40:43], s73 offen  lds\n" \
  "v_mfma_f32_16x16x32_bf16 v[116:119], v[40:43], a[164:167], v[116:119]\n" \
  "ds_read_b64_tr_b16 v[58:59], v7 offset:10240\n" \
  "v_mfma_f32_16x16x32_bf16 v[120:123], v[40:43], a[168:171], v[120:123]\n" \
  "v_mfma_f32_16x16x32_bf16 v[120:123], v[44:47], a[184:187], v[120:123]\n" \
  "ds_read_b64_tr_b16 v[60:61], v7 offset:12288\n" \
  "v_mfma_f32_16x16x32_bf16 v[124:127], v[44:47], a[188:191], v[124:127]\n" \
  "ds_read_b64_tr_b16 v[62:63], v7 offset:14336\n" \
  "v_mfma_f32_16x16x32_bf16 v[124:127], v[40:43], a[172:175], v[124:127]\n" \
  "s_waitcnt lgkmcnt(4)\n" \
  "v_mfma_f32_16x16x32_bf16 v[128:131], v[48:51], a[160:163], v[128:131]\n" \
  "v_mfma_f32_16x16x32_bf16 v[128:131], v[52:55], a[176:179], v[128:131]\n" \
  "ds_read_b64_tr_b16 v[40:41], v6 offset:512\n" \
  "v_mfma_f32_16x16x32_bf16 v[132:135], v[52:55], a[180:183], v[132:135]\n" \
  "v_mfma_f32_16x16x32_bf16 v[132:135], v[48:51], a[164:167], v[132:135]\n" \
  "ds_read_b64_tr_b16 v[42:43], v6 offset:2560\n" \
  "v_mfma_f32_16x16x32_bf16 v[136:139], v[48:51], a[168:171], v[136:139]\n" \
  "v_mfma_f32_16x16x32_bf16 v[136:139], v[52:55], a[184:187], v[136:139]\n" \
  "ds_read_b64_tr_b16 v[44:45], v6 offset:4608\n" \
  "v_mfma_f32_16x16x32_bf16 v[140:143], v[52:55], a[188:191], v[140:143]\n" \
  "ds_read_b64_tr_b16 v[46:47], v6 offset:6656\n" \
  "v_mfma_f32_16x16x32_bf16 v[140:143], v[48:51], a[172:175], v[140:143]\n" \
  "s_waitcnt lgkmcnt(4)\n" \
  "v_mfma_f32_16x16x32_bf16 v[144:147], v[56:59], a[160:163], v[144:147]\n" \
  "s_mov_b32 s73, 589952\n" \
  "s_add_u32 m0, s72, 40960\n" \
  "v_mfma_f32_16x16x32_bf16 v[144:147], v[60:63], a[176:179], v[144:147]\n" \
  "ds_read_b64_tr_b16 v[48:49], v6 offset:8704\n" \
  "v_mfma_f32_16x16x32_bf16 v[148:151], v[60:63], a[180:183], v[148:151]\n" \
  "buffer_load_dwordx4 v12, s[36:39], s73 offen  lds\n" \
  "v_mfma_f32_16x16x32_bf16 v[148:151], v[56:59], a[164:167], v[148:151]\n" \
  "ds_read_b64_tr_b16 v[50:51], v6 offset:10752\n" \
  "v_mfma_f32_16x16x32_bf16 v[152:155], v[56:59], a[168:171], v[152:155]\n" \
  "v_mfma_f32_16x16x32_bf16 v[152:155], v[60:63], a[184:187], v[152:155]\n" \
  "ds_read_b64_tr_b16 v[52:53], v6 offset:12800\n" \
  "v_mfma_f32_16x16x32_bf16 v[156:159], v[60:63], a[188:191], v[156:159]\n" \
  "ds_read_b64_tr_b16 v[54:55], v6 offset:14848\n" \
  "v_mfma_f32_16x16x32_bf16 v[156:159], v[56:59], a[172:175], v[156:159]\n" \
  "s_waitcnt lgkmcnt(4)\n" \
  "v_mfma_f32_16x16x32_bf16 v[160:163], v[40:43], a[160:163], v[160:163]\n" \
  "s_add_u32 m0, s72, 45056\n" \
  "v_mfma_f32_16x16x32_bf16 v[160:163], v[44:47], a[176:179], v[160:163]\n" \
  "ds_read_b64_tr_b16 v[56:57], v7 offset:512\n" \
  "v_mfma_f32_16x16x32_bf16 v[164:167], v[44:47], a[180:183], v[164:167]\n" \
  "buffer_load_dwordx4 v13, s[36:39], s73 offen  lds\n" \
  "v_mfma_f32_16x16x32_bf16 v[164:167], v[40:43], a[164:167], v[164:167]\n" \
  "ds_read_b64_tr_b16 v[58:59], v7 offset:2560\n" \
  "v_mfma_f32_16x16x32_bf16 v[168:171], v[40:43], a[168:171], v[168:171]\n" \
  "v_mfma_f32_16x16x32_bf16 v[168:171], v[44:47], a[184:187], v[168:171]\n" \
  "ds_read_b64_tr_b16 v[60:61], v7 offset:4608\n" \
  "v_mfma_f32_16x16x32_bf16 v[172:175], v[44:47], a[188:191], v[172:175]\n" \
  "ds_read_b64_tr_b16 v[62:63], v7 offset:6656\n" \
  "v_mfma_f32_16x16x32_bf16 v[172:175], v[40:43], a[172:175], v[172:175]\n" \
  "s_waitcnt lgkmcnt(4)\n" \
  "v_mfma_f32_16x16x32_bf16 v[176:179], v[48:51], a[160:163], v[176:179]\n" \
  "s_add_u32 m0, s72, 49152\n" \
  "v_mfma_f32_16x16x32_bf16 v[176:179], v[52:55], a[176:179], v[176:179]\n" \
  "ds_read_b64_tr_b16 v[40:41], v7 offset:8704\n" \
  "v_mfma_f32_16x16x32_bf16 v[180:183], v[52:55], a[180:183], v[180:183]\n" \
  "buffer_load_dwordx4 v12, s[40:43], s73 offen  lds\n" \
  "v_mfma_f32_16x16x32_bf16 v[180:183], v[48:51], a[164:167], v[180:183]\n" \
  "ds_read_b64_tr_b16 v[42:43], v7 offset:10752\n" \
  "v_mfma_f32_16x16x32_bf16 v[184:187], v[48:51], a[168:171], v[184:187]\n" \
  "v_mfma_f32_16x16x32_bf16 v[184:187], v[52:55], a[184:187], v[184:187]\n" \
  "ds_read_b64_tr_b16 v[44:45], v7 offset:12800\n" \
  "v_mfma_f32_16x16x32_bf16 v[188:191], v[52:55], a[188:191], v[188:191]\n" \
  "ds_read_b64_tr_b16 v[46:47], v7 offset:14848\n" \
  "v_mfma_f32_16x16x32_bf16 v[188:191], v[48:51], a[172:175], v[188:191]\n" \
  "s_waitcnt vmcnt(13)\n" \
  "s_barrier\n" \
  "s_waitcnt lgkmcnt(4)\n" \
  "v_mfma_f32_16x16x32_bf16 v[192:195], v[56:59], a[160:163], v[192:195]\n" \
  "s_add_u32 m0, s72, 53248\n" \
  "s_lshl_b32 s89, s79, 2\n" \
  "v_mfma_f32_16x16x32_bf16 v[192:195], v[60:63], a[176:179], v[192:195]\n" \
  "ds_read_b64_tr_b16 v[48:49], v10 offset:36864\n" \
  "s_add_u32 s89, s89, s77\n" \
  "v_mfma_f32_16x16x32_bf16 v[196:199], v[60:63], a[180:183], v[196:199]\n" \
  "buffer_load_dwordx4 v13, s[40:43], s73 offen  lds\n" \
  "v_mfma_f32_16x16x32_bf16 v[196:199], v[56:59], a[164:167], v[196:199]\n" \
  "ds_read_b64_tr_b16 v[50:51], v10 offset:37376\n" \
  "s_mul_i32 s89, s89, 162\n" \
  "v_mfma_f32_16x16x32_bf16 v[200:203], v[56:59], a[168:171], v[200:203]\n" \
  "s_mul_i32 s90, s78, 5184\n" \
  "s_add_u32 s89, s89, s90\n" \
  "v_mfma_f32_16x16x32_bf16 v[200:203], v[60:63], a[184:187], v[200:203]\n" \
  "ds_read_b64_tr_b16 v[52:53], v10 offset:38912\n" \
  "s_lshl_b32 s89, s89, 7\n" \
  "v_mfma_f32_16x16x32_bf16 v[204:207], v[60:63], a[188:191], v[204:207]\n" \
  "ds_read_b64_tr_b16 v[54:55], v10 offset:39424\n" \
  "s_mul_i32 s90, s80, 41\n" \
  "v_mfma_f32_16x16x32_bf16 v[204:207], v[56:59], a[172:175], v[204:207]\n" \
  "v_add_u32 v32, s90, v11\n" \
  "s_waitcnt lgkmcnt(4)\n" \
  "v_mfma_f32_16x16x32_bf16 v[208:211], v[40:43], a[160:163], v[208:211]\n" \
  "v_min_u32 v32, 161, v32\n" \
  "v_lshlrev_b32 v32, 7, v32\n" \
  "v_mfma_f32_16x16x32_bf16 v[208:211], v[44:47], a[176:179], v[208:211]\n" \
  "ds_read_b64_tr_b16 v[56:57], v10 offset:37888\n" \
  "v_add_u32 v33, 23, v11\n" \
  "v_mfma_f32_16x16x32_bf16 v[212:215], v[44:47], a[180:183], v[212:215]\n" \
  "v_lshrrev_b32 v33, 6, v33\n" \
  "v_lshl_add_u32 v32, v33, 30, v32\n" \
  "v_mfma_f32_16x16x32_bf16 v[212:215], v[40:43], a[164:167], v[212:215]\n" \
  "ds_read_b64_tr_b16 v[58:59], v10 offset:38400\n" \
  "v_mfma_f32_16x16x32_bf16 v[216:219], v[40:43], a[168:171], v[216:219]\n" \
  "buffer_load_dword v37, v32, s[48:51], s89 offen offset:0 sc1\n" \
  "v_mfma_f32_16x16x32_bf16 v[216:219], v[44:47], a[184:187], v[216:219]\n" \
  "ds_read_b64_tr_b16 v[60:61], v10 offset:39936\n" \
  "v_mfma_f32_16x16x32_bf16 v[220:223], v[44:47], a[188:191], v[220:223]\n" \
  "ds_read_b64_tr_b16 v[62:63], v10 offset:40448\n" \
  "v_mfma_f32_16x16x32_bf16 v[220:223], v[40:43], a[172:175], v[220:223]\n" \
  "s_waitcnt lgkmcnt(4)\n" \
  "v_mfma_f32_16x16x32_bf16 v[224:227], v[48:51], a[192:195], v[224:227]\n" \
  "s_mov_b32 s73, 1179776\n" \
  "s_add_u32 m0, s72, 61440\n" \
  "v_mfma_f32_16x16x32_bf16 v[224:227], v[52:55], a[208:211], v[224:227]\n" \
  "ds_read_b64_tr_b16 v[40:41], v6 offset:20480\n" \
  "v_mfma_f32_16x16x32_bf16 v[228:231], v[52:55], a[212:215], v[228:231]\n" \
  "buffer_load_dwordx4 v12, s[36:39], s73 offen  lds\n" \
  "v_mfma_f32_16x16x32_bf16 v[228:231], v[48:51], a[196:199], v[228:231]\n" \
  "ds_read_b64_tr_b16 v[42:43], v6 offset:22528\n" \
  "v_mfma_f32_16x16x32_bf16 v[232:235], v[48:51], a[200:203], v[232:235]\n" \
  "v_mfma_f32_16x16x32_bf16 v[232:235], v[52:55], a[216:219], v[232:235]\n" \
  "ds_read_b64_tr_b16 v[44:45], v6 offset:24576\n" \
  "v_mfma_f32_16x16x32_bf16 v[236:239], v[52:55], a[220:223], v[236:239]\n" \
  "ds_read_b64_tr_b16 v[46:47], v6 offset:26624\n" \
  "v_mfma_f32_16x16x32_bf16 v[236:239], v[48:51], a[204:207], v[236:239]\n" \
  "s_waitcnt lgkmcnt(4)\n" \
  "v_mfma_f32_16x16x32_bf16 v[240:243], v[56:59], a[192:195], v[240:243]\n" \
  "s_add_u32 m0, s72, 65536\n" \
  "v_mfma_f32_16x16x32_bf16 v[240:243], v[60:63], a[208:211], v[240:243]\n" \
  "ds_read_b64_tr_b16 v[48:49], v6 offset:28672\n" \
  "v_mfma_f32_16x16x32_bf16 v[244:247], v[60:63], a[212:215], v[244:247]\n" \
  "buffer_load_dwordx4 v13, s[36:39], s73 offen  lds\n" \
  "v_mfma_f32_16x16x32_bf16 v[244:247], v[56:59], a[196:199], v[244:247]\n" \
  "ds_read_b64_tr_b16 v[50:51], v6 offset:30720\n" \
  "v_mfma_f32_16x16x32_bf16 v[248:251], v[56:59], a[200:203], v[248:251]\n" \
  "v_mfma_f32_16x16x32_bf16 v[248:251], v[60:63], a[216:219], v[248:251]\n" \
  "ds_read_b64_tr_b16 v[52:53], v6 offset:32768\n" \
  "v_mfma_f32_16x16x32_bf16 v[252:255], v[60:63], a[220:223], v[252:255]\n" \
  "ds_read_b64_tr_b16 v[54:55], v6 offset:34816\n" \
  "v_mfma_f32_16x16x32_bf16 v[252:255], v[56:59], a[204:207], v[252:255]\n" \
  "s_waitcnt lgkmcnt(4)\n" \
  "v_mfma_f32_16x16x32_bf16 v[96:99], v[40:43], a[192:195], v[96:99]\n" \
  "v_mfma_f32_16x16x32_bf16 v[96:99], v[44:47], a[208:211], v[96:99]\n" \
  "ds_read_b64_tr_b16 v[56:57], v7 offset:20480\n" \
  "v_mfma_f32_16x16x32_bf16 v[100:103], v[44:47], a[212:215], v[100:103]\n" \
  "v_mfma_f32_16x16x32_bf16 v[100:103], v[40:43], a[196:199], v[100:103]\n" \
  "ds_read_b64_tr_b16 v[58:59], v7 offset:22528\n" \
  "v_mfma_f32_16x16x32_bf16 v[104:107], v[40:43], a[200:203], v[104:107]\n" \
  "v_mfma_f32_16x16x32_bf16 v[104:107], v[44:47], a[216:219], v[104:107]\n" \
  "ds_read_b64_tr_b16 v[60:61], v7 offset:24576\n" \
  "v_mfma_f32_16x16x32_bf16 v[108:111], v[44:47], a[220:223], v[108:111]\n" \
  "PG4_%=:\n" \
  "s_cbranch_execz PG8_%=\n" \
  "ds_read_b64_tr_b16 v[62:63], v7 offset:26624\n" \
  "v_mfma_f32_16x16x32_bf16 v[108:111], v[40:43], a[204:207], v[108:111]\n" \
  "s_waitcnt lgkmcnt(4)\n" \
  "v_mfma_f32_16x16x32_bf16 v[112:115], v[48:51], a[192:195], v[112:115]\n" \
  "s_add_u32 m0, s72, 69632\n" \
  "v_mfma_f32_16x16x32_bf16 v[112:115], v[52:55], a[208:211], v[112:115]\n" \
  "ds_read_b64_tr_b16 v[40:41], v7 offset:28672\n" \
  "v_mfma_f32_16x16x32_bf16 v[116:119], v[52:55], a[212:215], v[116:119]\n" \
  "buffer_load_dwordx4 v12, s[40:43], s73 offen  lds\n" \
  "v_mfma_f32_16x16x32_bf16 v[116:119], v[48:51], a[196:199], v[116:119]\n" \
  "ds_read_b64_tr_b16 v[42:43], v7 offset:30720\n" \
  "v_mfma_f32_16x16x32_bf16 v[120:123], v[48:51], a[200:203], v[120:123]\n" \
  "v_mfma_f32_16x16x32_bf16 v[120:123], v[52:55], a[216:219], v[120:123]\n" \
  "ds_read_b64_tr_b16 v[44:45], v7 offset:32768\n" \
  "v_mfma_f32_16x16x32_bf16 v[124:127], v[52:55], a[220:223], v[124:127]\n" \
  "ds_read_b64_tr_b16 v[46:47], v7 offset:34816\n" \
  "v_mfma_f32_16x16x32_bf16 v[124:127], v[48:51], a[204:207], v[124:127]\n" \
  "s_waitcnt lgkmcnt(4)\n" \
  "v_mfma_f32_16x16x32_bf16 v[128:131], v[56:59], a[192:195], v[128:131]\n" \
  "s_add_u32 m0, s72, 73728\n" \
  "v_mfma_f32_16x16x32_bf16 v[128:131], v[60:63], a[208:211], v[128:131]\n" \
  "ds_read_b64_tr_b16 v[48:49], v6 offset:20992\n" \
  "v_mfma_f32_16x16x32_bf16 v[132:135], v[60:63], a[212:215], v[132:135]\n" \
  "buffer_load_dwordx4 v13, s[40:43], s73 offen  lds\n" \
  "v_mfma_f32_16x16x32_bf16 v[132:135], v[56:59], a[196:199], v[132:135]\n" \
  "ds_read_b64_tr_b16 v[50:51], v6 offset:23040\n" \
  "v_mfma_f32_16x16x32_bf16 v[136:139], v[56:59], a[200:203], v[136:139]\n" \
  "v_mfma_f32_16x16x32_bf16 v[136:139], v[60:63], a[216:219], v[136:139]\n" \
  "ds_read_b64_tr_b16 v[52:53], v6 offset:25088\n" \
  "v_mfma_f32_16x16x32_bf16 v[140:143], v[60:63], a[220:223], v[140:143]\n" \
  "ds_read_b64_tr_b16 v[54:55], v6 offset:27136\n" \
  "v_mfma_f32_16x16x32_bf16 v[140:143], v[56:59], a[204:207], v[140:143]\n" \
  "s_waitcnt lgkmcnt(4)\n" \
  "v_mfma_f32_16x16x32_bf16 v[144:147], v[40:43], a[192:195], v[144:147]\n" \
  "v_mfma_f32_16x16x32_bf16 v[144:147], v[44:47], a[208:211], v[144:147]\n" \
  "ds_read_b64_tr_b16 v[56:57], v6 offset:29184\n" \
  "v_mfma_f32_16x16x32_bf16 v[148:151], v[44:47], a[212:215], v[148:151]\n" \
  "v_mfma_f32_16x16x32_bf16 v[148:151], v[40:43], a[196:199], v[148:151]\n" \
  "ds_read_b64_tr_b16 v[58:59], v6 offset:31232\n" \
  "v_mfma_f32_16x16x32_bf16 v[152:155], v[40:43], a[200:203], v[152:155]\n" \
  "v_mfma_f32_16x16x32_bf16 v[152:155], v[44:47], a[216:219], v[152:155]\n" \
  "ds_read_b64_tr_b16 v[60:61], v6 offset:33280\n" \
  "v_mfma_f32_16x16x32_bf16 v[156:159], v[44:47], a[220:223], v[156:159]\n" \
  "ds_read_b64_tr_b16 v[62:63], v6 offset:35328\n" \
  "v_mfma_f32_16x16x32_bf16 v[156:159], v[40:43], a[204:207], v[156:159]\n" \
  "s_waitcnt lgkmcnt(4)\n" \
  "v_mfma_f32_16x16x32_bf16 v[160:163], v[48:51], a[192:195], v[160:163]\n" \
  "s_mov_b32 s73, 1769600\n" \
  "s_add_u32 m0, s72, 81920\n" \
  "v_mfma_f32_16x16x32_bf16 v[160:163], v[52:55], a[208:211], v[160:163]\n" \
  "ds_read_b64_tr_b16 v[40:41], v7 offset:20992\n" \
  "v_mfma_f32_16x16x32_bf16 v[164:167], v[52:55], a[212:215], v[164:167]\n" \
  "buffer_load_dwordx4 v12, s[36:39], s73 offen  lds\n" \
  "v_mfma_f32_16x16x32_bf16 v[164:167], v[48:51], a[196:199], v[164:167]\n" \
  "ds_read_b64_tr_b16 v[42:43], v7 offset:23040\n" \
  "v_mfma_f32_16x16x32_bf16 v[168:171], v[48:51], a[200:203], v[168:171]\n" \
  "v_mfma_f32_16x16x32_bf16 v[168:171], v[52:55], a[216:219], v[168:171]\n" \
  "ds_read_b64_tr_b16 v[44:45], v7 offset:25088\n" \
  "v_mfma_f32_16x16x32_bf16 v[172:175], v[52:55], a[220:223], v[172:175]\n" \
  "ds_read_b64_tr_b16 v[46:47], v7 offset:27136\n" \
  "v_mfma_f32_16x16x32_bf16 v[172:175], v[48:51], a[204:207], v[172:175]\n" \
  "s_waitcnt lgkmcnt(4)\n" \
  "v_mfma_f32_16x16x32_bf16 v[176:179], v[56:59], a[192:195], v[176:179]\n" \
  "s_add_u32 m0, s72, 86016\n" \
  "v_mfma_f32_16x16x32_bf16 v[176:179], v[60:63], a[208:211], v[176:179]\n" \
  "ds_read_b64_tr_b16 v[48:49], v7 offset:29184\n" \
  "v_mfma_f32_16x16x32_bf16 v[180:183], v[60:63], a[212:215], v[180:183]\n" \
  "buffer_load_dwordx4 v13, s[36:39], s73 offen  lds\n" \
  "v_mfma_f32_16x16x32_bf16 v[180:183], v[56:59], a[196:199], v[180:183]\n" \
  "ds_read_b64_tr_b16 v[50:51], v7 offset:31232\n" \
  "v_mfma_f32_16x16x32_bf16 v[184:187], v[56:59], a[200:203], v[184:187]\n" \
  "v_mfma_f32_16x16x32_bf16 v[184:187], v[60:63], a[216:219], v[184:187]\n" \
  "ds_read_b64_tr_b16 v[52:53], v7 offset:33280\n" \
  "v_mfma_f32_16x16x32_bf16 v[188:191], v[60:63], a[220:223], v[188:191]\n" \
  "ds_read_b64_tr_b16 v[54:55], v7 offset:35328\n" \
  "v_mfma_f32_16x16x32_bf16 v[188:191], v[56:59], a[204:207], v[188:191]\n" \
  "s_waitcnt vmcnt(15)\n" \
  "s_barrier\n" \
  "s_waitcnt lgkmcnt(4)\n" \
  "v_mfma_f32_16x16x32_bf16 v[192:195], v[40:43], a[192:195], v[192:195]\n" \
  "v_mfma_f32_16x16x32_bf16 v[192:195], v[44:47], a[208:211], v[192:195]\n" \
  "ds_read_b64_tr_b16 v[56:57], v8 offset:16384\n" \
  "v_mfma_f32_16x16x32_bf16 v[196:199], v[44:47], a[212:215], v[196:199]\n" \
  "v_mfma_f32_16x16x32_bf16 v[196:199], v[40:43], a[196:199], v[196:199]\n" \
  "ds_read_b64_tr_b16 v[58:59], v8 offset:16896\n" \
  "v_mfma_f32_16x16x32_bf16 v[200:203], v[40:43], a[200:203], v[200:203]\n" \
  "v_mfma_f32_16x16x32_bf16 v[200:203], v[44:47], a[216:219], v[200:203]\n" \
  "ds_read_b64_tr_b16 v[60:61], v8 offset:18432\n" \
  "v_mfma_f32_16x16x32_bf16 v[204:207], v[44:47], a[220:223], v[204:207]\n" \
  "ds_read_b64_tr_b16 v[62:63], v8 offset:18944\n" \
  "v_mfma_f32_16x16x32_bf16 v[204:207], v[40:43], a[204:207], v[204:207]\n" \
  "s_waitcnt lgkmcnt(4)\n" \
  "v_mfma_f32_16x16x32_bf16 v[208:211], v[48:51], a[192:195], v[208:211]\n" \
  "s_add_u32 m0, s72, 90112\n" \
  "v_mfma_f32_16x16x32_bf16 v[208:211], v[52:55], a[208:211], v[208:211]\n" \
  "ds_read_b64_tr_b16 v[40:41], v8 offset:17408\n" \
  "v_mfma_f32_16x16x32_bf16 v[212:215], v[52:55], a[212:215], v[212:215]\n" \
  "buffer_load_dwordx4 v12, s[40:43], s73 offen  lds\n" \
  "v_mfma_f32_16x16x32_bf16 v[212:215], v[48:51], a[196:199], v[212:215]\n" \
  "ds_read_b64_tr_b16 v[42:43], v8 offset:17920\n" \
  "v_mfma_f32_16x16x32_bf16 v[216:219], v[48:51], a[200:203], v[216:219]\n" \
  "v_mfma_f32_16x16x32_bf16 v[216:219], v[52:55], a[216:219], v[216:219]\n" \
  "ds_read_b64_tr_b16 v[44:45], v8 offset:19456\n" \
  "v_mfma_f32_16x16x32_bf16 v[220:223], v[52:55], a[220:223], v[220:223]\n" \
  "ds_read_b64_tr_b16 v[46:47], v8 offset:19968\n" \
  "v_mfma_f32_16x16x32_bf16 v[220:223], v[48:51], a[204:207], v[220:223]\n" \
  "s_waitcnt lgkmcnt(4)\n" \
  "v_mfma_f32_16x16x32_bf16 v[224:227], v[56:59], a[224:227], v[224:227]\n" \
  "s_add_u32 m0, s72, 94208\n" \
  "v_mfma_f32_16x16x32_bf16 v[224:227], v[60:63], a[240:243], v[224:227]\n" \
  "ds_read_b64_tr_b16 v[48:49], v2 offset:0\n" \
  "v_mfma_f32_16x16x32_bf16 v[228:231], v[60:63], a[244:247], v[228:231]\n" \
  "buffer_load_dwordx4 v13, s[40:43], s73 offen  lds\n" \
  "v_mfma_f32_16x16x32_bf16 v[228:231], v[56:59], a[228:231], v[228:231]\n" \
  "ds_read_b64_tr_b16 v[50:51], v2 offset:2048\n" \
  "v_mfma_f32_16x16x32_bf16 v[232:235], v[56:59], a[232:235], v[232:235]\n" \
  "v_mfma_f32_16x16x32_bf16 v[232:235], v[60:63], a[248:251], v[232:235]\n" \
  "ds_read_b64_tr_b16 v[52:53], v2 offset:4096\n" \
  "v_mfma_f32_16x16x32_bf16 v[236:239], v[60:63], a[252:255], v[236:239]\n" \
  "ds_read_b64_tr_b16 v[54:55], v2 offset:6144\n" \
  "v_mfma_f32_16x16x32_bf16 v[236:239], v[56:59], a[236:239], v[236:239]\n" \
  "s_waitcnt lgkmcnt(4)\n" \
  "v_mfma_f32_16x16x32_bf16 v[240:243], v[40:43], a[224:227], v[240:243]\n" \
  "v_mfma_f32_16x16x32_bf16 v[240:243], v[44:47], a[240:243], v[240:243]\n" \
  "ds_read_b64_tr_b16 v[56:57], v2 offset:8192\n" \
  "v_mfma_f32_16x16x32_bf16 v[244:247], v[44:47], a[244:247], v[244:247]\n" \
  "v_mfma_f32_16x16x32_bf16 v[244:247], v[40:43], a[228:231], v[244:247]\n" \
  "ds_read_b64_tr_b16 v[58:59], v2 offset:10240\n" \
  "v_mfma_f32_16x16x32_bf16 v[248:251], v[40:43], a[232:235], v[248:251]\n" \
  "v_mfma_f32_16x16x32_bf16 v[248:251], v[44:47], a[248:251], v[248:251]\n" \
  "ds_read_b64_tr_b16 v[60:61], v2 offset:12288\n" \
  "v_mfma_f32_16x16x32_bf16 v[252:255], v[44:47], a[252:255], v[252:255]\n" \
  "ds_read_b64_tr_b16 v[62:63], v2 offset:14336\n" \
  "v_mfma_f32_16x16x32_bf16 v[252:255], v[40:43], a[236:239], v[252:255]\n" \
  "s_waitcnt lgkmcnt(4)\n" \
  "v_mfma_f32_16x16x32_bf16 v[96:99], v[48:51], a[224:227], v[96:99]\n" \
  "v_mul_f32 v224, v224, v240\n" \
  "v_mul_f32 v225, v225, v241\n" \
  "v_mfma_f32_16x16x32_bf16 v[96:99], v[52:55], a[240:243], v[96:99]\n" \
  "ds_read_b64_tr_b16 v[40:41], v3 offset:0\n" \
  "v_mul_f32 v226, v226, v242\n" \
  "v_mfma_f32_16x16x32_bf16 v[100:103], v[52:55], a[244:247], v[100:103]\n" \
  "v_mul_f32 v227, v227, v243\n" \
  "v_cvt_pk_bf16_f32 v16, v224, v225\n" \
  "v_mfma_f32_16x16x32_bf16 v[100:103], v[48:51], a[228:231], v[100:103]\n" \
  "ds_read_b64_tr_b16 v[42:43], v3 offset:2048\n" \
  "v_cvt_pk_bf16_f32 v17, v226, v227\n" \
  "v_mfma_f32_16x16x32_bf16 v[104:107], v[48:51], a[232:235], v[104:107]\n" \
  "v_mul_f32 v228, v228, v244\n" \
  "v_mul_f32 v229, v229, v245\n" \
  "v_mfma_f32_16x16x32_bf16 v[104:107], v[52:55], a[248:251], v[104:107]\n" \
  "ds_read_b64_tr_b16 v[44:45], v3 offset:4096\n" \
  "v_mul_f32 v230, v230, v246\n" \
  "v_mfma_f32_16x16x32_bf16 v[108:111], v[52:55], a[252:255], v[108:111]\n" \
  "ds_read_b64_tr_b16 v[46:47], v3 offset:6144\n" \
  "v_mul_f32 v231, v231, v247\n" \
  "v_mfma_f32_16x16x32_bf16 v[108:111], v[48:51], a[236:239], v[108:111]\n" \
  "v_cvt_pk_bf16_f32 v18, v228, v229\n" \
  "s_waitcnt lgkmcnt(4)\n" \
  "v_mfma_f32_16x16x32_bf16 v[112:115], v[56:59], a[224:227], v[112:115]\n" \
  "v_cvt_pk_bf16_f32 v19, v230, v231\n" \
  "v_mul_f32 v232, v232, v248\n" \
  "v_mfma_f32_16x16x32_bf16 v[112:115], v[60:63], a[240:243], v[112:115]\n" \
  "ds_read_b64_tr_b16 v[48:49], v3 offset:8192\n" \
  "v_mul_f32 v233, v233, v249\n" \
  "v_mfma_f32_16x16x32_bf16 v[116:119], v[60:63], a[244:247], v[116:119]\n" \
  "v_mul_f32 v234, v234, v250\n" \
  "v_mul_f32 v235, v235, v251\n" \
  "v_mfma_f32_16x16x32_bf16 v[116:119], v[56:59], a[228:231], v[116:119]\n" \
  "ds_read_b64_tr_b16 v[50:51], v3 offset:10240\n" \
  "v_cvt_pk_bf16_f32 v20, v232, v233\n" \
  "v_mfma_f32_16x16x32_bf16 v[120:123], v[56:59], a[232:235], v[120:123]\n" \
  "v_cvt_pk_bf16_f32 v21, v234, v235\n" \
  "v_mul_f32 v236, v236, v252\n" \
  "v_mfma_f32_16x16x32_bf16 v[120:123], v[60:63], a[248:251], v[120:123]\n" \
  "ds_read_b64_tr_b16 v[52:53], v3 offset:12288\n" \
  "v_mul_f32 v237, v237, v253\n" \
  "v_mfma_f32_16x16x32_bf16 v[124:127], v[60:63], a[252:255], v[124:127]\n" \
  "ds_read_b64_tr_b16 v[54:55], v3 offset:14336\n" \
  "v_mul_f32 v238, v238, v254\n" \
  "v_mfma_f32_16x16x32_bf16 v[124:127], v[56:59], a[236:239], v[124:127]\n" \
  "v_mul_f32 v239, v239, v255\n" \
  "s_waitcnt lgkmcnt(4)\n" \
  "v_mfma_f32_16x16x32_bf16 v[128:131], v[40:43], a[224:227], v[128:131]\n" \
  "v_cvt_pk_bf16_f32 v22, v236, v237\n" \
  "v_cvt_pk_bf16_f32 v23, v238, v239\n" \
  "v_mfma_f32_16x16x32_bf16 v[128:131], v[44:47], a[240:243], v[128:131]\n" \
  "ds_read_b64_tr_b16 v[56:57], v2 offset:512\n" \
  "v_mul_f32 v96, v96, v112\n" \
  "v_mfma_f32_16x16x32_bf16 v[132:135], v[44:47], a[244:247], v[132:135]\n" \
  "v_mul_f32 v97, v97, v113\n" \
  "v_mul_f32 v98, v98, v114\n" \
  "v_mfma_f32_16x16x32_bf16 v[132:135], v[40:43], a[228:231], v[132:135]\n" \
  "ds_read_b64_tr_b16 v[58:59], v2 offset:2560\n" \
  "v_mul_f32 v99, v99, v115\n" \
  "v_mfma_f32_16x16x32_bf16 v[136:139], v[40:43], a[232:235], v[136:139]\n" \
  "v_cvt_pk_bf16_f32 v224, v96, v97\n" \
  "v_cvt_pk_bf16_f32 v225, v98, v99\n" \
  "v_mfma_f32_16x16x32_bf16 v[136:139], v[44:47], a[248:251], v[136:139]\n" \
  "ds_read_b64_tr_b16 v[60:61], v2 offset:4608\n" \
  "v_mul_f32 v100, v100, v116\n" \
  "v_mfma_f32_16x16x32_bf16 v[140:143], v[44:47], a[252:255], v[140:143]\n" \
  "ds_read_b64_tr_b16 v[62:63], v2 offset:6656\n" \
  "v_mul_f32 v101, v101, v117\n" \
  "v_mfma_f32_16x16x32_bf16 v[140:143], v[40:43], a[236:239], v[140:143]\n" \
  "v_mul_f32 v102, v102, v118\n" \
  "s_waitcnt lgkmcnt(4)\n" \
  "v_mfma_f32_16x16x32_bf16 v[144:147], v[48:51], a[224:227], v[144:147]\n" \
  "v_mul_f32 v103, v103, v119\n" \
  "v_cvt_pk_bf16_f32 v228, v100, v101\n" \
  "v_mfma_f32_16x16x32_bf16 v[144:147], v[52:55], a[240:243], v[144:147]\n" \
  "ds_read_b64_tr_b16 v[40:41], v2 offset:8704\n" \
  "v_cvt_pk_bf16_f32 v229, v102, v103\n" \
  "v_mfma_f32_16x16x32_bf16 v[148:151], v[52:55], a[244:247], v[148:151]\n" \
  "v_mul_f32 v104, v104, v120\n" \
  "v_mul_f32 v105, v105, v121\n" \
  "v_mfma_f32_16x16x32_bf16 v[148:151], v[48:51], a[228:231], v[148:151]\n" \
  "ds_read_b64_tr_b16 v[42:43], v2 offset:10752\n" \
  "v_mul_f32 v106, v106, v122\n" \
  "v_mfma_f32_16x16x32_bf16 v[152:155], v[48:51], a[232:235], v[152:155]\n" \
  "v_mul_f32 v107, v107, v123\n" \
  "v_cvt_pk_bf16_f32 v232, v104, v105\n" \
  "v_mfma_f32_16x16x32_bf16 v[152:155], v[52:55], a[248:251], v[152:155]\n" \
  "ds_read_b64_tr_b16 v[44:45], v2 offset:12800\n" \
  "v_cvt_pk_bf16_f32 v233, v106, v107\n" \
  "v_mfma_f32_16x16x32_bf16 v[156:159], v[52:55], a[252:255], v[156:159]\n" \
  "ds_read_b64_tr_b16 v[46:47], v2 offset:14848\n" \
  "v_mul_f32 v108, v108, v124\n" \
  "v_mfma_f32_16x16x32_bf16 v[156:159], v[48:51], a[236:239], v[156:159]\n" \
  "v_mul_f32 v109, v109, v125\n" \
  "s_waitcnt lgkmcnt(4)\n" \
  "v_mfma_f32_16x16x32_bf16 v[160:163], v[56:59], a[224:227], v[160:163]\n" \
  "v_mul_f32 v110, v110, v126\n" \
  "v_mul_f32 v111, v111, v127\n" \
  "v_mfma_f32_16x16x32_bf16 v[160:163], v[60:63], a[240:243], v[160:163]\n" \
  "ds_read_b64_tr_b16 v[48:49], v3 offset:512\n" \
  "v_cvt_pk_bf16_f32 v236, v108, v109\n" \
  "v_mfma_f32_16x16x32_bf16 v[164:167], v[60:63], a[244:247], v[164:167]\n" \
  "v_cvt_pk_bf16_f32 v237, v110, v111\n" \
  "v_mul_f32 v128, v128, v144\n" \
  "v_mfma_f32_16x16x32_bf16 v[164:167], v[56:59], a[228:231], v[164:167]\n" \
  "ds_read_b64_tr_b16 v[50:51], v3 offset:2560\n" \
  "v_mul_f32 v129, v129, v145\n" \
  "v_mfma_f32_16x16x32_bf16 v[168:171], v[56:59], a[232:235], v[168:171]\n" \
  "v_mul_f32 v130, v130, v146\n" \
  "v_mul_f32 v131, v131, v147\n" \
  "v_mfma_f32_16x16x32_bf16 v[168:171], v[60:63], a[248:251], v[168:171]\n" \
  "ds_read_b64_tr_b16 v[52:53], v3 offset:4608\n" \
  "v_cvt_pk_bf16_f32 v226, v128, v129\n" \
  "v_mfma_f32_16x16x32_bf16 v[172:175], v[60:63], a[252:255], v[172:175]\n" \
  "ds_read_b64_tr_b16 v[54:55], v3 offset:6656\n" \
  "v_cvt_pk_bf16_f32 v227, v130, v131\n" \
  "v_mfma_f32_16x16x32_bf16 v[172:175], v[56:59], a[236:239], v[172:175]\n" \
  "v_mul_f32 v132, v132, v148\n" \
  "s_waitcnt lgkmcnt(4)\n" \
  "v_mfma_f32_16x16x32_bf16 v[176:179], v[40:43], a[224:227], v[176:179]\n" \
  "v_mul_f32 v133, v133, v149\n" \
  "v_mul_f32 v134, v134, v150\n" \
  "v_mfma_f32_16x16x32_bf16 v[176:179], v[44:47], a[240:243], v[176:179]\n" \
  "ds_read_b64_tr_b16 v[56:57], v3 offset:8704\n" \
  "v_mul_f32 v135, v135, v151\n" \
  "v_mfma_f32_16x16x32_bf16 v[180:183], v[44:47], a[244:247], v[180:183]\n" \
  "v_cvt_pk_bf16_f32 v230, v132, v133\n" \
  "v_cvt_pk_bf16_f32 v231, v134, v135\n" \
  "v_mfma_f32_16x16x32_bf16 v[180:183], v[40:43], a[228:231], v[180:183]\n" \
  "ds_read_b64_tr_b16 v[58:59], v3 offset:10752\n" \
  "v_mul_f32 v136, v136, v152\n" \
  "v_mfma_f32_16x16x32_bf16 v[184:187], v[40:43], a[232:235], v[184:187]\n" \
  "v_mul_f32 v137, v137, v153\n" \
  "v_mul_f32 v138, v138, v154\n" \
  "v_mfma_f32_16x16x32_bf16 v[184:187], v[44:47], a[248:251], v[184:187]\n" \
  "ds_read_b64_tr_b16 v[60:61], v3 offset:12800\n" \
  "v_mul_f32 v139, v139, v155\n" \
  "v_mfma_f32_16x16x32_bf16 v[188:191], v[44:47], a[252:255], v[188:191]\n" \
  "ds_read_b64_tr_b16 v[62:63], v3 offset:14848\n" \
  "v_cvt_pk_bf16_f32 v234, v136, v137\n" \
  "v_mfma_f32_16x16x32_bf16 v[188:191], v[40:43], a[236:239], v[188:191]\n" \
  "s_waitcnt vmcnt(13)\n" \
  "s_barrier\n" \
  "s_waitcnt lgkmcnt(4)\n" \
  "v_mfma_f32_16x16x32_bf16 v[192:195], v[48:51], a[224:227], v[192:195]\n" \
  "v_cvt_pk_bf16_f32 v235, v138, v139\n" \
  "v_mul_f32 v140, v140, v156\n" \
  "v_mfma_f32_16x16x32_bf16 v[192:195], v[52:55], a[240:243], v[192:195]\n" \
  "ds_read_b64_tr_b16 v[40:41], v2 offset:20480\n" \
  "v_mul_f32 v141, v141, v157\n" \
  "v_mfma_f32_16x16x32_bf16 v[196:199], v[52:55], a[244:247], v[196:199]\n" \
  "v_mul_f32 v142, v142, v158\n" \
  "v_mul_f32 v143, v143, v159\n" \
  "v_mfma_f32_16x16x32_bf16 v[196:199], v[48:51], a[228:231], v[196:199]\n" \
  "ds_read_b64_tr_b16 v[42:43], v2 offset:22528\n" \
  "v_cvt_pk_bf16_f32 v238, v140, v141\n" \
  "v_mfma_f32_16x16x32_bf16 v[200:203], v[48:51], a[232:235], v[200:203]\n" \
  "v_cvt_pk_bf16_f32 v239, v142, v143\n" \
  "v_mul_f32 v160, v160, v176\n" \
  "v_mfma_f32_16x16x32_bf16 v[200:203], v[52:55], a[248:251], v[200:203]\n" \
  "ds_read_b64_tr_b16 v[44:45], v2 offset:24576\n" \
  "v_mul_f32 v161, v161, v177\n" \
  "v_mfma_f32_16x16x32_bf16 v[204:207], v[52:55], a[252:255], v[204:207]\n" \
  "ds_read_b64_tr_b16 v[46:47], v2 offset:26624\n" \
  "v_mul_f32 v162, v162, v178\n" \
  "v_mfma_f32_16x16x32_bf16 v[204:207], v[48:51], a[236:239], v[204:207]\n" \
  "v_mul_f32 v163, v163, v179\n" \
  "s_waitcnt lgkmcnt(4)\n" \
  "v_mfma_f32_16x16x32_bf16 v[208:211], v[56:59], a[224:227], v[208:211]\n" \
  "v_cvt_pk_bf16_f32 v240, v160, v161\n" \
  "v_cvt_pk_bf16_f32 v241, v162, v163\n" \
  "v_mfma_f32_16x16x32_bf16 v[208:211], v[60:63], a[240:243], v[208:211]\n" \
  "ds_read_b64_tr_b16 v[48:49], v2 offset:28672\n" \
  "v_mul_f32 v164, v164, v180\n" \
  "v_mfma_f32_16x16x32_bf16 v[212:215], v[60:63], a[244:247], v[212:215]\n" \
  "v_mul_f32 v165, v165, v181\n" \
  "v_mul_f32 v166, v166, v182\n" \
  "v_mfma_f32_16x16x32_bf16 v[212:215], v[56:59], a[228:231], v[212:215]\n" \
  "ds_read_b64_tr_b16 v[50:51], v2 offset:30720\n" \
  "v_mul_f32 v167, v167, v183\n" \
  "v_mfma_f32_16x16x32_bf16 v[216:219], v[56:59], a[232:235], v[216:219]\n" \
  "v_cvt_pk_bf16_f32 v244, v164, v165\n" \
  "v_cvt_pk_bf16_f32 v245, v166, v167\n" \
  "v_mfma_f32_16x16x32_bf16 v[216:219], v[60:63], a[248:251], v[216:219]\n" \
  "ds_read_b64_tr_b16 v[52:53], v2 offset:32768\n" \
  "v_mul_f32 v168, v168, v184\n" \
  "v_mfma_f32_16x16x32_bf16 v[220:223], v[60:63], a[252:255], v[220:223]\n" \
  "ds_read_b64_tr_b16 v[54:55], v2 offset:34816\n" \
  "v_mul_f32 v169, v169, v185\n" \
  "v_mfma_f32_16x16x32_bf16 v[220:223], v[56:59], a[236:239], v[220:223]\n" \
  "v_mul_f32 v170, v170, v186\n" \
  "s_waitcnt lgkmcnt(4)\n" \
  "v_mfma_f32_16x16x32_bf16 v[96:99], v[40:43], v[64:67], 0\n" \
  "v_mul_f32 v171, v171, v187\n" \
  "v_cvt_pk_bf16_f32 v248, v168, v169\n" \
  "v_mfma_f32_16x16x32_bf16 v[96:99], v[44:47], v[80:83], v[96:99]\n" \
  "ds_read_b64_tr_b16 v[56:57], v3 offset:20480\n" \
  "v_cvt_pk_bf16_f32 v249, v170, v171\n" \
  "v_mfma_f32_16x16x32_bf16 v[100:103], v[44:47], v[84:87], 0\n" \
  "v_mul_f32 v172, v172, v188\n" \
  "v_mul_f32 v173, v173, v189\n" \
  "v_mfma_f32_16x16x32_bf16 v[100:103], v[40:43], v[68:71], v[100:103]\n" \
  "ds_read_b64_tr_b16 v[58:59], v3 offset:22528\n" \
  "v_mul_f32 v174, v174, v190\n" \
  "v_mfma_f32_16x16x32_bf16 v[104:107], v[40:43], v[72:75], 0\n" \
  "v_mul_f32 v175, v175, v191\n" \
  "v_cvt_pk_bf16_f32 v252, v172, v173\n" \
  "v_mfma_f32_16x16x32_bf16 v[104:107], v[44:47], v[88:91], v[104:107]\n" \
  "ds_read_b64_tr_b16 v[60:61], v3 offset:24576\n" \
  "v_cvt_pk_bf16_f32 v253, v174, v175\n" \
  "v_mfma_f32_16x16x32_bf16 v[108:111], v[44:47], v[92:95], 0\n" \
  "ds_read_b64_tr_b16 v[62:63], v3 offset:26624\n" \
  "v_mul_f32 v192, v192, v208\n" \
  "v_mfma_f32_16x16x32_bf16 v[108:111], v[40:43], v[76:79], v[108:111]\n" \
  "v_mul_f32 v193, v193, v209\n" \
  "s_waitcnt lgkmcnt(4)\n" \
  "v_mfma_f32_16x16x32_bf16 v[112:115], v[48:51], v[64:67], 0\n" \
  "v_mul_f32 v194, v194, v210\n" \
  "v_mul_f32 v195, v195, v211\n" \
  "v_mfma_f32_16x16x32_bf16 v[112:115], v[52:55], v[80:83], v[112:115]\n" \
  "ds_read_b64_tr_b16 v[40:41], v3 offset:28672\n" \
  "v_cvt_pk_bf16_f32 v242, v192, v193\n" \
  "v_mfma_f32_16x16x32_bf16 v[116:119], v[52:55], v[84:87], 0\n" \
  "v_cvt_pk_bf16_f32 v243, v194, v195\n" \
  "v_mul_f32 v196, v196, v212\n" \
  "v_mfma_f32_16x16x32_bf16 v[116:119], v[48:51], v[68:71], v[116:119]\n" \
  "ds_read_b64_tr_b16 v[42:43], v3 offset:30720\n" \
  "v_mul_f32 v197, v197, v213\n" \
  "v_mfma_f32_16x16x32_bf16 v[120:123], v[48:51], v[72:75], 0\n" \
  "v_mul_f32 v198, v198, v214\n" \
  "v_mul_f32 v199, v199, v215\n" \
  "v_mfma_f32_16x16x32_bf16 v[120:123], v[52:55], v[88:91], v[120:123]\n" \
  "ds_read_b64_tr_b16 v[44:45], v3 offset:32768\n" \
  "v_cvt_pk_bf16_f32 v246, v196, v197\n" \
  "v_mfma_f32_16x16x32_bf16 v[124:127], v[52:55], v[92:95], 0\n" \
  "ds_read_b64_tr_b16 v[46:47], v3 offset:34816\n" \
  "v_cvt_pk_bf16_f32 v247, v198, v199\n" \
  "v_mfma_f32_16x16x32_bf16 v[124:127], v[48:51], v[76:79], v[124:127]\n" \
  "v_mul_f32 v200, v200, v216\n" \
  "s_waitcnt lgkmcnt(4)\n" \
  "v_mfma_f32_16x16x32_bf16 v[128:131], v[56:59], v[64:67], 0\n" \
  "v_mul_f32 v201, v201, v217\n" \
  "v_mul_f32 v202, v202, v218\n" \
  "v_mfma_f32_16x16x32_bf16 v[128:131], v[60:63], v[80:83], v[128:131]\n" \
  "ds_read_b64_tr_b16 v[48:49], v2 offset:20992\n" \
  "v_mul_f32 v203, v203, v219\n" \
  "v_mfma_f32_16x16x32_bf16 v[132:135], v[60:63], v[84:87], 0\n" \
  "v_cvt_pk_bf16_f32 v250, v200, v201\n" \
  "v_cvt_pk_bf16_f32 v251, v202, v203\n" \
  "v_mfma_f32_16x16x32_bf16 v[132:135], v[56:59], v[68:71], v[132:135]\n" \
  "ds_read_b64_tr_b16 v[50:51], v2 offset:23040\n" \
  "v_mul_f32 v204, v204, v220\n" \
  "v_mfma_f32_16x16x32_bf16 v[136:139], v[56:59], v[72:75], 0\n" \
  "v_mul_f32 v205, v205, v221\n" \
  "v_mul_f32 v206, v206, v222\n" \
  "v_mfma_f32_16x16x32_bf16 v[136:139], v[60:63], v[88:91], v[136:139]\n" \
  "ds_read_b64_tr_b16 v[52:53], v2 offset:25088\n" \
  "v_mul_f32 v207, v207, v223\n" \
  "v_mfma_f32_16x16x32_bf16 v[140:143], v[60:63], v[92:95], 0\n" \
  "ds_read_b64_tr_b16 v[54:55], v2 offset:27136\n" \
  "v_cvt_pk_bf16_f32 v254, v204, v205\n" \
  "v_mfma_f32_16x16x32_bf16 v[140:143], v[56:59], v[76:79], v[140:143]\n" \
  "v_cvt_pk_bf16_f32 v255, v206, v207\n" \
  "s_waitcnt lgkmcnt(4)\n" \
  "v_mfma_f32_16x16x32_bf16 v[144:147], v[40:43], v[64:67], 0\n" \
  "buffer_store_dwordx4 v[224:227], v24, s[56:59], s83 offen offset:0 sc1\n" \
  "v_mfma_f32_16x16x32_bf16 v[144:147], v[44:47], v[80:83], v[144:147]\n" \
  "ds_read_b64_tr_b16 v[56:57], v2 offset:29184\n" \
  "v_mfma_f32_16x16x32_bf16 v[148:151], v[44:47], v[84:87], 0\n" \
  "v_mfma_f32_16x16x32_bf16 v[148:151], v[40:43], v[68:71], v[148:151]\n" \
  "ds_read_b64_tr_b16 v[58:59], v2 offset:31232\n" \
  "v_mfma_f32_16x16x32_bf16 v[152:155], v[40:43], v[72:75], 0\n" \
  "v_mfma_f32_16x16x32_bf16 v[152:155], v[44:47], v[88:91], v[152:155]\n" \
  "ds_read_b64_tr_b16 v[60:61], v2 offset:33280\n" \
  "v_mfma_f32_16x16x32_bf16 v[156:159], v[44:47], v[92:95], 0\n" \
  "ds_read_b64_tr_b16 v[62:63], v2 offset:35328\n" \
  "v_mfma_f32_16x16x32_bf16 v[156:159], v[40:43], v[76:79], v[156:159]\n" \
  "s_waitcnt lgkmcnt(4)\n" \
  "v_mfma_f32_16x16x32_bf16 v[160:163], v[48:51], v[64:67], 0\n" \
  "s_lshr_b32 s82, s77, 1\n" \
  "s_lshl_b32 s82, s82, 12\n" \
  "v_mfma_f32_16x16x32_bf16 v[160:163], v[52:55], v[80:83], v[160:163]\n" \
  "ds_read_b64_tr_b16 v[40:41], v3 offset:20992\n" \
  "s_and_b32 s81, s77, 1\n" \
  "v_mfma_f32_16x16x32_bf16 v[164:167], v[52:55], v[84:87], 0\n" \
  "s_lshl_b32 s81, s81, 9\n" \
  "s_add_u32 s82, s82, s81\n" \
  "v_mfma_f32_16x16x32_bf16 v[164:167], v[48:51], v[68:71], v[164:167]\n" \
  "ds_read_b64_tr_b16 v[42:43], v3 offset:23040\n" \
  "s_add_u32 s82, s82, 65536\n" \
  "v_mfma_f32_16x16x32_bf16 v[168:171], v[48:51], v[72:75], 0\n" \
  "buffer_store_dwordx4 v[228:231], v24, s[56:59], s83 offen offset:1024 sc1\n" \
  "v_mfma_f32_16x16x32_bf16 v[168:171], v[52:55], v[88:91], v[168:171]\n" \
  "ds_read_b64_tr_b16 v[44:45], v3 offset:25088\n" \
  "v_mfma_f32_16x16x32_bf16 v[172:175], v[52:55], v[92:95], 0\n" \
  "ds_read_b64_tr_b16 v[46:47], v3 offset:27136\n" \
  "v_mfma_f32_16x16x32_bf16 v[172:175], v[48:51], v[76:79], v[172:175]\n" \
  "s_waitcnt lgkmcnt(4)\n" \
  "v_mfma_f32_16x16x32_bf16 v[176:179], v[56:59], v[64:67], 0\n" \
  "v_mfma_f32_16x16x32_bf16 v[176:179], v[60:63], v[80:83], v[176:179]\n" \
  "ds_read_b64_tr_b16 v[48:49], v3 offset:29184\n" \
  "v_mfma_f32_16x16x32_bf16 v[180:183], v[60:63], v[84:87], 0\n" \
  "v_mfma_f32_16x16x32_bf16 v[180:183], v[56:59], v[68:71], v[180:183]\n" \
  "ds_read_b64_tr_b16 v[50:51], v3 offset:31232\n" \
  "v_mfma_f32_16x16x32_bf16 v[184:187], v[56:59], v[72:75], 0\n" \
  "v_mfma_f32_16x16x32_bf16 v[184:187], v[60:63], v[88:91], v[184:187]\n" \
  "ds_read_b64_tr_b16 v[52:53], v3 offset:33280\n" \
  "v_mfma_f32_16x16x32_bf16 v[188:191], v[60:63], v[92:95], 0\n" \
  "ds_read_b64_tr_b16 v[54:55], v3 offset:35328\n" \
  "v_mfma_f32_16x16x32_bf16 v[188:191], v[56:59], v[76:79], v[188:191]\n" \
  "s_waitcnt vmcnt(11)\n" \
  "s_barrier\n" \
  "s_waitcnt lgkmcnt(4)\n" \
  "v_mfma_f32_16x16x32_bf16 v[192:195], v[40:43], v[64:67], 0\n" \
  "buffer_store_dwordx4 v[232:235], v24, s[56:59], s83 offen offset:2048 sc1\n" \
  "v_mfma_f32_16x16x32_bf16 v[192:195], v[44:47], v[80:83], v[192:195]\n" \
  "ds_read_b64_tr_b16 v[56:57], v2 offset:40960\n" \
  "v_mfma_f32_16x16x32_bf16 v[196:199], v[44:47], v[84:87], 0\n" \
  "v_mfma_f32_16x16x32_bf16 v[196:199], v[40:43], v[68:71], v[196:199]\n" \
  "ds_read_b64_tr_b16 v[58:59], v2 offset:43008\n" \
  "v_mfma_f32_16x16x32_bf16 v[200:203], v[40:43], v[72:75], 0\n" \
  "v_mfma_f32_16x16x32_bf16 v[200:203], v[44:47], v[88:91], v[200:203]\n" \
  "ds_read_b64_tr_b16 v[60:61], v2 offset:45056\n" \
  "v_mfma_f32_16x16x32_bf16 v[204:207], v[44:47], v[92:95], 0\n" \
  "ds_read_b64_tr_b16 v[62:63], v2 offset:47104\n" \
  "v_mfma_f32_16x16x32_bf16 v[204:207], v[40:43], v[76:79], v[204:207]\n" \
  "s_waitcnt lgkmcnt(4)\n" \
  "v_mfma_f32_16x16x32_bf16 v[208:211], v[48:51], v[64:67], 0\n" \
  "buffer_store_dwordx4 v[236:239], v24, s[56:59], s83 offen offset:3072 sc1\n" \
  "v_mfma_f32_16x16x32_bf16 v[208:211], v[52:55], v[80:83], v[208:211]\n" \
  "ds_read_b64_tr_b16 v[40:41], v2 offset:49152\n" \
  "v_mfma_f32_16x16x32_bf16 v[212:215], v[52:55], v[84:87], 0\n" \
  "v_mfma_f32_16x16x32_bf16 v[212:215], v[48:51], v[68:71], v[212:215]\n" \
  "ds_read_b64_tr_b16 v[42:43], v2 offset:51200\n" \
  "v_mfma_f32_16x16x32_bf16 v[216:219], v[48:51], v[72:75], 0\n" \
  "v_mfma_f32_16x16x32_bf16 v[216:219], v[52:55], v[88:91], v[216:219]\n" \
  "ds_read_b64_tr_b16 v[44:45], v2 offset:53248\n" \
  "v_mfma_f32_16x16x32_bf16 v[220:223], v[52:55], v[92:95], 0\n" \
  "ds_read_b64_tr_b16 v[46:47], v2 offset:55296\n" \
  "v_mfma_f32_16x16x32_bf16 v[220:223], v[48:51], v[76:79], v[220:223]\n" \
  "s_waitcnt lgkmcnt(4)\n" \
  "v_mfma_f32_16x16x32_bf16 v[96:99], v[56:59], a[0:3], v[96:99]\n" \
  "s_mov_b32 s73, 2359424\n" \
  "s_add_u32 m0, s72, 102400\n" \
  "v_mfma_f32_16x16x32_bf16 v[96:99], v[60:63], a[16:19], v[96:99]\n" \
  "ds_read_b64_tr_b16 v[48:49], v3 offset:40960\n" \
  "v_mfma_f32_16x16x32_bf16 v[100:103], v[60:63], a[20:23], v[100:103]\n" \
  "buffer_load_dwordx4 v12, s[36:39], s73 offen  lds\n" \
  "v_mfma_f32_16x16x32_bf16 v[100:103], v[56:59], a[4:7], v[100:103]\n" \
  "ds_read_b64_tr_b16 v[50:51], v3 offset:43008\n" \
  "v_mfma_f32_16x16x32_bf16 v[104:107], v[56:59], a[8:11], v[104:107]\n" \
  "v_mfma_f32_16x16x32_bf16 v[104:107], v[60:63], a[24:27], v[104:107]\n" \
  "ds_read_b64_tr_b16 v[52:53], v3 offset:45056\n" \
  "v_mfma_f32_16x16x32_bf16 v[108:111], v[60:63], a[28:31], v[108:111]\n" \
  "ds_read_b64_tr_b16 v[54:55], v3 offset:47104\n" \
  "v_mfma_f32_16x16x32_bf16 v[108:111], v[56:59], a[12:15], v[108:111]\n" \
  "s_waitcnt lgkmcnt(4)\n" \
  "v_mfma_f32_16x16x32_bf16 v[112:115], v[40:43], a[0:3], v[112:115]\n" \
  "buffer_store_dwordx4 v[240:243], v25, s[56:59], s83 offen offset:0 sc1\n" \
  "v_mfma_f32_16x16x32_bf16 v[112:115], v[44:47], a[16:19], v[112:115]\n" \
  "ds_read_b64_tr_b16 v[56:57], v3 offset:49152\n" \
  "s_add_u32 m0, s72, 106496\n" \
  "v_mfma_f32_16x16x32_bf16 v[116:119], v[44:47], a[20:23], v[116:119]\n" \
  "buffer_load_dwordx4 v13, s[36:39], s73 offen  lds\n" \
  "v_mfma_f32_16x16x32_bf16 v[116:119], v[40:43], a[4:7], v[116:119]\n" \
  "ds_read_b64_tr_b16 v[58:59], v3 offset:51200\n" \
  "v_mfma_f32_16x16x32_bf16 v[120:123], v[40:43], a[8:11], v[120:123]\n" \
  "v_mfma_f32_16x16x32_bf16 v[120:123], v[44:47], a[24:27], v[120:123]\n" \
  "ds_read_b64_tr_b16 v[60:61], v3 offset:53248\n" \
  "v_mfma_f32_16x16x32_bf16 v[124:127], v[44:47], a[28:31], v[124:127]\n" \
  "ds_read_b64_tr_b16 v[62:63], v3 offset:55296\n" \
  "v_mfma_f32_16x16x32_bf16 v[124:127], v[40:43], a[12:15], v[124:127]\n" \
  "s_waitcnt lgkmcnt(4)\n" \
  "v_mfma_f32_16x16x32_bf16 v[128:131], v[48:51], a[0:3], v[128:131]\n" \
  "buffer_store_dwordx4 v[244:247], v25, s[56:59], s83 offen offset:1024 sc1\n" \
  "v_mfma_f32_16x16x32_bf16 v[128:131], v[52:55], a[16:19], v[128:131]\n" \
  "ds_read_b64_tr_b16 v[40:41], v2 offset:41472\n" \
  "s_add_u32 m0, s72, 110592\n" \
  "v_mfma_f32_16x16x32_bf16 v[132:135], v[52:55], a[20:23], v[132:135]\n" \
  "buffer_load_dwordx4 v12, s[40:43], s73 offen  lds\n" \
  "v_mfma_f32_16x16x32_bf16 v[132:135], v[48:51], a[4:7], v[132:135]\n" \
  "ds_read_b64_tr_b16 v[42:43], v2 offset:43520\n" \
  "v_mfma_f32_16x16x32_bf16 v[136:139], v[48:51], a[8:11], v[136:139]\n" \
  "v_mfma_f32_16x16x32_bf16 v[136:139], v[52:55], a[24:27], v[136:139]\n" \
  "ds_read_b64_tr_b16 v[44:45], v2 offset:45568\n" \
  "v_mfma_f32_16x16x32_bf16 v[140:143], v[52:55], a[28:31], v[140:143]\n" \
  "ds_read_b64_tr_b16 v[46:47], v2 offset:47616\n" \
  "v_mfma_f32_16x16x32_bf16 v[140:143], v[48:51], a[12:15], v[140:143]\n" \
  "s_waitcnt lgkmcnt(4)\n" \
  "v_mfma_f32_16x16x32_bf16 v[144:147], v[56:59], a[0:3], v[144:147]\n" \
  "s_add_u32 m0, s72, 114688\n" \
  "v_mfma_f32_16x16x32_bf16 v[144:147], v[60:63], a[16:19], v[144:147]\n" \
  "ds_read_b64_tr_b16 v[48:49], v2 offset:49664\n" \
  "v_mfma_f32_16x16x32_bf16 v[148:151], v[60:63], a[20:23], v[148:151]\n" \
  "buffer_load_dwordx4 v13, s[40:43], s73 offen  lds\n" \
  "v_mfma_f32_16x16x32_bf16 v[148:151], v[56:59], a[4:7], v[148:151]\n" \
  "ds_read_b64_tr_b16 v[50:51], v2 offset:51712\n" \
  "v_mfma_f32_16x16x32_bf16 v[152:155], v[56:59], a[8:11], v[152:155]\n" \
  "v_mfma_f32_16x16x32_bf16 v[152:155], v[60:63], a[24:27], v[152:155]\n" \
  "ds_read_b64_tr_b16 v[52:53], v2 offset:53760\n" \
  "v_mfma_f32_16x16x32_bf16 v[156:159], v[60:63], a[28:31], v[156:159]\n" \
  "ds_read_b64_tr_b16 v[54:55], v2 offset:55808\n" \
  "v_mfma_f32_16x16x32_bf16 v[156:159], v[56:59], a[12:15], v[156:159]\n" \
  "s_waitcnt lgkmcnt(4)\n" \
  "v_mfma_f32_16x16x32_bf16 v[160:163], v[40:43], a[0:3], v[160:163]\n" \
  "buffer_store_dwordx4 v[248:251], v25, s[56:59], s83 offen offset:2048 sc1\n" \
  "v_mfma_f32_16x16x32_bf16 v[160:163], v[44:47], a[16:19], v[160:163]\n" \
  "ds_read_b64_tr_b16 v[56:57], v3 offset:41472\n" \
  "v_mfma_f32_16x16x32_bf16 v[164:167], v[44:47], a[20:23], v[164:167]\n" \
  "v_mfma_f32_16x16x32_bf16 v[164:167], v[40:43], a[4:7], v[164:167]\n" \
  "ds_read_b64_tr_b16 v[58:59], v3 offset:43520\n" \
  "v_mfma_f32_16x16x32_bf16 v[168:171], v[40:43], a[8:11], v[168:171]\n" \
  "v_mfma_f32_16x16x32_bf16 v[168:171], v[44:47], a[24:27], v[168:171]\n" \
  "ds_read_b64_tr_b16 v[60:61], v3 offset:45568\n" \
  "v_mfma_f32_16x16x32_bf16 v[172:175], v[44:47], a[28:31], v[172:175]\n" \
  "ds_read_b64_tr_b16 v[62:63], v3 offset:47616\n" \
  "v_mfma_f32_16x16x32_bf16 v[172:175], v[40:43], a[12:15], v[172:175]\n" \
  "s_waitcnt lgkmcnt(4)\n" \
  "v_mfma_f32_16x16x32_bf16 v[176:179], v[48:51], a[0:3], v[176:179]\n" \
  "s_mov_b32 s73, 2949248\n" \
  "s_add_u32 m0, s72, 122880\n" \
  "v_mfma_f32_16x16x32_bf16 v[176:179], v[52:55], a[16:19], v[176:179]\n" \
  "ds_read_b64_tr_b16 v[40:41], v3 offset:49664\n" \
  "v_mfma_f32_16x16x32_bf16 v[180:183], v[52:55], a[20:23], v[180:183]\n" \
  "buffer_load_dwordx4 v12, s[36:39], s73 offen  lds\n" \
  "v_mfma_f32_16x16x32_bf16 v[180:183], v[48:51], a[4:7], v[180:183]\n" \
  "ds_read_b64_tr_b16 v[42:43], v3 offset:51712\n" \
  "v_mfma_f32_16x16x32_bf16 v[184:187], v[48:51], a[8:11], v[184:187]\n" \
  "v_mfma_f32_16x16x32_bf16 v[184:187], v[52:55], a[24:27], v[184:187]\n" \
  "ds_read_b64_tr_b16 v[44:45], v3 offset:53760\n" \
  "v_mfma_f32_16x16x32_bf16 v[188:191], v[52:55], a[28:31], v[188:191]\n" \
  "ds_read_b64_tr_b16 v[46:47], v3 offset:55808\n" \
  "v_mfma_f32_16x16x32_bf16 v[188:191], v[48:51], a[12:15], v[188:191]\n" \
  "s_waitcnt vmcnt(16)\n" \
  "s_barrier\n" \
  "s_waitcnt lgkmcnt(4)\n" \
  "v_mfma_f32_16x16x32_bf16 v[192:195], v[56:59], a[0:3], v[192:195]\n" \
  "buffer_store_dwordx4 v[252:255], v25, s[56:59], s83 offen offset:3072 sc1\n" \
  "PG5_%=:\n" \
  "s_cbranch_execz PG9_%=\n" \
  "v_mfma_f32_16x16x32_bf16 v[192:195], v[60:63], a[16:19], v[192:195]\n" \
  "ds_read_b64_tr_b16 v[48:49], v4 offset:0\n" \
  "s_add_u32 m0, s72, 126976\n" \
  "v_mfma_f32_16x16x32_bf16 v[196:199], v[60:63], a[20:23], v[196:199]\n" \
  "buffer_load_dwordx4 v13, s[36:39], s73 offen  lds\n" \
  "v_mfma_f32_16x16x32_bf16 v[196:199], v[56:59], a[4:7], v[196:199]\n" \
  "ds_read_b64_tr_b16 v[50:51], v4 offset:2048\n" \
  "v_mfma_f32_16x16x32_bf16 v[200:203], v[56:59], a[8:11], v[200:203]\n" \
  "v_mfma_f32_16x16x32_bf16 v[200:203], v[60:63], a[24:27], v[200:203]\n" \
  "ds_read_b64_tr_b16 v[52:53], v4 offset:4096\n" \
  "v_mfma_f32_16x16x32_bf16 v[204:207], v[60:63], a[28:31], v[204:207]\n" \
  "ds_read_b64_tr_b16 v[54:55], v4 offset:6144\n" \
  "v_mfma_f32_16x16x32_bf16 v[204:207], v[56:59], a[12:15], v[204:207]\n" \
  "s_waitcnt lgkmcnt(4)\n" \
  "v_mfma_f32_16x16x32_bf16 v[208:211], v[40:43], a[0:3], v[208:211]\n" \
  "buffer_store_dwordx2 v[16:17], v8, s[56:59], s82 offen offset:0 sc1\n" \
  "v_mfma_f32_16x16x32_bf16 v[208:211], v[44:47], a[16:19], v[208:211]\n" \
  "ds_read_b64_tr_b16 v[56:57], v4 offset:8192\n" \
  "s_add_u32 m0, s72, 131072\n" \
  "v_mfma_f32_16x16x32_bf16 v[212:215], v[44:47], a[20:23], v[212:215]\n" \
  "buffer_load_dwordx4 v12, s[40:43], s73 offen  lds\n" \
  "v_mfma_f32_16x16x32_bf16 v[212:215], v[40:43], a[4:7], v[212:215]\n" \
  "ds_read_b64_tr_b16 v[58:59], v4 offset:10240\n" \
  "v_mfma_f32_16x16x32_bf16 v[216:219], v[40:43], a[8:11], v[216:219]\n" \
  "v_mfma_f32_16x16x32_bf16 v[216:219], v[44:47], a[24:27], v[216:219]\n" \
  "ds_read_b64_tr_b16 v[60:61], v4 offset:12288\n" \
  "v_mfma_f32_16x16x32_bf16 v[220:223], v[44:47], a[28:31], v[220:223]\n" \
  "ds_read_b64_tr_b16 v[62:63], v4 offset:14336\n" \
  "v_mfma_f32_16x16x32_bf16 v[220:223], v[40:43], a[12:15], v[220:223]\n" \
  "s_waitcnt lgkmcnt(4)\n" \
  "v_mfma_f32_16x16x32_bf16 v[96:99], v[48:51], a[32:35], v[96:99]\n" \
  "s_add_u32 m0, s72, 135168\n" \
  "v_accvgpr_write_b32 a0, v224\n" \
  "v_mfma_f32_16x16x32_bf16 v[96:99], v[52:55], a[48:51], v[96:99]\n" \
  "ds_read_b64_tr_b16 v[40:41], v5 offset:0\n" \
  "v_accvgpr_write_b32 a1, v225\n" \
  "v_mfma_f32_16x16x32_bf16 v[100:103], v[52:55], a[52:55], v[100:103]\n" \
  "buffer_load_dwordx4 v13, s[40:43], s73 offen  lds\n" \
  "v_mfma_f32_16x16x32_bf16 v[100:103], v[48:51], a[36:39], v[100:103]\n" \
  "ds_read_b64_tr_b16 v[42:43], v5 offset:2048\n" \
  "v_accvgpr_write_b32 a2, v226\n" \
  "v_mfma_f32_16x16x32_bf16 v[104:107], v[48:51], a[40:43], v[104:107]\n" \
  "v_accvgpr_write_b32 a3, v227\n" \
  "v_accvgpr_write_b32 a4, v228\n" \
  "v_mfma_f32_16x16x32_bf16 v[104:107], v[52:55], a[56:59], v[104:107]\n" \
  "ds_read_b64_tr_b16 v[44:45], v5 offset:4096\n" \
  "v_accvgpr_write_b32 a5, v229\n" \
  "v_mfma_f32_16x16x32_bf16 v[108:111], v[52:55], a[60:63], v[108:111]\n" \
  "ds_read_b64_tr_b16 v[46:47], v5 offset:6144\n" \
  "v_accvgpr_write_b32 a6, v230\n" \
  "v_mfma_f32_16x16x32_bf16 v[108:111], v[48:51], a[44:47], v[108:111]\n" \
  "v_accvgpr_write_b32 a7, v231\n" \
  "s_waitcnt lgkmcnt(4)\n" \
  "v_mfma_f32_16x16x32_bf16 v[112:115], v[56:59], a[32:35], v[112:115]\n" \
  "buffer_store_dwordx2 v[18:19], v8, s[56:59], s82 offen offset:1024 sc1\n" \
  "v_mfma_f32_16x16x32_bf16 v[112:115], v[60:63], a[48:51], v[112:115]\n" \
  "ds_read_b64_tr_b16 v[48:49], v5 offset:8192\n" \
  "v_accvgpr_write_b32 a8, v232\n" \
  "v_mfma_f32_16x16x32_bf16 v[116:119], v[60:63], a[52:55], v[116:119]\n" \
  "v_accvgpr_write_b32 a9, v233\n" \
  "v_accvgpr_write_b32 a10, v234\n" \
  "v_mfma_f32_16x16x32_bf16 v[116:119], v[56:59], a[36:39], v[116:119]\n" \
  "ds_read_b64_tr_b16 v[50:51], v5 offset:10240\n" \
  "v_accvgpr_write_b32 a11, v235\n" \
  "v_mfma_f32_16x16x32_bf16 v[120:123], v[56:59], a[40:43], v[120:123]\n" \
  "v_accvgpr_write_b32 a12, v236\n" \
  "v_accvgpr_write_b32 a13, v237\n" \
  "v_mfma_f32_16x16x32_bf16 v[120:123], v[60:63], a[56:59], v[120:123]\n" \
  "ds_read_b64_tr_b16 v[52:53], v5 offset:12288\n" \
  "v_accvgpr_write_b32 a14, v238\n" \
  "v_mfma_f32_16x16x32_bf16 v[124:127], v[60:63], a[60:63], v[124:127]\n" \
  "ds_read_b64_tr_b16 v[54:55], v5 offset:14336\n" \
  "v_accvgpr_write_b32 a15, v239\n" \
  "v_mfma_f32_16x16x32_bf16 v[124:127], v[56:59], a[44:47], v[124:127]\n" \
  "v_accvgpr_write_b32 a16, v240\n" \
  "s_waitcnt lgkmcnt(4)\n" \
  "v_mfma_f32_16x16x32_bf16 v[128:131], v[40:43], a[32:35], v[128:131]\n" \
  "buffer_store_dwordx2 v[20:21], v8, s[56:59], s82 offen offset:2048 sc1\n" \
  "v_mfma_f32_16x16x32_bf16 v[128:131], v[44:47], a[48:51], v[128:131]\n" \
  "ds_read_b64_tr_b16 v[56:57], v4 offset:512\n" \
  "s_mov_b32 s73, 3539072\n" \
  "v_mfma_f32_16x16x32_bf16 v[132:135], v[44:47], a[52:55], v[132:135]\n" \
  "s_add_u32 m0, s72, 143360\n" \
  "v_accvgpr_write_b32 a17, v241\n" \
  "v_mfma_f32_16x16x32_bf16 v[132:135], v[40:43], a[36:39], v[132:135]\n" \
  "ds_read_b64_tr_b16 v[58:59], v4 offset:2560\n" \
  "v_accvgpr_write_b32 a18, v242\n" \
  "v_mfma_f32_16x16x32_bf16 v[136:139], v[40:43], a[40:43], v[136:139]\n" \
  "buffer_load_dwordx4 v12, s[36:39], s73 offen  lds\n" \
  "v_mfma_f32_16x16x32_bf16 v[136:139], v[44:47], a[56:59], v[136:139]\n" \
  "ds_read_b64_tr_b16 v[60:61], v4 offset:4608\n" \
  "v_accvgpr_write_b32 a19, v243\n" \
  "v_mfma_f32_16x16x32_bf16 v[140:143], v[44:47], a[60:63], v[140:143]\n" \
  "ds_read_b64_tr_b16 v[62:63], v4 offset:6656\n" \
  "v_accvgpr_write_b32 a20, v244\n" \
  "v_mfma_f32_16x16x32_bf16 v[140:143], v[40:43], a[44:47], v[140:143]\n" \
  "v_accvgpr_write_b32 a21, v245\n" \
  "s_waitcnt lgkmcnt(4)\n" \
  "v_mfma_f32_16x16x32_bf16 v[144:147], v[48:51], a[32:35], v[144:147]\n" \
  "s_add_u32 m0, s72, 147456\n" \
  "v_accvgpr_write_b32 a22, v246\n" \
  "v_mfma_f32_16x16x32_bf16 v[144:147], v[52:55], a[48:51], v[144:147]\n" \
  "ds_read_b64_tr_b16 v[40:41], v4 offset:8704\n" \
  "v_accvgpr_write_b32 a23, v247\n" \
  "v_mfma_f32_16x16x32_bf16 v[148:151], v[52:55], a[52:55], v[148:151]\n" \
  "buffer_load_dwordx4 v13, s[36:39], s73 offen  lds\n" \
  "v_mfma_f32_16x16x32_bf16 v[148:151], v[48:51], a[36:39], v[148:151]\n" \
  "ds_read_b64_tr_b16 v[42:43], v4 offset:10752\n" \
  "v_accvgpr_write_b32 a24, v248\n" \
  "v_mfma_f32_16x16x32_bf16 v[152:155], v[48:51], a[40:43], v[152:155]\n" \
  "v_accvgpr_write_b32 a25, v249\n" \
  "v_accvgpr_write_b32 a26, v250\n" \
  "v_mfma_f32_16x16x32_bf16 v[152:155], v[52:55], a[56:59], v[152:155]\n" \
  "ds_read_b64_tr_b16 v[44:45], v4 offset:12800\n" \
  "v_accvgpr_write_b32 a27, v251\n" \
  "v_mfma_f32_16x16x32_bf16 v[156:159], v[52:55], a[60:63], v[156:159]\n" \
  "ds_read_b64_tr_b16 v[46:47], v4 offset:14848\n" \
  "v_accvgpr_write_b32 a28, v252\n" \
  "v_mfma_f32_16x16x32_bf16 v[156:159], v[48:51], a[44:47], v[156:159]\n" \
  "v_accvgpr_write_b32 a29, v253\n" \
  "s_waitcnt lgkmcnt(4)\n" \
  "v_mfma_f32_16x16x32_bf16 v[160:163], v[56:59], a[32:35], v[160:163]\n" \
  "buffer_store_dwordx2 v[22:23], v8, s[56:59], s82 offen offset:3072 sc1\n" \
  "v_mfma_f32_16x16x32_bf16 v[160:163], v[60:63], a[48:51], v[160:163]\n" \
  "ds_read_b64_tr_b16 v[48:49], v5 offset:512\n" \
  "s_add_u32 m0, s72, 151552\n" \
  "v_mfma_f32_16x16x32_bf16 v[164:167], v[60:63], a[52:55], v[164:167]\n" \
  "buffer_load_dwordx4 v12, s[40:43], s73 offen  lds\n" \
  "v_mfma_f32_16x16x32_bf16 v[164:167], v[56:59], a[36:39], v[164:167]\n" \
  "ds_read_b64_tr_b16 v[50:51], v5 offset:2560\n" \
  "v_accvgpr_write_b32 a30, v254\n" \
  "v_mfma_f32_16x16x32_bf16 v[168:171], v[56:59], a[40:43], v[168:171]\n" \
  "v_accvgpr_write_b32 a31, v255\n" \
  "v_mfma_f32_16x16x32_bf16 v[168:171], v[60:63], a[56:59], v[168:171]\n" \
  "ds_read_b64_tr_b16 v[52:53], v5 offset:4608\n" \
  "v_mfma_f32_16x16x32_bf16 v[172:175], v[60:63], a[60:63], v[172:175]\n" \
  "ds_read_b64_tr_b16 v[54:55], v5 offset:6656\n" \
  "v_mfma_f32_16x16x32_bf16 v[172:175], v[56:59], a[44:47], v[172:175]\n" \
  "s_waitcnt lgkmcnt(4)\n" \
  "v_mfma_f32_16x16x32_bf16 v[176:179], v[40:43], a[32:35], v[176:179]\n" \
  "s_add_u32 m0, s72, 155648\n" \
  "v_mfma_f32_16x16x32_bf16 v[176:179], v[44:47], a[48:51], v[176:179]\n" \
  "ds_read_b64_tr_b16 v[56:57], v5 offset:8704\n" \
  "v_mfma_f32_16x16x32_bf16 v[180:183], v[44:47], a[52:55], v[180:183]\n" \
  "buffer_load_dwordx4 v13, s[40:43], s73 offen  lds\n" \
  "v_mfma_f32_16x16x32_bf16 v[180:183], v[40:43], a[36:39], v[180:183]\n" \
  "ds_read_b64_tr_b16 v[58:59], v5 offset:10752\n" \
  "v_mfma_f32_16x16x32_bf16 v[184:187], v[40:43], a[40:43], v[184:187]\n" \
  "v_mfma_f32_16x16x32_bf16 v[184:187], v[44:47], a[56:59], v[184:187]\n" \
  "ds_read_b64_tr_b16 v[60:61], v5 offset:12800\n" \
  "v_mfma_f32_16x16x32_bf16 v[188:191], v[44:47], a[60:63], v[188:191]\n" \
  "ds_read_b64_tr_b16 v[62:63], v5 offset:14848\n" \
  "v_mfma_f32_16x16x32_bf16 v[188:191], v[40:43], a[44:47], v[188:191]\n" \
  "s_waitcnt vmcnt(24)\n" \
  "s_barrier\n" \
  "s_waitcnt lgkmcnt(4)\n" \
  "v_mfma_f32_16x16x32_bf16 v[192:195], v[48:51], a[32:35], v[192:195]\n" \
  "v_mfma_f32_16x16x32_bf16 v[192:195], v[52:55], a[48:51], v[192:195]\n" \
  "ds_read_b64_tr_b16 v[40:41], v4 offset:20480\n" \
  "v_mfma_f32_16x16x32_bf16 v[196:199], v[52:55], a[52:55], v[196:199]\n" \
  "v_mfma_f32_16x16x32_bf16 v[196:199], v[48:51], a[36:39], v[196:199]\n" \
  "ds_read_b64_tr_b16 v[42:43], v4 offset:22528\n" \
  "v_mfma_f32_16x16x32_bf16 v[200:203], v[48:51], a[40:43], v[200:203]\n" \
  "v_mfma_f32_16x16x32_bf16 v[200:203], v[52:55], a[56:59], v[200:203]\n" \
  "ds_read_b64_tr_b16 v[44:45], v4 offset:24576\n" \
  "v_mfma_f32_16x16x32_bf16 v[204:207], v[52:55], a[60:63], v[204:207]\n" \
  "ds_read_b64_tr_b16 v[46:47], v4 offset:26624\n" \
  "v_mfma_f32_16x16x32_bf16 v[204:207], v[48:51], a[44:47], v[204:207]\n" \
  "s_waitcnt lgkmcnt(4)\n" \
  "v_mfma_f32_16x16x32_bf16 v[208:211], v[56:59], a[32:35], v[208:211]\n" \
  "s_mov_b32 s73, 4128896\n" \
  "s_add_u32 m0, s72, 0\n" \
  "v_mfma_f32_16x16x32_bf16 v[208:211], v[60:63], a[48:51], v[208:211]\n" \
  "ds_read_b64_tr_b16 v[48:49], v4 offset:28672\n" \
  "v_mfma_f32_16x16x32_bf16 v[212:215], v[60:63], a[52:55], v[212:215]\n" \
  "buffer_load_dwordx4 v12, s[36:39], s73 offen  lds\n" \
  "v_mfma_f32_16x16x32_bf16 v[212:215], v[56:59], a[36:39], v[212:215]\n" \
  "ds_read_b64_tr_b16 v[50:51], v4 offset:30720\n" \
  "v_mfma_f32_16x16x32_bf16 v[216:219], v[56:59], a[40:43], v[216:219]\n" \
  "v_mfma_f32_16x16x32_bf16 v[216:219], v[60:63], a[56:59], v[216:219]\n" \
  "ds_read_b64_tr_b16 v[52:53], v4 offset:32768\n" \
  "v_mfma_f32_16x16x32_bf16 v[220:223], v[60:63], a[60:63], v[220:223]\n" \
  "ds_read_b64_tr_b16 v[54:55], v4 offset:34816\n" \
  "v_mfma_f32_16x16x32_bf16 v[220:223], v[56:59], a[44:47], v[220:223]\n" \
  "s_waitcnt lgkmcnt(4)\n" \
  "v_mfma_f32_16x16x32_bf16 v[96:99], v[40:43], a[64:67], v[96:99]\n" \
  "s_add_u32 m0, s72, 4096\n" \
  "v_mfma_f32_16x16x32_bf16 v[96:99], v[44:47], a[80:83], v[96:99]\n" \
  "ds_read_b64_tr_b16 v[56:57], v5 offset:20480\n" \
  "v_mfma_f32_16x16x32_bf16 v[100:103], v[44:47], a[84:87], v[100:103]\n" \
  "buffer_load_dwordx4 v13, s[36:39], s73 offen  lds\n" \
  "v_mfma_f32_16x16x32_bf16 v[100:103], v[40:43], a[68:71], v[100:103]\n" \
  "ds_read_b64_tr_b16 v[58:59], v5 offset:22528\n" \
  "v_mfma_f32_16x16x32_bf16 v[104:107], v[40:43], a[72:75], v[104:107]\n" \
  "v_mfma_f32_16x16x32_bf16 v[104:107], v[44:47], a[88:91], v[104:107]\n" \
  "ds_read_b64_tr_b16 v[60:61], v5 offset:24576\n" \
  "v_mfma_f32_16x16x32_bf16 v[108:111], v[44:47], a[92:95], v[108:111]\n" \
  "ds_read_b64_tr_b16 v[62:63], v5 offset:26624\n" \
  "v_mfma_f32_16x16x32_bf16 v[108:111], v[40:43], a[76:79], v[108:111]\n" \
  "s_waitcnt lgkmcnt(4)\n" \
  "v_mfma_f32_16x16x32_bf16 v[112:115], v[48:51], a[64:67], v[112:115]\n" \
  "s_add_u32 m0, s72, 8192\n" \
  "v_mfma_f32_16x16x32_bf16 v[112:115], v[52:55], a[80:83], v[112:115]\n" \
  "ds_read_b64_tr_b16 v[40:41], v5 offset:28672\n" \
  "v_mfma_f32_16x16x32_bf16 v[116:119], v[52:55], a[84:87], v[116:119]\n" \
  "buffer_load_dwordx4 v12, s[40:43], s73 offen  lds\n" \
  "v_mfma_f32_16x16x32_bf16 v[116:119], v[48:51], a[68:71], v[116:119]\n" \
  "ds_read_b64_tr_b16 v[42:43], v5 offset:30720\n" \
  "v_mfma_f32_16x16x32_bf16 v[120:123], v[48:51], a[72:75], v[120:123]\n" \
  "v_mfma_f32_16x16x32_bf16 v[120:123], v[52:55], a[88:91], v[120:123]\n" \
  "ds_read_b64_tr_b16 v[44:45], v5 offset:32768\n" \
  "v_mfma_f32_16x16x32_bf16 v[124:127], v[52:55], a[92:95], v[124:127]\n" \
  "ds_read_b64_tr_b16 v[46:47], v5 offset:34816\n" \
  "v_mfma_f32_16x16x32_bf16 v[124:127], v[48:51], a[76:79], v[124:127]\n" \
  "s_waitcnt lgkmcnt(4)\n" \
  "v_mfma_f32_16x16x32_bf16 v[128:131], v[56:59], a[64:67], v[128:131]\n" \
  "s_add_u32 m0, s72, 12288\n" \
  "v_mfma_f32_16x16x32_bf16 v[128:131], v[60:63], a[80:83], v[128:131]\n" \
  "ds_read_b64_tr_b16 v[48:49], v4 offset:20992\n" \
  "v_mfma_f32_16x16x32_bf16 v[132:135], v[60:63], a[84:87], v[132:135]\n" \
  "buffer_load_dwordx4 v13, s[40:43], s73 offen  lds\n" \
  "v_mfma_f32_16x16x32_bf16 v[132:135], v[56:59], a[68:71], v[132:135]\n" \
  "ds_read_b64_tr_b16 v[50:51], v4 offset:23040\n" \
  "v_mfma_f32_16x16x32_bf16 v[136:139], v[56:59], a[72:75], v[136:139]\n" \
  "v_mfma_f32_16x16x32_bf16 v[136:139], v[60:63], a[88:91], v[136:139]\n" \
  "ds_read_b64_tr_b16 v[52:53], v4 offset:25088\n" \
  "v_mfma_f32_16x16x32_bf16 v[140:143], v[60:63], a[92:95], v[140:143]\n" \
  "ds_read_b64_tr_b16 v[54:55], v4 offset:27136\n" \
  "v_mfma_f32_16x16x32_bf16 v[140:143], v[56:59], a[76:79], v[140:143]\n" \
  "s_waitcnt lgkmcnt(4)\n" \
  "v_mfma_f32_16x16x32_bf16 v[144:147], v[40:43], a[64:67], v[144:147]\n" \
  "s_lshl_b32 s81, s77, 7\n" \
  "s_add_u32 s81, s81, s86\n" \
  "v_mfma_f32_16x16x32_bf16 v[144:147], v[44:47], a[80:83], v[144:147]\n" \
  "ds_read_b64_tr_b16 v[56:57], v4 offset:29184\n" \
  "s_waitcnt vmcnt(6)\n" \
  "v_mfma_f32_16x16x32_bf16 v[148:151], v[44:47], a[84:87], v[148:151]\n" \
  "buffer_store_dword v28, v1, s[60:63], s81 offen offset:0 sc1\n" \
  "v_mfma_f32_16x16x32_bf16 v[148:151], v[40:43], a[68:71], v[148:151]\n" \
  "ds_read_b64_tr_b16 v[58:59], v4 offset:31232\n" \
  "v_mfma_f32_16x16x32_bf16 v[152:155], v[40:43], a[72:75], v[152:155]\n" \
  "v_mfma_f32_16x16x32_bf16 v[152:155], v[44:47], a[88:91], v[152:155]\n" \
  "ds_read_b64_tr_b16 v[60:61], v4 offset:33280\n" \
  "v_mfma_f32_16x16x32_bf16 v[156:159], v[44:47], a[92:95], v[156:159]\n" \
  "ds_read_b64_tr_b16 v[62:63], v4 offset:35328\n" \
  "v_mfma_f32_16x16x32_bf16 v[156:159], v[40:43], a[76:79], v[156:159]\n" \
  "s_waitcnt lgkmcnt(4)\n" \
  "v_mfma_f32_16x16x32_bf16 v[160:163], v[48:51], a[64:67], v[160:163]\n" \
  "s_mov_b32 s73, 4718720\n" \
  "s_add_u32 m0, s72, 20480\n" \
  "v_mfma_f32_16x16x32_bf16 v[160:163], v[52:55], a[80:83], v[160:163]\n" \
  "ds_read_b64_tr_b16 v[40:41], v5 offset:20992\n" \
  "v_lshrrev_b32 v32, 4, v11\n" \
  "v_mfma_f32_16x16x32_bf16 v[164:167], v[52:55], a[84:87], v[164:167]\n" \
  "buffer_load_dwordx4 v12, s[36:39], s73 offen  lds\n" \
  "v_mfma_f32_16x16x32_bf16 v[164:167], v[48:51], a[68:71], v[164:167]\n" \
  "ds_read_b64_tr_b16 v[42:43], v5 offset:23040\n" \
  "v_lshlrev_b32 v32, 2, v32\n" \
  "v_mfma_f32_16x16x32_bf16 v[168:171], v[48:51], a[72:75], v[168:171]\n" \
  "v_and_b32 v33, 15, v11\n" \
  "v_sub_u32 v32, v33, v32\n" \
  "v_mfma_f32_16x16x32_bf16 v[168:171], v[52:55], a[88:91], v[168:171]\n" \
  "ds_read_b64_tr_b16 v[44:45], v5 offset:25088\n" \
  "v_mov_b32 v35, 0x3f80\n" \
  "v_mfma_f32_16x16x32_bf16 v[172:175], v[52:55], a[92:95], v[172:175]\n" \
  "ds_read_b64_tr_b16 v[46:47], v5 offset:27136\n" \
  "v_mov_b32 v36, 0x3f800000\n" \
  "v_mfma_f32_16x16x32_bf16 v[172:175], v[48:51], a[76:79], v[172:175]\n" \
  "v_cmp_eq_u32 s[90:91], 0, v32\n" \
  "s_waitcnt lgkmcnt(4)\n" \
  "v_mfma_f32_16x16x32_bf16 v[176:179], v[56:59], a[64:67], v[176:179]\n" \
  "s_add_u32 m0, s72, 24576\n" \
  "s_nop 0\n" \
  "v_mfma_f32_16x16x32_bf16 v[176:179], v[60:63], a[80:83], v[176:179]\n" \
  "ds_read_b64_tr_b16 v[48:49], v5 offset:29184\n" \
  "v_cndmask_b32 v226, 0, v35, s[90:91]\n" \
  "v_mfma_f32_16x16x32_bf16 v[180:183], v[60:63], a[84:87], v[180:183]\n" \
  "buffer_load_dwordx4 v13, s[36:39], s73 offen  lds\n" \
  "v_mfma_f32_16x16x32_bf16 v[180:183], v[56:59], a[68:71], v[180:183]\n" \
  "ds_read_b64_tr_b16 v[50:51], v5 offset:31232\n" \
  "v_cmp_eq_u32 s[90:91], 1, v32\n" \
  "v_mfma_f32_16x16x32_bf16 v[184:187], v[56:59], a[72:75], v[184:187]\n" \
  "s_nop 0\n" \
  "v_cndmask_b32 v226, v226, v36, s[90:91]\n" \
  "v_mfma_f32_16x16x32_bf16 v[184:187], v[60:63], a[88:91], v[184:187]\n" \
  "ds_read_b64_tr_b16 v[52:53], v5 offset:33280\n" \
  "v_cmp_eq_u32 s[90:91], 2, v32\n" \
  "v_mfma_f32_16x16x32_bf16 v[188:191], v[60:63], a[92:95], v[188:191]\n" \
  "ds_read_b64_tr_b16 v[54:55], v5 offset:35328\n" \
  "s_nop 0\n" \
  "v_mfma_f32_16x16x32_bf16 v[188:191], v[56:59], a[76:79], v[188:191]\n" \
  "s_barrier\n" \
  "s_waitcnt lgkmcnt(4)\n" \
  "v_mfma_f32_16x16x32_bf16 v[192:195], v[40:43], a[64:67], v[192:195]\n" \
  "s_add_u32 m0, s72, 28672\n" \
  "v_cndmask_b32 v227, 0, v35, s[90:91]\n" \
  "v_mfma_f32_16x16x32_bf16 v[192:195], v[44:47], a[80:83], v[192:195]\n" \
  "ds_read_b64_tr_b16 v[56:57], v4 offset:40960\n" \
  "v_cmp_eq_u32 s[90:91], 3, v32\n" \
  "v_mfma_f32_16x16x32_bf16 v[196:199], v[44:47], a[84:87], v[196:199]\n" \
  "buffer_load_dwordx4 v12, s[40:43], s73 offen  lds\n" \
  "v_mfma_f32_16x16x32_bf16 v[196:199], v[40:43], a[68:71], v[196:199]\n" \
  "ds_read_b64_tr_b16 v[58:59], v4 offset:43008\n" \
  "s_nop 0\n" \
  "v_mfma_f32_16x16x32_bf16 v[200:203], v[40:43], a[72:75], v[200:203]\n" \
  "v_cndmask_b32 v227, v227, v36, s[90:91]\n" \
  "v_mov_b32 v228, v226\n" \
  "v_mfma_f32_16x16x32_bf16 v[200:203], v[44:47], a[88:91], v[200:203]\n" \
  "ds_read_b64_tr_b16 v[60:61], v4 offset:45056\n" \
  "v_mov_b32 v229, v227\n" \
  "v_mfma_f32_16x16x32_bf16 v[204:207], v[44:47], a[92:95], v[204:207]\n" \
  "ds_read_b64_tr_b16 v[62:63], v4 offset:47104\n" \
  "v_mov_b32 v224, 0\n" \
  "v_mfma_f32_16x16x32_bf16 v[204:207], v[40:43], a[76:79], v[204:207]\n" \
  "v_mov_b32 v225, 0\n" \
  "s_waitcnt lgkmcnt(4)\n" \
  "v_mfma_f32_16x16x32_bf16 v[208:211], v[48:51], a[64:67], v[208:211]\n" \
  "s_add_u32 m0, s72, 32768\n" \
  "v_mov_b32 v230, 0\n" \
  "v_mfma_f32_16x16x32_bf16 v[208:211], v[52:55], a[80:83], v[208:211]\n" \
  "ds_read_b64_tr_b16 v[40:41], v4 offset:49152\n" \
  "v_mov_b32 v231, 0\n" \
  "v_mfma_f32_16x16x32_bf16 v[212:215], v[52:55], a[84:87], v[212:215]\n" \
  "buffer_load_dwordx4 v13, s[40:43], s73 offen  lds\n" \
  "v_mfma_f32_16x16x32_bf16 v[212:215], v[48:51], a[68:71], v[212:215]\n" \
  "ds_read_b64_tr_b16 v[42:43], v4 offset:51200\n" \
  "v_cmp_eq_u32 s[90:91], 0, v33\n" \
  "v_mfma_f32_16x16x32_bf16 v[216:219], v[48:51], a[72:75], v[216:219]\n" \
  "s_nop 0\n" \
  "v_cndmask_b32 v232, 0, v35, s[90:91]\n" \
  "v_mfma_f32_16x16x32_bf16 v[216:219], v[52:55], a[88:91], v[216:219]\n" \
  "ds_read_b64_tr_b16 v[44:45], v4 offset:53248\n" \
  "v_cmp_eq_u32 s[90:91], 1, v33\n" \
  "v_mfma_f32_16x16x32_bf16 v[220:223], v[52:55], a[92:95], v[220:223]\n" \
  "ds_read_b64_tr_b16 v[46:47], v4 offset:55296\n" \
  "s_nop 0\n" \
  "v_mfma_f32_16x16x32_bf16 v[220:223], v[48:51], a[76:79], v[220:223]\n" \
  "v_cndmask_b32 v232, v232, v36, s[90:91]\n" \
  "s_waitcnt lgkmcnt(4)\n" \
  "v_mfma_f32_16x16x32_bf16 v[96:99], v[56:59], a[96:99], v[96:99]\n" \
  "v_cmp_eq_u32 s[90:91], 2, v33\n" \
  "s_nop 0\n" \
  "v_mfma_f32_16x16x32_bf16 v[96:99], v[60:63], a[112:115], v[96:99]\n" \
  "ds_read_b64_tr_b16 v[48:49], v5 offset:40960\n" \
  "v_cndmask_b32 v233, 0, v35, s[90:91]\n" \
  "v_mfma_f32_16x16x32_bf16 v[100:103], v[60:63], a[116:119], v[100:103]\n" \
  "v_cmp_eq_u32 s[90:91], 3, v33\n" \
  "s_nop 0\n" \
  "v_mfma_f32_16x16x32_bf16 v[100:103], v[56:59], a[100:103], v[100:103]\n" \
  "ds_read_b64_tr_b16 v[50:51], v5 offset:43008\n" \
  "v_cndmask_b32 v233, v233, v36, s[90:91]\n" \
  "v_mfma_f32_16x16x32_bf16 v[104:107], v[56:59], a[104:107], v[104:107]\n" \
  "v_cmp_eq_u32 s[90:91], 4, v33\n" \
  "s_nop 0\n" \
  "v_mfma_f32_16x16x32_bf16 v[104:107], v[60:63], a[120:123], v[104:107]\n" \
  "ds_read_b64_tr_b16 v[52:53], v5 offset:45056\n" \
  "v_cndmask_b32 v234, 0, v35, s[90:91]\n" \
  "v_mfma_f32_16x16x32_bf16 v[108:111], v[60:63], a[124:127], v[108:111]\n" \
  "ds_read_b64_tr_b16 v[54:55], v5 offset:47104\n" \
  "v_cmp_eq_u32 s[90:91], 5, v33\n" \
  "v_mfma_f32_16x16x32_bf16 v[108:111], v[56:59], a[108:111], v[108:111]\n" \
  "s_nop 0\n" \
  "s_waitcnt lgkmcnt(4)\n" \
  "v_mfma_f32_16x16x32_bf16 v[112:115], v[40:43], a[96:99], v[112:115]\n" \
  "buffer_load_dword v27, v26, s[60:63], s86 offen offset:0 sc1\n" \
  "v_mfma_f32_16x16x32_bf16 v[112:115], v[44:47], a[112:115], v[112:115]\n" \
  "ds_read_b64_tr_b16 v[56:57], v5 offset:49152\n" \
  "s_and_b32 s73, s94, 128\n" \
  "v_mfma_f32_16x16x32_bf16 v[116:119], v[44:47], a[116:119], v[116:119]\n" \
  "s_lshl_b32 s73, s73, 1\n" \
  "v_cndmask_b32 v234, v234, v36, s[90:91]\n" \
  "v_mfma_f32_16x16x32_bf16 v[116:119], v[40:43], a[100:103], v[116:119]\n" \
  "ds_read_b64_tr_b16 v[58:59], v5 offset:51200\n" \
  "v_cmp_eq_u32 s[90:91], 6, v33\n" \
  "v_mfma_f32_16x16x32_bf16 v[120:123], v[40:43], a[104:107], v[120:123]\n" \
  "s_add_u32 s44, s48, 128\n" \
  "s_addc_u32 s45, s49, 0\n" \
  "v_mfma_f32_16x16x32_bf16 v[120:123], v[44:47], a[120:123], v[120:123]\n" \
  "ds_read_b64_tr_b16 v[60:61], v5 offset:53248\n" \
  "s_nop 0\n" \
  "v_mfma_f32_16x16x32_bf16 v[124:127], v[44:47], a[124:127], v[124:127]\n" \
  "ds_read_b64_tr_b16 v[62:63], v5 offset:55296\n" \
  "v_cndmask_b32 v235, 0, v35, s[90:91]\n" \
  "v_mfma_f32_16x16x32_bf16 v[124:127], v[40:43], a[108:111], v[124:127]\n" \
  "v_cmp_eq_u32 s[90:91], 7, v33\n" \
  "s_waitcnt lgkmcnt(4)\n" \
  "v_mfma_f32_16x16x32_bf16 v[128:131], v[48:51], a[96:99], v[128:131]\n" \
  "s_sub_u32 s44, s44, s73\n" \
  "s_subb_u32 s45, s45, 0\n" \
  "v_mfma_f32_16x16x32_bf16 v[128:131], v[52:55], a[112:115], v[128:131]\n" \
  "ds_read_b64_tr_b16 v[40:41], v4 offset:41472\n" \
  "s_add_u32 m0, s72, 57344\n" \
  "v_mfma_f32_16x16x32_bf16 v[132:135], v[52:55], a[116:119], v[132:135]\n" \
  "buffer_load_dwordx4 v31, s[60:63], s22 offen sc1 lds\n" \
  "v_mfma_f32_16x16x32_bf16 v[132:135], v[48:51], a[100:103], v[132:135]\n" \
  "ds_read_b64_tr_b16 v[42:43], v4 offset:43520\n" \
  "s_add_u32 m0, s72, 40960\n" \
  "v_mfma_f32_16x16x32_bf16 v[136:139], v[48:51], a[104:107], v[136:139]\n" \
  "buffer_load_dwordx4 v29, s[44:47], s13 offen  lds\n" \
  "v_mfma_f32_16x16x32_bf16 v[136:139], v[52:55], a[120:123], v[136:139]\n" \
  "ds_read_b64_tr_b16 v[44:45], v4 offset:45568\n" \
  "s_nop 0\n" \
  "v_mfma_f32_16x16x32_bf16 v[140:143], v[52:55], a[124:127], v[140:143]\n" \
  "ds_read_b64_tr_b16 v[46:47], v4 offset:47616\n" \
  "v_cndmask_b32 v235, v235, v36, s[90:91]\n" \
  "v_mfma_f32_16x16x32_bf16 v[140:143], v[48:51], a[108:111], v[140:143]\n" \
  "v_lshrrev_b32 v32, 4, v11\n" \
  "s_waitcnt lgkmcnt(4)\n" \
  "v_mfma_f32_16x16x32_bf16 v[144:147], v[56:59], a[96:99], v[144:147]\n" \
  "s_add_u32 m0, s72, 45056\n" \
  "v_lshlrev_b32 v32, 3, v32\n" \
  "v_mfma_f32_16x16x32_bf16 v[144:147], v[60:63], a[112:115], v[144:147]\n" \
  "ds_read_b64_tr_b16 v[48:49], v4 offset:49664\n" \
  "v_lshl_add_u32 v254, v33, 5, v32\n" \
  "v_mfma_f32_16x16x32_bf16 v[148:151], v[60:63], a[116:119], v[148:151]\n" \
  "buffer_load_dwordx4 v30, s[44:47], s13 offen  lds\n" \
  "v_mfma_f32_16x16x32_bf16 v[148:151], v[56:59], a[100:103], v[148:151]\n" \
  "ds_read_b64_tr_b16 v[50:51], v4 offset:51712\n" \
  "s_lshl_b32 s89, s77, 8\n" \
  "v_mfma_f32_16x16x32_bf16 v[152:155], v[56:59], a[104:107], v[152:155]\n" \
  "s_add_u32 s99, s89, s94\n" \
  "s_add_u32 s89, s99, 5760\n" \
  "v_mfma_f32_16x16x32_bf16 v[152:155], v[60:63], a[120:123], v[152:155]\n" \
  "ds_read_b64_tr_b16 v[52:53], v4 offset:53760\n" \
  "v_pk_mov_b32 v[248:249], v[38:39], v[38:39] op_sel:[0,1]\n" \
  "v_mfma_f32_16x16x32_bf16 v[156:159], v[60:63], a[124:127], v[156:159]\n" \
  "ds_read_b64_tr_b16 v[54:55], v4 offset:55808\n" \
  "s_lshl_b32 s91, s79, 2\n" \
  "v_mfma_f32_16x16x32_bf16 v[156:159], v[56:59], a[108:111], v[156:159]\n" \
  "s_add_u32 s91, s91, s80\n" \
  "s_waitcnt lgkmcnt(4)\n" \
  "v_mfma_f32_16x16x32_bf16 v[160:163], v[40:43], a[96:99], v[160:163]\n" \
  "s_add_u32 m0, s72, 49152\n" \
  "s_mul_i32 s90, s91, 73728\n" \
  "v_mfma_f32_16x16x32_bf16 v[160:163], v[44:47], a[112:115], v[160:163]\n" \
  "ds_read_b64_tr_b16 v[56:57], v5 offset:41472\n" \
  "s_lshl_b32 s89, s99, 6\n" \
  "v_mfma_f32_16x16x32_bf16 v[164:167], v[44:47], a[116:119], v[164:167]\n" \
  "buffer_load_dwordx4 v29, s[44:47], s14 offen  lds\n" \
  "v_mfma_f32_16x16x32_bf16 v[164:167], v[40:43], a[100:103], v[164:167]\n" \
  "ds_read_b64_tr_b16 v[58:59], v5 offset:43520\n" \
  "s_add_u32 s90, s90, s89\n" \
  "v_mfma_f32_16x16x32_bf16 v[168:171], v[40:43], a[104:107], v[168:171]\n" \
  "s_add_u32 s90, s90, 4456448\n" \
  "v_mfma_f32_16x16x32_bf16 v[168:171], v[44:47], a[120:123], v[168:171]\n" \
  "ds_read_b64_tr_b16 v[60:61], v5 offset:45568\n" \
  "v_mfma_f32_16x16x32_bf16 v[172:175], v[44:47], a[124:127], v[172:175]\n" \
  "ds_read_b64_tr_b16 v[62:63], v5 offset:47616\n" \
  "v_mfma_f32_16x16x32_bf16 v[172:175], v[40:43], a[108:111], v[172:175]\n" \
  "s_waitcnt lgkmcnt(4)\n" \
  "v_mfma_f32_16x16x32_bf16 v[176:179], v[48:51], a[96:99], v[176:179]\n" \
  "s_waitcnt vmcnt(4)\n" \
  "s_add_u32 m0, s72, 53248\n" \
  "v_mfma_f32_16x16x32_bf16 v[176:179], v[52:55], a[112:115], v[176:179]\n" \
  "ds_read_b64_tr_b16 v[40:41], v5 offset:49664\n" \
  "v_mfma_f32_16x16x32_bf16 v[180:183], v[52:55], a[116:119], v[180:183]\n" \
  "v_cmp_ne_u32 vcc, s84, v27\n" \
  "s_and_b32 vcc_lo, vcc_lo, 0xe\n" \
  "s_cbranch_scc1 SLOW_HE_%=\n" \
  "BACK_HE_%=:\n" \
  "v_mfma_f32_16x16x32_bf16 v[180:183], v[48:51], a[100:103], v[180:183]\n" \
  "ds_read_b64_tr_b16 v[42:43], v5 offset:51712\n" \
  "v_mfma_f32_16x16x32_bf16 v[184:187], v[48:51], a[104:107], v[184:187]\n" \
  "buffer_load_dwordx4 v30, s[44:47], s14 offen  lds\n" \
  "v_mfma_f32_16x16x32_bf16 v[184:187], v[52:55], a[120:123], v[184:187]\n" \
  "ds_read_b64_tr_b16 v[44:45], v5 offset:53760\n" \
  "v_mfma_f32_16x16x32_bf16 v[188:191], v[52:55], a[124:127], v[188:191]\n" \
  "ds_read_b64_tr_b16 v[46:47], v5 offset:55808\n" \
  "v_mfma_f32_16x16x32_bf16 v[188:191], v[48:51], a[108:111], v[188:191]\n" \
  "s_barrier\n" \
  "s_waitcnt lgkmcnt(4)\n" \
  "v_mfma_f32_16x16x32_bf16 v[192:195], v[56:59], a[96:99], v[192:195]\n" \
  "buffer_load_dwordx4 a[32:35], v24, s[56:59], s5 offen offset:0 sc1\n" \
  "v_mfma_f32_16x16x32_bf16 v[192:195], v[60:63], a[112:115], v[192:195]\n" \
  "ds_read_b64_tr_b16 v[48:49], v6 offset:0\n" \
  "v_mfma_f32_16x16x32_bf16 v[196:199], v[60:63], a[116:119], v[196:199]\n" \
  "buffer_load_dwordx2 v[240:241], v254, s[60:63], s90 offen offset:0 sc1\n" \
  "v_mfma_f32_16x16x32_bf16 v[196:199], v[56:59], a[100:103], v[196:199]\n" \
  "ds_read_b64_tr_b16 v[50:51], v6 offset:2048\n" \
  "v_mfma_f32_16x16x32_bf16 v[200:203], v[56:59], a[104:107], v[200:203]\n" \
  "buffer_load_dwordx2 v[242:243], v254, s[60:63], s90 offen offset:512 sc1\n" \
  "v_mfma_f32_16x16x32_bf16 v[200:203], v[60:63], a[120:123], v[200:203]\n" \
  "ds_read_b64_tr_b16 v[52:53], v6 offset:4096\n" \
  "v_mfma_f32_16x16x32_bf16 v[204:207], v[60:63], a[124:127], v[204:207]\n" \
  "ds_read_b64_tr_b16 v[54:55], v6 offset:6144\n" \
  "v_mfma_f32_16x16x32_bf16 v[204:207], v[56:59], a[108:111], v[204:207]\n" \
  "s_waitcnt lgkmcnt(4)\n" \
  "v_mfma_f32_16x16x32_bf16 v[208:211], v[40:43], a[96:99], v[208:211]\n" \
  "s_add_u32 m0, s72, 77824\n" \
  "v_mfma_f32_16x16x32_bf16 v[208:211], v[44:47], a[112:115], v[208:211]\n" \
  "ds_read_b64_tr_b16 v[56:57], v6 offset:8192\n" \
  "v_mfma_f32_16x16x32_bf16 v[212:215], v[44:47], a[116:119], v[212:215]\n" \
  "buffer_load_dwordx4 v31, s[60:63], s23 offen sc1 lds\n" \
  "v_mfma_f32_16x16x32_bf16 v[212:215], v[40:43], a[100:103], v[212:215]\n" \
  "ds_read_b64_tr_b16 v[58:59], v6 offset:10240\n" \
  "v_mfma_f32_16x16x32_bf16 v[216:219], v[40:43], a[104:107], v[216:219]\n" \
  "buffer_load_dwordx4 a[36:39], v24, s[56:59], s5 offen offset:1024 sc1\n" \
  "v_mfma_f32_16x16x32_bf16 v[216:219], v[44:47], a[120:123], v[216:219]\n" \
  "ds_read_b64_tr_b16 v[60:61], v6 offset:12288\n" \
  "v_mfma_f32_16x16x32_bf16 v[220:223], v[44:47], a[124:127], v[220:223]\n" \
  "ds_read_b64_tr_b16 v[62:63], v6 offset:14336\n" \
  "v_mfma_f32_16x16x32_bf16 v[220:223], v[40:43], a[108:111], v[220:223]\n" \
  "s_waitcnt lgkmcnt(4)\n" \
  "v_mfma_f32_16x16x32_bf16 v[96:99], v[48:51], a[128:131], v[96:99]\n" \
  "s_add_u32 m0, s72, 61440\n" \
  "v_mfma_f32_16x16x32_bf16 v[96:99], v[52:55], a[144:147], v[96:99]\n" \
  "ds_read_b64_tr_b16 v[40:41], v7 offset:0\n" \
  "v_mfma_f32_16x16x32_bf16 v[100:103], v[52:55], a[148:151], v[100:103]\n" \
  "buffer_load_dwordx4 v29, s[44:47], s15 offen  lds\n" \
  "v_mfma_f32_16x16x32_bf16 v[100:103], v[48:51], a[132:135], v[100:103]\n" \
  "ds_read_b64_tr_b16 v[42:43], v7 offset:2048\n" \
  "v_mfma_f32_16x16x32_bf16 v[104:107], v[48:51], a[136:139], v[104:107]\n" \
  "buffer_load_dwordx4 a[40:43], v24, s[56:59], s5 offen offset:2048 sc1\n" \
  "v_mfma_f32_16x16x32_bf16 v[104:107], v[52:55], a[152:155], v[104:107]\n" \
  "ds_read_b64_tr_b16 v[44:45], v7 offset:4096\n" \
  "v_mfma_f32_16x16x32_bf16 v[108:111], v[52:55], a[156:159], v[108:111]\n" \
  "ds_read_b64_tr_b16 v[46:47], v7 offset:6144\n" \
  "v_mfma_f32_16x16x32_bf16 v[108:111], v[48:51], a[140:143], v[108:111]\n" \
  "s_waitcnt lgkmcnt(4)\n" \
  "v_mfma_f32_16x16x32_bf16 v[112:115], v[56:59], a[128:131], v[112:115]\n" \
  "s_add_u32 m0, s72, 65536\n" \
  "v_mfma_f32_16x16x32_bf16 v[112:115], v[60:63], a[144:147], v[112:115]\n" \
  "ds_read_b64_tr_b16 v[48:49], v7 offset:8192\n" \
  "v_mfma_f32_16x16x32_bf16 v[116:119], v[60:63], a[148:151], v[116:119]\n" \
  "buffer_load_dwordx4 v30, s[44:47], s15 offen  lds\n" \
  "v_mfma_f32_16x16x32_bf16 v[116:119], v[56:59], a[132:135], v[116:119]\n" \
  "ds_read_b64_tr_b16 v[50:51], v7 offset:10240\n" \
  "v_mfma_f32_16x16x32_bf16 v[120:123], v[56:59], a[136:139], v[120:123]\n" \
  "buffer_load_dwordx4 a[44:47], v24, s[56:59], s5 offen offset:3072 sc1\n" \
  "v_mfma_f32_16x16x32_bf16 v[120:123], v[60:63], a[152:155], v[120:123]\n" \
  "ds_read_b64_tr_b16 v[52:53], v7 offset:12288\n" \
  "v_mfma_f32_16x16x32_bf16 v[124:127], v[60:63], a[156:159], v[124:127]\n" \
  "ds_read_b64_tr_b16 v[54:55], v7 offset:14336\n" \
  "v_mfma_f32_16x16x32_bf16 v[124:127], v[56:59], a[140:143], v[124:127]\n" \
  "s_waitcnt lgkmcnt(4)\n" \
  "v_mfma_f32_16x16x32_bf16 v[128:131], v[40:43], a[128:131], v[128:131]\n" \
  "s_add_u32 m0, s72, 69632\n" \
  "v_mfma_f32_16x16x32_bf16 v[128:131], v[44:47], a[144:147], v[128:131]\n" \
  "ds_read_b64_tr_b16 v[56:57], v6 offset:512\n" \
  "v_mfma_f32_16x16x32_bf16 v[132:135], v[44:47], a[148:151], v[132:135]\n" \
  "buffer_load_dwordx4 v29, s[44:47], s16 offen  lds\n" \
  "v_mfma_f32_16x16x32_bf16 v[132:135], v[40:43], a[132:135], v[132:135]\n" \
  "ds_read_b64_tr_b16 v[58:59], v6 offset:2560\n" \
  "v_mfma_f32_16x16x32_bf16 v[136:139], v[40:43], a[136:139], v[136:139]\n" \
  "buffer_load_dwordx4 a[48:51], v25, s[56:59], s5 offen offset:0 sc1\n" \
  "v_mfma_f32_16x16x32_bf16 v[136:139], v[44:47], a[152:155], v[136:139]\n" \
  "ds_read_b64_tr_b16 v[60:61], v6 offset:4608\n" \
  "v_mfma_f32_16x16x32_bf16 v[140:143], v[44:47], a[156:159], v[140:143]\n" \
  "ds_read_b64_tr_b16 v[62:63], v6 offset:6656\n" \
  "v_mfma_f32_16x16x32_bf16 v[140:143], v[40:43], a[140:143], v[140:143]\n" \
  "PG6_%=:\n" \
  "s_cbranch_execz HOPRET_%=\n" \
  "s_waitcnt lgkmcnt(4)\n" \
  "v_mfma_f32_16x16x32_bf16 v[144:147], v[48:51], a[128:131], v[144:147]\n" \
  "s_add_u32 m0, s72, 73728\n" \
  "v_mfma_f32_16x16x32_bf16 v[144:147], v[52:55], a[144:147], v[144:147]\n" \
  "ds_read_b64_tr_b16 v[40:41], v6 offset:8704\n" \
  "v_mfma_f32_16x16x32_bf16 v[148:151], v[52:55], a[148:151], v[148:151]\n" \
  "buffer_load_dwordx4 v30, s[44:47], s16 offen  lds\n" \
  "v_mfma_f32_16x16x32_bf16 v[148:151], v[48:51], a[132:135], v[148:151]\n" \
  "ds_read_b64_tr_b16 v[42:43], v6 offset:10752\n" \
  "v_mfma_f32_16x16x32_bf16 v[152:155], v[48:51], a[136:139], v[152:155]\n" \
  "buffer_load_dwordx4 a[52:55], v25, s[56:59], s5 offen offset:1024 sc1\n" \
  "v_mfma_f32_16x16x32_bf16 v[152:155], v[52:55], a[152:155], v[152:155]\n" \
  "ds_read_b64_tr_b16 v[44:45], v6 offset:12800\n" \
  "v_mfma_f32_16x16x32_bf16 v[156:159], v[52:55], a[156:159], v[156:159]\n" \
  "ds_read_b64_tr_b16 v[46:47], v6 offset:14848\n" \
  "v_mfma_f32_16x16x32_bf16 v[156:159], v[48:51], a[140:143], v[156:159]\n" \
  "s_waitcnt lgkmcnt(4)\n" \
  "v_mfma_f32_16x16x32_bf16 v[160:163], v[56:59], a[128:131], v[160:163]\n" \
  "buffer_load_dwordx4 a[56:59], v25, s[56:59], s5 offen offset:2048 sc1\n" \
  "v_mfma_f32_16x16x32_bf16 v[160:163], v[60:63], a[144:147], v[160:163]\n" \
  "ds_read_b64_tr_b16 v[48:49], v7 offset:512\n" \
  "v_mfma_f32_16x16x32_bf16 v[164:167], v[60:63], a[148:151], v[164:167]\n" \
  "buffer_load_dwordx2 v[244:245], v254, s[60:63], s90 offen offset:1024 sc1\n" \
  "v_mfma_f32_16x16x32_bf16 v[164:167], v[56:59], a[132:135], v[164:167]\n" \
  "ds_read_b64_tr_b16 v[50:51], v7 offset:2560\n" \
  "v_mfma_f32_16x16x32_bf16 v[168:171], v[56:59], a[136:139], v[168:171]\n" \
  "buffer_load_dwordx2 v[246:247], v254, s[60:63], s90 offen offset:1536 sc1\n" \
  "v_mfma_f32_16x16x32_bf16 v[168:171], v[60:63], a[152:155], v[168:171]\n" \
  "ds_read_b64_tr_b16 v[52:53], v7 offset:4608\n" \
  "s_mul_i32 s91, s91, 9216\n" \
  "v_mfma_f32_16x16x32_bf16 v[172:175], v[60:63], a[156:159], v[172:175]\n" \
  "ds_read_b64_tr_b16 v[54:55], v7 offset:6656\n" \
  "s_add_u32 s99, s99, s91\n" \
  "v_mfma_f32_16x16x32_bf16 v[172:175], v[56:59], a[140:143], v[172:175]\n" \
  "s_add_u32 s100, s99, 4608\n" \
  "s_waitcnt lgkmcnt(4)\n" \
  "v_mfma_f32_16x16x32_bf16 v[176:179], v[40:43], a[128:131], v[176:179]\n" \
  "s_add_u32 m0, s72, 98304\n" \
  "s_lshl_b32 s89, s77, 2\n" \
  "v_mfma_f32_16x16x32_bf16 v[176:179], v[44:47], a[144:147], v[176:179]\n" \
  "ds_read_b64_tr_b16 v[56:57], v7 offset:8704\n" \
  "s_add_u32 s89, s89, s80\n" \
  "v_mfma_f32_16x16x32_bf16 v[180:183], v[44:47], a[148:151], v[180:183]\n" \
  "buffer_load_dwordx4 v31, s[60:63], s24 offen sc1 lds\n" \
  "v_mfma_f32_16x16x32_bf16 v[180:183], v[40:43], a[132:135], v[180:183]\n" \
  "ds_read_b64_tr_b16 v[58:59], v7 offset:10752\n" \
  "s_lshl_b32 s89, s89, 3\n" \
  "v_mfma_f32_16x16x32_bf16 v[184:187], v[40:43], a[136:139], v[184:187]\n" \
  "buffer_load_dwordx4 a[60:63], v25, s[56:59], s5 offen offset:3072 sc1\n" \
  "v_mfma_f32_16x16x32_bf16 v[184:187], v[44:47], a[152:155], v[184:187]\n" \
  "ds_read_b64_tr_b16 v[60:61], v7 offset:12800\n" \
  "s_add_u32 s91, s89, 1024\n" \
  "v_mfma_f32_16x16x32_bf16 v[188:191], v[44:47], a[156:159], v[188:191]\n" \
  "ds_read_b64_tr_b16 v[62:63], v7 offset:14848\n" \
  "s_add_u32 s89, s91, 5760\n" \
  "v_mfma_f32_16x16x32_bf16 v[188:191], v[40:43], a[140:143], v[188:191]\n" \
  "s_barrier\n" \
  "s_waitcnt lgkmcnt(4)\n" \
  "v_mfma_f32_16x16x32_bf16 v[192:195], v[48:51], a[128:131], v[192:195]\n" \
  "s_add_u32 m0, s72, 81920\n" \
  "v_pk_mov_b32 v[252:253], s[2:3], s[2:3] op_sel:[0,1]\n" \
  "v_mfma_f32_16x16x32_bf16 v[192:195], v[52:55], a[144:147], v[192:195]\n" \
  "ds_read_b64_tr_b16 v[40:41], v6 offset:20480\n" \
  "v_and_b32 v32, 31, v11\n" \
  "v_mfma_f32_16x16x32_bf16 v[196:199], v[52:55], a[148:151], v[196:199]\n" \
  "buffer_load_dwordx4 v29, s[44:47], s17 offen  lds\n" \
  "v_mfma_f32_16x16x32_bf16 v[196:199], v[48:51], a[132:135], v[196:199]\n" \
  "ds_read_b64_tr_b16 v[42:43], v6 offset:22528\n" \
  "v_lshlrev_b32 v33, 5, v32\n" \
  "v_mfma_f32_16x16x32_bf16 v[200:203], v[48:51], a[136:139], v[200:203]\n" \
  "buffer_load_dwordx4 a[64:67], v24, s[56:59], s6 offen offset:0 sc1\n" \
  "v_mfma_f32_16x16x32_bf16 v[200:203], v[52:55], a[152:155], v[200:203]\n" \
  "ds_read_b64_tr_b16 v[44:45], v6 offset:24576\n" \
  "s_lshl_b32 s89, s79, 2\n" \
  "v_mfma_f32_16x16x32_bf16 v[204:207], v[52:55], a[156:159], v[204:207]\n" \
  "ds_read_b64_tr_b16 v[46:47], v6 offset:26624\n" \
  "s_lshr_b32 s90, s78, 1\n" \
  "v_mfma_f32_16x16x32_bf16 v[204:207], v[48:51], a[140:143], v[204:207]\n" \
  "s_add_u32 s89, s89, s90\n" \
  "s_waitcnt lgkmcnt(4)\n" \
  "v_mfma_f32_16x16x32_bf16 v[208:211], v[56:59], a[128:131], v[208:211]\n" \
  "buffer_load_dwordx4 a[68:71], v24, s[56:59], s6 offen offset:1024 sc1\n" \
  "v_mfma_f32_16x16x32_bf16 v[208:211], v[60:63], a[144:147], v[208:211]\n" \
  "ds_read_b64_tr_b16 v[48:49], v6 offset:28672\n" \
  "s_add_u32 m0, s72, 86016\n" \
  "v_mfma_f32_16x16x32_bf16 v[212:215], v[60:63], a[148:151], v[212:215]\n" \
  "buffer_load_dwordx4 v30, s[44:47], s17 offen  lds\n" \
  "v_mfma_f32_16x16x32_bf16 v[212:215], v[56:59], a[132:135], v[212:215]\n" \
  "ds_read_b64_tr_b16 v[50:51], v6 offset:30720\n" \
  "s_mul_i32 s89, s89, 73728\n" \
  "v_mfma_f32_16x16x32_bf16 v[216:219], v[56:59], a[136:139], v[216:219]\n" \
  "buffer_load_dwordx4 a[72:75], v24, s[56:59], s6 offen offset:2048 sc1\n" \
  "v_mfma_f32_16x16x32_bf16 v[216:219], v[60:63], a[152:155], v[216:219]\n" \
  "ds_read_b64_tr_b16 v[52:53], v6 offset:32768\n" \
  "s_lshl_b32 s90, s77, 11\n" \
  "v_mfma_f32_16x16x32_bf16 v[220:223], v[60:63], a[156:159], v[220:223]\n" \
  "ds_read_b64_tr_b16 v[54:55], v6 offset:34816\n" \
  "s_add_u32 s89, s89, s90\n" \
  "v_mfma_f32_16x16x32_bf16 v[220:223], v[56:59], a[140:143], v[220:223]\n" \
  "s_and_b32 s90, s78, 1\n" \
  "s_waitcnt lgkmcnt(4)\n" \
  "v_mfma_f32_16x16x32_bf16 v[96:99], v[40:43], a[160:163], v[96:99]\n" \
  "s_add_u32 m0, s72, 90112\n" \
  "s_lshl_b32 s90, s90, 10\n" \
  "v_mfma_f32_16x16x32_bf16 v[96:99], v[44:47], a[176:179], v[96:99]\n" \
  "ds_read_b64_tr_b16 v[56:57], v7 offset:20480\n" \
  "s_add_u32 s89, s89, s90\n" \
  "v_mfma_f32_16x16x32_bf16 v[100:103], v[44:47], a[180:183], v[100:103]\n" \
  "buffer_load_dwordx4 v29, s[44:47], s18 offen  lds\n" \
  "v_mfma_f32_16x16x32_bf16 v[100:103], v[40:43], a[164:167], v[100:103]\n" \
  "ds_read_b64_tr_b16 v[58:59], v7 offset:22528\n" \
  "s_lshl_b32 s90, s80, 3\n" \
  "v_mfma_f32_16x16x32_bf16 v[104:107], v[40:43], a[168:171], v[104:107]\n" \
  "buffer_load_dwordx4 a[76:79], v24, s[56:59], s6 offen offset:3072 sc1\n" \
  "v_mfma_f32_16x16x32_bf16 v[104:107], v[44:47], a[184:187], v[104:107]\n" \
  "ds_read_b64_tr_b16 v[60:61], v7 offset:24576\n" \
  "s_add_u32 s89, s89, s90\n" \
  "v_mfma_f32_16x16x32_bf16 v[108:111], v[44:47], a[188:191], v[108:111]\n" \
  "ds_read_b64_tr_b16 v[62:63], v7 offset:26624\n" \
  "s_add_u32 s89, s89, 4521984\n" \
  "v_mfma_f32_16x16x32_bf16 v[108:111], v[40:43], a[172:175], v[108:111]\n" \
  "s_waitcnt lgkmcnt(4)\n" \
  "v_mfma_f32_16x16x32_bf16 v[112:115], v[48:51], a[160:163], v[112:115]\n" \
  "buffer_load_dwordx4 a[80:83], v25, s[56:59], s6 offen offset:0 sc1\n" \
  "v_mfma_f32_16x16x32_bf16 v[112:115], v[52:55], a[176:179], v[112:115]\n" \
  "ds_read_b64_tr_b16 v[40:41], v7 offset:28672\n" \
  "s_add_u32 m0, s72, 94208\n" \
  "v_mfma_f32_16x16x32_bf16 v[116:119], v[52:55], a[180:183], v[116:119]\n" \
  "buffer_load_dwordx4 v30, s[44:47], s18 offen  lds\n" \
  "v_mfma_f32_16x16x32_bf16 v[116:119], v[48:51], a[164:167], v[116:119]\n" \
  "ds_read_b64_tr_b16 v[42:43], v7 offset:30720\n" \
  "v_mfma_f32_16x16x32_bf16 v[120:123], v[48:51], a[168:171], v[120:123]\n" \
  "buffer_load_dwordx4 a[84:87], v25, s[56:59], s6 offen offset:1024 sc1\n" \
  "v_mfma_f32_16x16x32_bf16 v[120:123], v[52:55], a[184:187], v[120:123]\n" \
  "ds_read_b64_tr_b16 v[44:45], v7 offset:32768\n" \
  "v_mfma_f32_16x16x32_bf16 v[124:127], v[52:55], a[188:191], v[124:127]\n" \
  "ds_read_b64_tr_b16 v[46:47], v7 offset:34816\n" \
  "v_mfma_f32_16x16x32_bf16 v[124:127], v[48:51], a[172:175], v[124:127]\n" \
  "s_barrier\n" \
  "s_waitcnt lgkmcnt(4)\n" \
  "v_mfma_f32_16x16x32_bf16 v[128:131], v[56:59], a[160:163], v[128:131]\n" \
  "buffer_load_dwordx4 a[88:91], v25, s[56:59], s6 offen offset:2048 sc1\n" \
  "v_mfma_f32_16x16x32_bf16 v[128:131], v[60:63], a[176:179], v[128:131]\n" \
  "ds_read_b64_tr_b16 v[48:49], v2 offset:0\n" \
  "v_mfma_f32_16x16x32_bf16 v[132:135], v[60:63], a[180:183], v[132:135]\n" \
  "buffer_load_dwordx4 a[92:95], v25, s[56:59], s6 offen offset:3072 sc1\n" \
  "v_mfma_f32_16x16x32_bf16 v[132:135], v[56:59], a[164:167], v[132:135]\n" \
  "ds_read_b64_tr_b16 v[50:51], v2 offset:2048\n" \
  "v_mfma_f32_16x16x32_bf16 v[136:139], v[56:59], a[168:171], v[136:139]\n" \
  "buffer_load_dwordx2 v[250:251], v33, s[60:63], s89 offen sc1\n" \
  "v_mfma_f32_16x16x32_bf16 v[136:139], v[60:63], a[184:187], v[136:139]\n" \
  "ds_read_b64_tr_b16 v[52:53], v2 offset:4096\n" \
  "v_and_b32 v34, 7, v11\n" \
  "v_mfma_f32_16x16x32_bf16 v[140:143], v[60:63], a[188:191], v[140:143]\n" \
  "ds_read_b64_tr_b16 v[54:55], v2 offset:6144\n" \
  "v_lshlrev_b32 v34, 8, v34\n" \
  "v_mfma_f32_16x16x32_bf16 v[140:143], v[56:59], a[172:175], v[140:143]\n" \
  "v_lshrrev_b32 v32, 3, v32\n" \
  "s_waitcnt lgkmcnt(4)\n" \
  "v_mfma_f32_16x16x32_bf16 v[144:147], v[40:43], a[160:163], v[144:147]\n" \
  "s_add_u32 m0, s72, 118784\n" \
  "v_add_u32 v32, v32, v34\n" \
  "v_mfma_f32_16x16x32_bf16 v[144:147], v[44:47], a[176:179], v[144:147]\n" \
  "ds_read_b64_tr_b16 v[56:57], v2 offset:8192\n" \
  "v_mul_u32_u24 v237, 1152, v32\n" \
  "v_mfma_f32_16x16x32_bf16 v[148:151], v[44:47], a[180:183], v[148:151]\n" \
  "buffer_load_dwordx4 v31, s[60:63], s25 offen sc1 lds\n" \
  "v_mfma_f32_16x16x32_bf16 v[148:151], v[40:43], a[164:167], v[148:151]\n" \
  "ds_read_b64_tr_b16 v[58:59], v2 offset:10240\n" \
  "s_lshl_b32 s90, s79, 3\n" \
  "v_mfma_f32_16x16x32_bf16 v[152:155], v[40:43], a[168:171], v[152:155]\n" \
  "buffer_load_dwordx4 a[96:99], v24, s[56:59], s7 offen offset:0 sc1\n" \
  "v_mfma_f32_16x16x32_bf16 v[152:155], v[44:47], a[184:187], v[152:155]\n" \
  "ds_read_b64_tr_b16 v[60:61], v2 offset:12288\n" \
  "s_add_u32 s90, s90, s78\n" \
  "v_mfma_f32_16x16x32_bf16 v[156:159], v[44:47], a[188:191], v[156:159]\n" \
  "ds_read_b64_tr_b16 v[62:63], v2 offset:14336\n" \
  "s_mul_i32 s89, s90, 4608\n" \
  "v_mfma_f32_16x16x32_bf16 v[156:159], v[40:43], a[172:175], v[156:159]\n" \
  "s_add_u32 s89, s89, s91\n" \
  "s_waitcnt lgkmcnt(4)\n" \
  "v_mfma_f32_16x16x32_bf16 v[96:99], v[48:51], a[192:195], v[96:99]\n" \
  "s_add_u32 m0, s72, 102400\n" \
  "v_add_u32 v237, s89, v237\n" \
  "v_mfma_f32_16x16x32_bf16 v[96:99], v[52:55], a[208:211], v[96:99]\n" \
  "ds_read_b64_tr_b16 v[40:41], v3 offset:0\n" \
  "v_and_b32 v32, 32, v11\n" \
  "v_mfma_f32_16x16x32_bf16 v[100:103], v[52:55], a[212:215], v[100:103]\n" \
  "buffer_load_dwordx4 v29, s[44:47], s19 offen  lds\n" \
  "v_mfma_f32_16x16x32_bf16 v[100:103], v[48:51], a[196:199], v[100:103]\n" \
  "ds_read_b64_tr_b16 v[42:43], v3 offset:2048\n" \
  "v_lshl_add_u32 v237, v32, 24, v237\n" \
  "v_mfma_f32_16x16x32_bf16 v[104:107], v[48:51], a[200:203], v[104:107]\n" \
  "buffer_load_dwordx4 a[100:103], v24, s[56:59], s7 offen offset:1024 sc1\n" \
  "v_mfma_f32_16x16x32_bf16 v[104:107], v[52:55], a[216:219], v[104:107]\n" \
  "ds_read_b64_tr_b16 v[44:45], v3 offset:4096\n" \
  "v_bfe_u32 v32, v11, 1, 3\n" \
  "v_mfma_f32_16x16x32_bf16 v[108:111], v[52:55], a[220:223], v[108:111]\n" \
  "ds_read_b64_tr_b16 v[46:47], v3 offset:6144\n" \
  "v_lshlrev_b32 v32, 8, v32\n" \
  "v_mfma_f32_16x16x32_bf16 v[108:111], v[48:51], a[204:207], v[108:111]\n" \
  "v_lshrrev_b32 v34, 4, v11\n" \
  "s_waitcnt lgkmcnt(4)\n" \
  "v_mfma_f32_16x16x32_bf16 v[112:115], v[56:59], a[192:195], v[112:115]\n" \
  "buffer_load_dwordx4 a[104:107], v24, s[56:59], s7 offen offset:2048 sc1\n" \
  "v_mfma_f32_16x16x32_bf16 v[112:115], v[60:63], a[208:211], v[112:115]\n" \
  "ds_read_b64_tr_b16 v[48:49], v3 offset:8192\n" \
  "s_add_u32 m0, s72, 106496\n" \
  "v_mfma_f32_16x16x32_bf16 v[116:119], v[60:63], a[212:215], v[116:119]\n" \
  "buffer_load_dwordx4 v30, s[44:47], s19 offen  lds\n" \
  "v_mfma_f32_16x16x32_bf16 v[116:119], v[56:59], a[196:199], v[116:119]\n" \
  "ds_read_b64_tr_b16 v[50:51], v3 offset:10240\n" \
  "v_add_u32 v32, v32, v34\n" \
  "v_mfma_f32_16x16x32_bf16 v[120:123], v[56:59], a[200:203], v[120:123]\n" \
  "buffer_load_dwordx4 a[108:111], v24, s[56:59], s7 offen offset:3072 sc1\n" \
  "v_mfma_f32_16x16x32_bf16 v[120:123], v[60:63], a[216:219], v[120:123]\n" \
  "ds_read_b64_tr_b16 v[52:53], v3 offset:12288\n" \
  "v_mul_u32_u24 v236, 1152, v32\n" \
  "v_mfma_f32_16x16x32_bf16 v[124:127], v[60:63], a[220:223], v[124:127]\n" \
  "ds_read_b64_tr_b16 v[54:55], v3 offset:14336\n" \
  "v_and_b32 v32, 1, v11\n" \
  "v_mfma_f32_16x16x32_bf16 v[124:127], v[56:59], a[204:207], v[124:127]\n" \
  "s_barrier\n" \
  "s_waitcnt lgkmcnt(4)\n" \
  "v_mfma_f32_16x16x32_bf16 v[128:131], v[40:43], a[192:195], v[128:131]\n" \
  "s_add_u32 m0, s72, 110592\n" \
  "v_lshl_add_u32 v236, v32, 4, v236\n" \
  "v_mfma_f32_16x16x32_bf16 v[128:131], v[44:47], a[208:211], v[128:131]\n" \
  "ds_read_b64_tr_b16 v[56:57], v2 offset:20480\n" \
  "v_and_b32 v32, 15, v11\n" \
  "v_mfma_f32_16x16x32_bf16 v[132:135], v[44:47], a[212:215], v[132:135]\n" \
  "buffer_load_dwordx4 v29, s[44:47], s20 offen  lds\n" \
  "v_mfma_f32_16x16x32_bf16 v[132:135], v[40:43], a[196:199], v[132:135]\n" \
  "ds_read_b64_tr_b16 v[58:59], v2 offset:22528\n" \
  "v_lshlrev_b32 v32, 4, v32\n" \
  "v_mfma_f32_16x16x32_bf16 v[136:139], v[40:43], a[200:203], v[136:139]\n" \
  "buffer_load_dwordx4 a[112:115], v25, s[56:59], s7 offen offset:0 sc1\n" \
  "v_mfma_f32_16x16x32_bf16 v[136:139], v[44:47], a[216:219], v[136:139]\n" \
  "ds_read_b64_tr_b16 v[60:61], v2 offset:24576\n" \
  "v_lshl_add_u32 v238, v34, 10, v32\n" \
  "v_mfma_f32_16x16x32_bf16 v[140:143], v[44:47], a[220:223], v[140:143]\n" \
  "ds_read_b64_tr_b16 v[62:63], v2 offset:26624\n" \
  "s_lshl_b32 s89, s72, 1\n" \
  "v_mfma_f32_16x16x32_bf16 v[140:143], v[40:43], a[204:207], v[140:143]\n" \
  "s_add_u32 s89, s89, 61440\n" \
  "s_waitcnt lgkmcnt(4)\n" \
  "v_mfma_f32_16x16x32_bf16 v[144:147], v[48:51], a[192:195], v[144:147]\n" \
  "buffer_load_dwordx4 a[116:119], v25, s[56:59], s7 offen offset:1024 sc1\n" \
  "v_mfma_f32_16x16x32_bf16 v[144:147], v[52:55], a[208:211], v[144:147]\n" \
  "ds_read_b64_tr_b16 v[40:41], v2 offset:28672\n" \
  "s_add_u32 m0, s72, 114688\n" \
  "v_mfma_f32_16x16x32_bf16 v[148:151], v[52:55], a[212:215], v[148:151]\n" \
  "buffer_load_dwordx4 v30, s[44:47], s20 offen  lds\n" \
  "v_mfma_f32_16x16x32_bf16 v[148:151], v[48:51], a[196:199], v[148:151]\n" \
  "ds_read_b64_tr_b16 v[42:43], v2 offset:30720\n" \
  "v_add_u32 v254, s89, v254\n" \
  "v_mfma_f32_16x16x32_bf16 v[152:155], v[48:51], a[200:203], v[152:155]\n" \
  "buffer_load_dwordx4 a[120:123], v25, s[56:59], s7 offen offset:2048 sc1\n" \
  "v_mfma_f32_16x16x32_bf16 v[152:155], v[52:55], a[216:219], v[152:155]\n" \
  "ds_read_b64_tr_b16 v[44:45], v2 offset:32768\n" \
  "v_add_u32 v255, s89, v24\n" \
  "v_mfma_f32_16x16x32_bf16 v[156:159], v[52:55], a[220:223], v[156:159]\n" \
  "ds_read_b64_tr_b16 v[46:47], v2 offset:34816\n" \
  "s_lshr_b32 s89, s78, 1\n" \
  "v_mfma_f32_16x16x32_bf16 v[156:159], v[48:51], a[204:207], v[156:159]\n" \
  "s_sub_u32 s89, s89, s80\n" \
  "s_waitcnt lgkmcnt(4)\n" \
  "v_mfma_f32_16x16x32_bf16 v[96:99], v[56:59], a[224:227], v[96:99]\n" \
  "buffer_load_dwordx4 a[124:127], v25, s[56:59], s7 offen offset:3072 sc1\n" \
  "v_mfma_f32_16x16x32_bf16 v[96:99], v[60:63], a[240:243], v[96:99]\n" \
  "ds_read_b64_tr_b16 v[48:49], v3 offset:20480\n" \
  "v_bfe_u32 v32, v11, 3, 1\n" \
  "v_mfma_f32_16x16x32_bf16 v[100:103], v[60:63], a[244:247], v[100:103]\n" \
  "buffer_load_dwordx2 a[128:129], v8, s[56:59], s8 offen offset:0 sc1\n" \
  "v_mfma_f32_16x16x32_bf16 v[100:103], v[56:59], a[228:231], v[100:103]\n" \
  "ds_read_b64_tr_b16 v[50:51], v3 offset:22528\n" \
  "v_mul_i32_i24 v32, s89, v32\n" \
  "v_mfma_f32_16x16x32_bf16 v[104:107], v[56:59], a[232:235], v[104:107]\n" \
  "v_add_u32 v32, s80, v32\n" \
  "v_and_b32 v33, 7, v11\n" \
  "v_mfma_f32_16x16x32_bf16 v[104:107], v[60:63], a[248:251], v[104:107]\n" \
  "ds_read_b64_tr_b16 v[52:53], v3 offset:24576\n" \
  "v_lshl_add_u32 v32, v32, 3, v33\n" \
  "v_mfma_f32_16x16x32_bf16 v[108:111], v[60:63], a[252:255], v[108:111]\n" \
  "ds_read_b64_tr_b16 v[54:55], v3 offset:26624\n" \
  "v_lshlrev_b32 v239, 10, v32\n" \
  "v_mfma_f32_16x16x32_bf16 v[108:111], v[56:59], a[236:239], v[108:111]\n" \
  "v_lshrrev_b32 v32, 4, v11\n" \
  "s_waitcnt lgkmcnt(4)\n" \
  "v_mfma_f32_16x16x32_bf16 v[112:115], v[40:43], a[224:227], v[112:115]\n" \
  "s_add_u32 m0, s72, 139264\n" \
  "v_lshl_add_u32 v239, v32, 29, v239\n" \
  "v_mfma_f32_16x16x32_bf16 v[112:115], v[44:47], a[240:243], v[112:115]\n" \
  "ds_read_b64_tr_b16 v[56:57], v3 offset:28672\n" \
  "v_xor_b32 v33, s78, v33\n" \
  "v_mfma_f32_16x16x32_bf16 v[116:119], v[44:47], a[244:247], v[116:119]\n" \
  "buffer_load_dwordx4 v31, s[60:63], s26 offen sc1 lds\n" \
  "v_mfma_f32_16x16x32_bf16 v[116:119], v[40:43], a[228:231], v[116:119]\n" \
  "ds_read_b64_tr_b16 v[58:59], v3 offset:30720\n" \
  "s_lshl_b32 s89, s80, 3\n" \
  "v_mfma_f32_16x16x32_bf16 v[120:123], v[40:43], a[232:235], v[120:123]\n" \
  "buffer_load_dwordx2 a[130:131], v8, s[56:59], s8 offen offset:512 sc1\n" \
  "v_mfma_f32_16x16x32_bf16 v[120:123], v[44:47], a[248:251], v[120:123]\n" \
  "ds_read_b64_tr_b16 v[60:61], v3 offset:32768\n" \
  "v_add_lshl_u32 v14, v33, s89, 10\n" \
  "v_mfma_f32_16x16x32_bf16 v[124:127], v[44:47], a[252:255], v[124:127]\n" \
  "ds_read_b64_tr_b16 v[62:63], v3 offset:34816\n" \
  "s_lshl_b32 s52, s80, 3\n" \
  "v_mfma_f32_16x16x32_bf16 v[124:127], v[40:43], a[236:239], v[124:127]\n" \
  "s_add_u32 s52, s52, s78\n" \
  "s_waitcnt lgkmcnt(4)\n" \
  "v_mfma_f32_16x16x32_bf16 v[128:131], v[48:51], a[224:227], v[128:131]\n" \
  "v_mul_f32 v96, v96, v112\n" \
  "v_mul_f32 v97, v97, v113\n" \
  "v_mfma_f32_16x16x32_bf16 v[128:131], v[52:55], a[240:243], v[128:131]\n" \
  "ds_read_b64_tr_b16 v[40:41], v6 offset:20992\n" \
  "v_mul_f32 v98, v98, v114\n" \
  "v_mfma_f32_16x16x32_bf16 v[132:135], v[52:55], a[244:247], v[132:135]\n" \
  "v_mul_f32 v99, v99, v115\n" \
  "v_cvt_pk_bf16_f32 v64, v96, v97\n" \
  "v_mfma_f32_16x16x32_bf16 v[132:135], v[48:51], a[228:231], v[132:135]\n" \
  "ds_read_b64_tr_b16 v[42:43], v6 offset:23040\n" \
  "v_cvt_pk_bf16_f32 v65, v98, v99\n" \
  "v_mfma_f32_16x16x32_bf16 v[136:139], v[48:51], a[232:235], v[136:139]\n" \
  "v_mul_f32 v100, v100, v116\n" \
  "v_mul_f32 v101, v101, v117\n" \
  "v_mfma_f32_16x16x32_bf16 v[136:139], v[52:55], a[248:251], v[136:139]\n" \
  "ds_read_b64_tr_b16 v[44:45], v6 offset:25088\n" \
  "v_mul_f32 v102, v102, v118\n" \
  "v_mfma_f32_16x16x32_bf16 v[140:143], v[52:55], a[252:255], v[140:143]\n" \
  "ds_read_b64_tr_b16 v[46:47], v6 offset:27136\n" \
  "v_mul_f32 v103, v103, v119\n" \
  "v_mfma_f32_16x16x32_bf16 v[140:143], v[48:51], a[236:239], v[140:143]\n" \
  "v_cvt_pk_bf16_f32 v68, v100, v101\n" \
  "s_waitcnt lgkmcnt(4)\n" \
  "v_mfma_f32_16x16x32_bf16 v[144:147], v[56:59], a[224:227], v[144:147]\n" \
  "v_cvt_pk_bf16_f32 v69, v102, v103\n" \
  "v_mul_f32 v104, v104, v120\n" \
  "v_mfma_f32_16x16x32_bf16 v[144:147], v[60:63], a[240:243], v[144:147]\n" \
  "ds_read_b64_tr_b16 v[48:49], v6 offset:29184\n" \
  "v_mul_f32 v105, v105, v121\n" \
  "v_mfma_f32_16x16x32_bf16 v[148:151], v[60:63], a[244:247], v[148:151]\n" \
  "buffer_load_dwordx2 a[132:133], v8, s[56:59], s8 offen offset:1024 sc1\n" \
  "v_mfma_f32_16x16x32_bf16 v[148:151], v[56:59], a[228:231], v[148:151]\n" \
  "ds_read_b64_tr_b16 v[50:51], v6 offset:31232\n" \
  "v_mul_f32 v106, v106, v122\n" \
  "v_mfma_f32_16x16x32_bf16 v[152:155], v[56:59], a[232:235], v[152:155]\n" \
  "buffer_load_dwordx2 a[134:135], v8, s[56:59], s8 offen offset:1536 sc1\n" \
  "v_mfma_f32_16x16x32_bf16 v[152:155], v[60:63], a[248:251], v[152:155]\n" \
  "ds_read_b64_tr_b16 v[52:53], v6 offset:33280\n" \
  "v_mul_f32 v107, v107, v123\n" \
  "v_mfma_f32_16x16x32_bf16 v[156:159], v[60:63], a[252:255], v[156:159]\n" \
  "ds_read_b64_tr_b16 v[54:55], v6 offset:35328\n" \
  "v_cvt_pk_bf16_f32 v72, v104, v105\n" \
  "v_mfma_f32_16x16x32_bf16 v[156:159], v[56:59], a[236:239], v[156:159]\n" \
  "v_cvt_pk_bf16_f32 v73, v106, v107\n" \
  "s_lshl_b32 s52, s52, 10\n" \
  "s_add_u32 s52, s52, s87\n" \
  "s_lshl_b32 s53, s79, 2\n" \
  "s_add_u32 s53, s53, s80\n" \
  "s_mul_i32 s53, s53, 589824\n" \
  "s_add_u32 s53, s53, s83\n" \
  "s_add_u32 s53, s53, 6815744\n" \
  "s_waitcnt lgkmcnt(4)\n" \
  "v_mfma_f32_16x16x32_bf16 v[160:163], v[40:43], a[160:163], v[160:163]\n" \
  "v_mul_f32 v108, v108, v124\n" \
  "v_mul_f32 v109, v109, v125\n" \
  "v_mfma_f32_16x16x32_bf16 v[160:163], v[44:47], a[176:179], v[160:163]\n" \
  "ds_read_b64_tr_b16 v[56:57], v7 offset:20992\n" \
  "v_mul_f32 v110, v110, v126\n" \
  "v_mfma_f32_16x16x32_bf16 v[164:167], v[44:47], a[180:183], v[164:167]\n" \
  "v_mul_f32 v111, v111, v127\n" \
  "v_cvt_pk_bf16_f32 v76, v108, v109\n" \
  "v_mfma_f32_16x16x32_bf16 v[164:167], v[40:43], a[164:167], v[164:167]\n" \
  "ds_read_b64_tr_b16 v[58:59], v7 offset:23040\n" \
  "v_cvt_pk_bf16_f32 v77, v110, v111\n" \
  "v_mfma_f32_16x16x32_bf16 v[168:171], v[40:43], a[168:171], v[168:171]\n" \
  "v_mul_f32 v128, v128, v144\n" \
  "v_mul_f32 v129, v129, v145\n" \
  "v_mfma_f32_16x16x32_bf16 v[168:171], v[44:47], a[184:187], v[168:171]\n" \
  "ds_read_b64_tr_b16 v[60:61], v7 offset:25088\n" \
  "v_mul_f32 v130, v130, v146\n" \
  "v_mfma_f32_16x16x32_bf16 v[172:175], v[44:47], a[188:191], v[172:175]\n" \
  "ds_read_b64_tr_b16 v[62:63], v7 offset:27136\n" \
  "v_mul_f32 v131, v131, v147\n" \
  "v_mfma_f32_16x16x32_bf16 v[172:175], v[40:43], a[172:175], v[172:175]\n" \
  "v_cvt_pk_bf16_f32 v66, v128, v129\n" \
  "s_waitcnt lgkmcnt(4)\n" \
  "v_mfma_f32_16x16x32_bf16 v[176:179], v[48:51], a[160:163], v[176:179]\n" \
  "v_cvt_pk_bf16_f32 v67, v130, v131\n" \
  "v_mul_f32 v132, v132, v148\n" \
  "v_mfma_f32_16x16x32_bf16 v[176:179], v[52:55], a[176:179], v[176:179]\n" \
  "ds_read_b64_tr_b16 v[40:41], v7 offset:29184\n" \
  "v_mul_f32 v133, v133, v149\n" \
  "v_mfma_f32_16x16x32_bf16 v[180:183], v[52:55], a[180:183], v[180:183]\n" \
  "v_mul_f32 v134, v134, v150\n" \
  "v_mul_f32 v135, v135, v151\n" \
  "v_mfma_f32_16x16x32_bf16 v[180:183], v[48:51], a[164:167], v[180:183]\n" \
  "ds_read_b64_tr_b16 v[42:43], v7 offset:31232\n" \
  "v_cvt_pk_bf16_f32 v70, v132, v133\n" \
  "v_mfma_f32_16x16x32_bf16 v[184:187], v[48:51], a[168:171], v[184:187]\n" \
  "v_cvt_pk_bf16_f32 v71, v134, v135\n" \
  "v_mul_f32 v136, v136, v152\n" \
  "v_mfma_f32_16x16x32_bf16 v[184:187], v[52:55], a[184:187], v[184:187]\n" \
  "ds_read_b64_tr_b16 v[44:45], v7 offset:33280\n" \
  "v_mul_f32 v137, v137, v153\n" \
  "v_mfma_f32_16x16x32_bf16 v[188:191], v[52:55], a[188:191], v[188:191]\n" \
  "ds_read_b64_tr_b16 v[46:47], v7 offset:35328\n" \
  "v_mul_f32 v138, v138, v154\n" \
  "v_mfma_f32_16x16x32_bf16 v[188:191], v[48:51], a[172:175], v[188:191]\n" \
  "v_mul_f32 v139, v139, v155\n" \
  "s_waitcnt lgkmcnt(4)\n" \
  "v_mfma_f32_16x16x32_bf16 v[192:195], v[56:59], a[160:163], v[192:195]\n" \
  "buffer_load_dwordx2 a[136:137], v8, s[56:59], s8 offen offset:2048 sc1\n" \
  "v_mfma_f32_16x16x32_bf16 v[192:195], v[60:63], a[176:179], v[192:195]\n" \
  "ds_read_b64_tr_b16 v[48:49], v2 offset:512\n" \
  "v_cvt_pk_bf16_f32 v74, v136, v137\n" \
  "v_mfma_f32_16x16x32_bf16 v[196:199], v[60:63], a[180:183], v[196:199]\n" \
  "buffer_load_dwordx2 a[138:139], v8, s[56:59], s8 offen offset:2560 sc1\n" \
  "v_mfma_f32_16x16x32_bf16 v[196:199], v[56:59], a[164:167], v[196:199]\n" \
  "ds_read_b64_tr_b16 v[50:51], v2 offset:2560\n" \
  "v_cvt_pk_bf16_f32 v75, v138, v139\n" \
  "v_mfma_f32_16x16x32_bf16 v[200:203], v[56:59], a[168:171], v[200:203]\n" \
  "buffer_load_dwordx2 a[140:141], v8, s[56:59], s8 offen offset:3072 sc1\n" \
  "v_mfma_f32_16x16x32_bf16 v[200:203], v[60:63], a[184:187], v[200:203]\n" \
  "ds_read_b64_tr_b16 v[52:53], v2 offset:4608\n" \
  "v_mul_f32 v140, v140, v156\n" \
  "v_mfma_f32_16x16x32_bf16 v[204:207], v[60:63], a[188:191], v[204:207]\n" \
  "ds_read_b64_tr_b16 v[54:55], v2 offset:6656\n" \
  "v_mul_f32 v141, v141, v157\n" \
  "v_mfma_f32_16x16x32_bf16 v[204:207], v[56:59], a[172:175], v[204:207]\n" \
  "v_mul_f32 v142, v142, v158\n" \
  "s_waitcnt lgkmcnt(4)\n" \
  "v_mfma_f32_16x16x32_bf16 v[208:211], v[40:43], a[160:163], v[208:211]\n" \
  "v_mul_f32 v143, v143, v159\n" \
  "v_cvt_pk_bf16_f32 v78, v140, v141\n" \
  "v_mfma_f32_16x16x32_bf16 v[208:211], v[44:47], a[176:179], v[208:211]\n" \
  "ds_read_b64_tr_b16 v[56:57], v2 offset:8704\n" \
  "v_cvt_pk_bf16_f32 v79, v142, v143\n" \
  "v_mfma_f32_16x16x32_bf16 v[212:215], v[44:47], a[180:183], v[212:215]\n" \
  "buffer_store_dwordx4 v[64:67], v24, s[56:59], s9 offen offset:0 sc1\n" \
  "v_mfma_f32_16x16x32_bf16 v[212:215], v[40:43], a[164:167], v[212:215]\n" \
  "ds_read_b64_tr_b16 v[58:59], v2 offset:10752\n" \
  "s_add_u32 m0, s72, 122880\n" \
  "v_mfma_f32_16x16x32_bf16 v[216:219], v[40:43], a[168:171], v[216:219]\n" \
  "buffer_load_dwordx4 v29, s[44:47], s21 offen  lds\n" \
  "v_mfma_f32_16x16x32_bf16 v[216:219], v[44:47], a[184:187], v[216:219]\n" \
  "ds_read_b64_tr_b16 v[60:61], v2 offset:12800\n" \
  "v_mfma_f32_16x16x32_bf16 v[220:223], v[44:47], a[188:191], v[220:223]\n" \
  "ds_read_b64_tr_b16 v[62:63], v2 offset:14848\n" \
  "v_mfma_f32_16x16x32_bf16 v[220:223], v[40:43], a[172:175], v[220:223]\n" \
  "s_waitcnt lgkmcnt(4)\n" \
  "v_mfma_f32_16x16x32_bf16 v[160:163], v[48:51], a[192:195], v[160:163]\n" \
  "buffer_store_dwordx4 v[68:71], v24, s[56:59], s9 offen offset:1024 sc1\n" \
  "v_mfma_f32_16x16x32_bf16 v[160:163], v[52:55], a[208:211], v[160:163]\n" \
  "ds_read_b64_tr_b16 v[40:41], v3 offset:512\n" \
  "v_mfma_f32_16x16x32_bf16 v[164:167], v[52:55], a[212:215], v[164:167]\n" \
  "buffer_load_dwordx2 a[142:143], v8, s[56:59], s8 offen offset:3584 sc1\n" \
  "v_mfma_f32_16x16x32_bf16 v[164:167], v[48:51], a[196:199], v[164:167]\n" \
  "ds_read_b64_tr_b16 v[42:43], v3 offset:2560\n" \
  "v_mfma_f32_16x16x32_bf16 v[168:171], v[48:51], a[200:203], v[168:171]\n" \
  "buffer_load_dwordx2 a[144:145], v8, s[56:59], s76 offen offset:0 sc1\n" \
  "v_mfma_f32_16x16x32_bf16 v[168:171], v[52:55], a[216:219], v[168:171]\n" \
  "ds_read_b64_tr_b16 v[44:45], v3 offset:4608\n" \
  "v_mfma_f32_16x16x32_bf16 v[172:175], v[52:55], a[220:223], v[172:175]\n" \
  "ds_read_b64_tr_b16 v[46:47], v3 offset:6656\n" \
  "v_mfma_f32_16x16x32_bf16 v[172:175], v[48:51], a[204:207], v[172:175]\n" \
  "s_waitcnt lgkmcnt(4)\n" \
  "v_mfma_f32_16x16x32_bf16 v[176:179], v[56:59], a[192:195], v[176:179]\n" \
  "buffer_load_dwordx2 a[146:147], v8, s[56:59], s76 offen offset:512 sc1\n" \
  "v_mfma_f32_16x16x32_bf16 v[176:179], v[60:63], a[208:211], v[176:179]\n" \
  "ds_read_b64_tr_b16 v[48:49], v3 offset:8704\n" \
  "v_mfma_f32_16x16x32_bf16 v[180:183], v[60:63], a[212:215], v[180:183]\n" \
  "buffer_load_dwordx2 a[148:149], v8, s[56:59], s76 offen offset:1024 sc1\n" \
  "v_mfma_f32_16x16x32_bf16 v[180:183], v[56:59], a[196:199], v[180:183]\n" \
  "ds_read_b64_tr_b16 v[50:51], v3 offset:10752\n" \
  "v_mfma_f32_16x16x32_bf16 v[184:187], v[56:59], a[200:203], v[184:187]\n" \
  "buffer_load_dwordx2 a[150:151], v8, s[56:59], s76 offen offset:1536 sc1\n" \
  "v_mfma_f32_16x16x32_bf16 v[184:187], v[60:63], a[216:219], v[184:187]\n" \
  "ds_read_b64_tr_b16 v[52:53], v3 offset:12800\n" \
  "v_mfma_f32_16x16x32_bf16 v[188:191], v[60:63], a[220:223], v[188:191]\n" \
  "ds_read_b64_tr_b16 v[54:55], v3 offset:14848\n" \
  "v_mfma_f32_16x16x32_bf16 v[188:191], v[56:59], a[204:207], v[188:191]\n" \
  "s_barrier\n" \
  "s_waitcnt lgkmcnt(4)\n" \
  "v_mfma_f32_16x16x32_bf16 v[192:195], v[40:43], a[192:195], v[192:195]\n" \
  "buffer_store_dwordx4 v[72:75], v24, s[56:59], s9 offen offset:2048 sc1\n" \
  "v_mfma_f32_16x16x32_bf16 v[192:195], v[44:47], a[208:211], v[192:195]\n" \
  "ds_read_b64_tr_b16 v[56:57], v2 offset:20992\n" \
  "v_mfma_f32_16x16x32_bf16 v[196:199], v[44:47], a[212:215], v[196:199]\n" \
  "buffer_load_dwordx2 a[152:153], v8, s[56:59], s76 offen offset:2048 sc1\n" \
  "v_mfma_f32_16x16x32_bf16 v[196:199], v[40:43], a[196:199], v[196:199]\n" \
  "ds_read_b64_tr_b16 v[58:59], v2 offset:23040\n" \
  "s_add_u32 m0, s72, 126976\n" \
  "v_mfma_f32_16x16x32_bf16 v[200:203], v[40:43], a[200:203], v[200:203]\n" \
  "buffer_load_dwordx4 v30, s[44:47], s21 offen  lds\n" \
  "v_mfma_f32_16x16x32_bf16 v[200:203], v[44:47], a[216:219], v[200:203]\n" \
  "ds_read_b64_tr_b16 v[60:61], v2 offset:25088\n" \
  "v_mfma_f32_16x16x32_bf16 v[204:207], v[44:47], a[220:223], v[204:207]\n" \
  "ds_read_b64_tr_b16 v[62:63], v2 offset:27136\n" \
  "v_mfma_f32_16x16x32_bf16 v[204:207], v[40:43], a[204:207], v[204:207]\n" \
  "s_waitcnt lgkmcnt(4)\n" \
  "v_mfma_f32_16x16x32_bf16 v[208:211], v[48:51], a[192:195], v[208:211]\n" \
  "buffer_load_dwordx2 a[154:155], v8, s[56:59], s76 offen offset:2560 sc1\n" \
  "v_mfma_f32_16x16x32_bf16 v[208:211], v[52:55], a[208:211], v[208:211]\n" \
  "ds_read_b64_tr_b16 v[40:41], v2 offset:29184\n" \
  "v_mfma_f32_16x16x32_bf16 v[212:215], v[52:55], a[212:215], v[212:215]\n" \
  "buffer_load_dwordx2 a[156:157], v8, s[56:59], s76 offen offset:3072 sc1\n" \
  "v_mfma_f32_16x16x32_bf16 v[212:215], v[48:51], a[196:199], v[212:215]\n" \
  "ds_read_b64_tr_b16 v[42:43], v2 offset:31232\n" \
  "v_mfma_f32_16x16x32_bf16 v[216:219], v[48:51], a[200:203], v[216:219]\n" \
  "buffer_load_dwordx2 a[158:159], v8, s[56:59], s76 offen offset:3584 sc1\n" \
  "v_mfma_f32_16x16x32_bf16 v[216:219], v[52:55], a[216:219], v[216:219]\n" \
  "ds_read_b64_tr_b16 v[44:45], v2 offset:33280\n" \
  "v_mfma_f32_16x16x32_bf16 v[220:223], v[52:55], a[220:223], v[220:223]\n" \
  "ds_read_b64_tr_b16 v[46:47], v2 offset:35328\n" \
  "v_mfma_f32_16x16x32_bf16 v[220:223], v[48:51], a[204:207], v[220:223]\n" \
  "s_waitcnt lgkmcnt(4)\n" \
  "v_mfma_f32_16x16x32_bf16 v[160:163], v[56:59], a[224:227], v[160:163]\n" \
  "buffer_store_dwordx4 v[76:79], v24, s[56:59], s9 offen offset:3072 sc1\n" \
  "v_mfma_f32_16x16x32_bf16 v[160:163], v[60:63], a[240:243], v[160:163]\n" \
  "ds_read_b64_tr_b16 v[48:49], v3 offset:20992\n" \
  "s_add_u32 m0, s72, 143360\n" \
  "v_mfma_f32_16x16x32_bf16 v[164:167], v[60:63], a[244:247], v[164:167]\n" \
  "buffer_load_dwordx4 v29, s[48:51], s13 offen  lds\n" \
  "v_mfma_f32_16x16x32_bf16 v[164:167], v[56:59], a[228:231], v[164:167]\n" \
  "ds_read_b64_tr_b16 v[50:51], v3 offset:23040\n" \
  "s_add_u32 m0, s72, 147456\n" \
  "v_mfma_f32_16x16x32_bf16 v[168:171], v[56:59], a[232:235], v[168:171]\n" \
  "buffer_load_dwordx4 v30, s[48:51], s13 offen  lds\n" \
  "v_mfma_f32_16x16x32_bf16 v[168:171], v[60:63], a[248:251], v[168:171]\n" \
  "ds_read_b64_tr_b16 v[52:53], v3 offset:25088\n" \
  "v_mfma_f32_16x16x32_bf16 v[172:175], v[60:63], a[252:255], v[172:175]\n" \
  "ds_read_b64_tr_b16 v[54:55], v3 offset:27136\n" \
  "v_mfma_f32_16x16x32_bf16 v[172:175], v[56:59], a[236:239], v[172:175]\n" \
  "s_waitcnt lgkmcnt(4)\n" \
  "v_mfma_f32_16x16x32_bf16 v[176:179], v[40:43], a[224:227], v[176:179]\n" \
  "s_add_u32 m0, s72, 151552\n" \
  "v_mfma_f32_16x16x32_bf16 v[176:179], v[44:47], a[240:243], v[176:179]\n" \
  "ds_read_b64_tr_b16 v[56:57], v3 offset:29184\n" \
  "v_mfma_f32_16x16x32_bf16 v[180:183], v[44:47], a[244:247], v[180:183]\n" \
  "buffer_load_dwordx4 v29, s[48:51], s14 offen  lds\n" \
  "v_mfma_f32_16x16x32_bf16 v[180:183], v[40:43], a[228:231], v[180:183]\n" \
  "ds_read_b64_tr_b16 v[58:59], v3 offset:31232\n" \
  "v_mfma_f32_16x16x32_bf16 v[184:187], v[40:43], a[232:235], v[184:187]\n" \
  "v_mfma_f32_16x16x32_bf16 v[184:187], v[44:47], a[248:251], v[184:187]\n" \
  "ds_read_b64_tr_b16 v[60:61], v3 offset:33280\n" \
  "v_mfma_f32_16x16x32_bf16 v[188:191], v[44:47], a[252:255], v[188:191]\n" \
  "ds_read_b64_tr_b16 v[62:63], v3 offset:35328\n" \
  "v_mfma_f32_16x16x32_bf16 v[188:191], v[40:43], a[236:239], v[188:191]\n" \
  "s_waitcnt vmcnt(63)\n" \
  "s_barrier\n" \
  "s_waitcnt lgkmcnt(4)\n" \
  "v_mfma_f32_16x16x32_bf16 v[192:195], v[48:51], a[224:227], v[192:195]\n" \
  "v_mul_f32 v160, v160, v176\n" \
  "v_mul_f32 v161, v161, v177\n" \
  "v_mfma_f32_16x16x32_bf16 v[192:195], v[52:55], a[240:243], v[192:195]\n" \
  "ds_read_b64_tr_b16 v[40:41], v8 offset:57344\n" \
  "v_mul_f32 v162, v162, v178\n" \
  "v_mfma_f32_16x16x32_bf16 v[196:199], v[52:55], a[244:247], v[196:199]\n" \
  "v_mul_f32 v163, v163, v179\n" \
  "v_cvt_pk_bf16_f32 v80, v160, v161\n" \
  "v_mfma_f32_16x16x32_bf16 v[196:199], v[48:51], a[228:231], v[196:199]\n" \
  "ds_read_b64_tr_b16 v[42:43], v8 offset:57856\n" \
  "v_cvt_pk_bf16_f32 v81, v162, v163\n" \
  "v_mfma_f32_16x16x32_bf16 v[200:203], v[48:51], a[232:235], v[200:203]\n" \
  "v_mul_f32 v164, v164, v180\n" \
  "v_mul_f32 v165, v165, v181\n" \
  "v_mfma_f32_16x16x32_bf16 v[200:203], v[52:55], a[248:251], v[200:203]\n" \
  "ds_read_b64_tr_b16 v[44:45], v8 offset:58368\n" \
  "v_mul_f32 v166, v166, v182\n" \
  "v_mfma_f32_16x16x32_bf16 v[204:207], v[52:55], a[252:255], v[204:207]\n" \
  "ds_read_b64_tr_b16 v[46:47], v8 offset:58880\n" \
  "PG7_%=:\n" \
  "s_cbranch_execz HOPRET_%=\n" \
  "v_mul_f32 v167, v167, v183\n" \
  "v_mfma_f32_16x16x32_bf16 v[204:207], v[48:51], a[236:239], v[204:207]\n" \
  "v_cvt_pk_bf16_f32 v84, v164, v165\n" \
  "s_waitcnt lgkmcnt(4)\n" \
  "v_mfma_f32_16x16x32_bf16 v[208:211], v[56:59], a[224:227], v[208:211]\n" \
  "v_cvt_pk_bf16_f32 v85, v166, v167\n" \
  "v_mul_f32 v168, v168, v184\n" \
  "v_mfma_f32_16x16x32_bf16 v[208:211], v[60:63], a[240:243], v[208:211]\n" \
  "ds_read_b64_tr_b16 v[48:49], v3 offset:41472\n" \
  "v_mul_f32 v169, v169, v185\n" \
  "v_mfma_f32_16x16x32_bf16 v[212:215], v[60:63], a[244:247], v[212:215]\n" \
  "v_mul_f32 v170, v170, v186\n" \
  "v_mul_f32 v171, v171, v187\n" \
  "v_mfma_f32_16x16x32_bf16 v[212:215], v[56:59], a[228:231], v[212:215]\n" \
  "ds_read_b64_tr_b16 v[50:51], v3 offset:43520\n" \
  "v_cvt_pk_bf16_f32 v88, v168, v169\n" \
  "v_mfma_f32_16x16x32_bf16 v[216:219], v[56:59], a[232:235], v[216:219]\n" \
  "v_cvt_pk_bf16_f32 v89, v170, v171\n" \
  "v_mul_f32 v172, v172, v188\n" \
  "v_mfma_f32_16x16x32_bf16 v[216:219], v[60:63], a[248:251], v[216:219]\n" \
  "ds_read_b64_tr_b16 v[52:53], v3 offset:45568\n" \
  "v_mul_f32 v173, v173, v189\n" \
  "v_mfma_f32_16x16x32_bf16 v[220:223], v[60:63], a[252:255], v[220:223]\n" \
  "ds_read_b64_tr_b16 v[54:55], v3 offset:47616\n" \
  "v_mul_f32 v174, v174, v190\n" \
  "v_mfma_f32_16x16x32_bf16 v[220:223], v[56:59], a[236:239], v[220:223]\n" \
  "v_mul_f32 v175, v175, v191\n" \
  "s_waitcnt lgkmcnt(4)\n" \
  "v_mfma_f32_16x16x32_bf16 v[96:99], v[40:43], a[0:3], 0\n" \
  "v_cvt_pk_bf16_f32 v92, v172, v173\n" \
  "v_cvt_pk_bf16_f32 v93, v174, v175\n" \
  "v_mfma_f32_16x16x32_bf16 v[96:99], v[44:47], a[16:19], v[96:99]\n" \
  "ds_read_b64_tr_b16 v[56:57], v2 offset:41472\n" \
  "v_mul_f32 v192, v192, v208\n" \
  "v_mfma_f32_16x16x32_bf16 v[100:103], v[44:47], a[20:23], 0\n" \
  "v_mul_f32 v193, v193, v209\n" \
  "v_mul_f32 v194, v194, v210\n" \
  "v_mfma_f32_16x16x32_bf16 v[100:103], v[40:43], a[4:7], v[100:103]\n" \
  "ds_read_b64_tr_b16 v[58:59], v2 offset:43520\n" \
  "v_mul_f32 v195, v195, v211\n" \
  "v_mfma_f32_16x16x32_bf16 v[104:107], v[40:43], a[8:11], 0\n" \
  "v_cvt_pk_bf16_f32 v82, v192, v193\n" \
  "v_cvt_pk_bf16_f32 v83, v194, v195\n" \
  "v_mfma_f32_16x16x32_bf16 v[104:107], v[44:47], a[24:27], v[104:107]\n" \
  "ds_read_b64_tr_b16 v[60:61], v2 offset:45568\n" \
  "v_mul_f32 v196, v196, v212\n" \
  "v_mfma_f32_16x16x32_bf16 v[108:111], v[44:47], a[28:31], 0\n" \
  "ds_read_b64_tr_b16 v[62:63], v2 offset:47616\n" \
  "v_mul_f32 v197, v197, v213\n" \
  "v_mfma_f32_16x16x32_bf16 v[108:111], v[40:43], a[12:15], v[108:111]\n" \
  "v_mul_f32 v198, v198, v214\n" \
  "s_waitcnt lgkmcnt(4)\n" \
  "v_mfma_f32_16x16x32_bf16 v[112:115], v[48:51], a[0:3], 0\n" \
  "v_mul_f32 v199, v199, v215\n" \
  "v_cvt_pk_bf16_f32 v86, v196, v197\n" \
  "v_mfma_f32_16x16x32_bf16 v[112:115], v[52:55], a[16:19], v[112:115]\n" \
  "ds_read_b64_tr_b16 v[40:41], v3 offset:40960\n" \
  "v_cvt_pk_bf16_f32 v87, v198, v199\n" \
  "v_mfma_f32_16x16x32_bf16 v[116:119], v[52:55], a[20:23], 0\n" \
  "v_mul_f32 v200, v200, v216\n" \
  "v_mul_f32 v201, v201, v217\n" \
  "v_mfma_f32_16x16x32_bf16 v[116:119], v[48:51], a[4:7], v[116:119]\n" \
  "ds_read_b64_tr_b16 v[42:43], v3 offset:43008\n" \
  "v_mul_f32 v202, v202, v218\n" \
  "v_mfma_f32_16x16x32_bf16 v[120:123], v[48:51], a[8:11], 0\n" \
  "v_mul_f32 v203, v203, v219\n" \
  "v_cvt_pk_bf16_f32 v90, v200, v201\n" \
  "v_mfma_f32_16x16x32_bf16 v[120:123], v[52:55], a[24:27], v[120:123]\n" \
  "ds_read_b64_tr_b16 v[44:45], v3 offset:45056\n" \
  "v_cvt_pk_bf16_f32 v91, v202, v203\n" \
  "v_mfma_f32_16x16x32_bf16 v[124:127], v[52:55], a[28:31], 0\n" \
  "ds_read_b64_tr_b16 v[46:47], v3 offset:47104\n" \
  "v_mul_f32 v204, v204, v220\n" \
  "v_mfma_f32_16x16x32_bf16 v[124:127], v[48:51], a[12:15], v[124:127]\n" \
  "v_mul_f32 v205, v205, v221\n" \
  "s_waitcnt lgkmcnt(4)\n" \
  "v_mfma_f32_16x16x32_bf16 v[128:131], v[56:59], a[0:3], 0\n" \
  "v_mul_f32 v206, v206, v222\n" \
  "v_mul_f32 v207, v207, v223\n" \
  "v_mfma_f32_16x16x32_bf16 v[128:131], v[60:63], a[16:19], v[128:131]\n" \
  "ds_read_b64_tr_b16 v[48:49], v2 offset:40960\n" \
  "v_cvt_pk_bf16_f32 v94, v204, v205\n" \
  "v_mfma_f32_16x16x32_bf16 v[132:135], v[60:63], a[20:23], 0\n" \
  "v_cvt_pk_bf16_f32 v95, v206, v207\n" \
  "s_waitcnt vmcnt(3)\n" \
  "v_mfma_f32_16x16x32_bf16 v[132:135], v[56:59], a[4:7], v[132:135]\n" \
  "ds_read_b64_tr_b16 v[50:51], v2 offset:43008\n" \
  "s_add_u32 m0, s72, 155648\n" \
  "v_mfma_f32_16x16x32_bf16 v[136:139], v[56:59], a[8:11], 0\n" \
  "buffer_store_dword v28, v1, s[60:63], s81 offen offset:1024 sc1\n" \
  "v_mfma_f32_16x16x32_bf16 v[136:139], v[60:63], a[24:27], v[136:139]\n" \
  "ds_read_b64_tr_b16 v[52:53], v2 offset:45056\n" \
  "v_mfma_f32_16x16x32_bf16 v[140:143], v[60:63], a[28:31], 0\n" \
  "ds_read_b64_tr_b16 v[54:55], v2 offset:47104\n" \
  "v_mfma_f32_16x16x32_bf16 v[140:143], v[56:59], a[12:15], v[140:143]\n" \
  "s_barrier\n" \
  "s_waitcnt lgkmcnt(4)\n" \
  "v_mfma_f32_16x16x32_bf16 v[144:147], v[40:43], a[0:3], 0\n" \
  "buffer_store_dwordx4 v[80:83], v25, s[56:59], s9 offen offset:0 sc1\n" \
  "v_mfma_f32_16x16x32_bf16 v[144:147], v[44:47], a[16:19], v[144:147]\n" \
  "ds_read_b64_tr_b16 v[56:57], v8 offset:59392\n" \
  "v_mfma_f32_16x16x32_bf16 v[148:151], v[44:47], a[20:23], 0\n" \
  "buffer_store_dwordx4 v[84:87], v25, s[56:59], s9 offen offset:1024 sc1\n" \
  "v_mfma_f32_16x16x32_bf16 v[148:151], v[40:43], a[4:7], v[148:151]\n" \
  "ds_read_b64_tr_b16 v[58:59], v8 offset:59904\n" \
  "v_mfma_f32_16x16x32_bf16 v[152:155], v[40:43], a[8:11], 0\n" \
  "buffer_load_dwordx4 v30, s[48:51], s14 offen  lds\n" \
  "v_mfma_f32_16x16x32_bf16 v[152:155], v[44:47], a[24:27], v[152:155]\n" \
  "ds_read_b64_tr_b16 v[60:61], v8 offset:60416\n" \
  "s_add_u32 m0, s72, 159744\n" \
  "v_mfma_f32_16x16x32_bf16 v[156:159], v[44:47], a[28:31], 0\n" \
  "ds_read_b64_tr_b16 v[62:63], v8 offset:60928\n" \
  "v_mfma_f32_16x16x32_bf16 v[156:159], v[40:43], a[12:15], v[156:159]\n" \
  "s_waitcnt lgkmcnt(4)\n" \
  "v_mfma_f32_16x16x32_bf16 v[160:163], v[48:51], a[0:3], 0\n" \
  "buffer_store_dwordx4 v[88:91], v25, s[56:59], s9 offen offset:2048 sc1\n" \
  "v_mfma_f32_16x16x32_bf16 v[160:163], v[52:55], a[16:19], v[160:163]\n" \
  "ds_read_b64_tr_b16 v[40:41], v3 offset:49664\n" \
  "v_mfma_f32_16x16x32_bf16 v[164:167], v[52:55], a[20:23], 0\n" \
  "buffer_load_dwordx4 v29, s[48:51], s15 offen  lds\n" \
  "v_mfma_f32_16x16x32_bf16 v[164:167], v[48:51], a[4:7], v[164:167]\n" \
  "ds_read_b64_tr_b16 v[42:43], v3 offset:51712\n" \
  "v_mfma_f32_16x16x32_bf16 v[168:171], v[48:51], a[8:11], 0\n" \
  "v_mfma_f32_16x16x32_bf16 v[168:171], v[52:55], a[24:27], v[168:171]\n" \
  "ds_read_b64_tr_b16 v[44:45], v3 offset:53760\n" \
  "v_mfma_f32_16x16x32_bf16 v[172:175], v[52:55], a[28:31], 0\n" \
  "ds_read_b64_tr_b16 v[46:47], v3 offset:55808\n" \
  "v_mfma_f32_16x16x32_bf16 v[172:175], v[48:51], a[12:15], v[172:175]\n" \
  "s_waitcnt lgkmcnt(4)\n" \
  "v_mfma_f32_16x16x32_bf16 v[96:99], v[56:59], a[32:35], v[96:99]\n" \
  "buffer_store_dwordx4 v[92:95], v25, s[56:59], s9 offen offset:3072 sc1\n" \
  "v_mfma_f32_16x16x32_bf16 v[96:99], v[60:63], a[48:51], v[96:99]\n" \
  "ds_read_b64_tr_b16 v[48:49], v2 offset:49664\n" \
  "v_mfma_f32_16x16x32_bf16 v[100:103], v[60:63], a[52:55], v[100:103]\n" \
  "v_mfma_f32_16x16x32_bf16 v[100:103], v[56:59], a[36:39], v[100:103]\n" \
  "ds_read_b64_tr_b16 v[50:51], v2 offset:51712\n" \
  "v_mfma_f32_16x16x32_bf16 v[104:107], v[56:59], a[40:43], v[104:107]\n" \
  "v_mfma_f32_16x16x32_bf16 v[104:107], v[60:63], a[56:59], v[104:107]\n" \
  "ds_read_b64_tr_b16 v[52:53], v2 offset:53760\n" \
  "v_mfma_f32_16x16x32_bf16 v[108:111], v[60:63], a[60:63], v[108:111]\n" \
  "ds_read_b64_tr_b16 v[54:55], v2 offset:55808\n" \
  "v_mfma_f32_16x16x32_bf16 v[108:111], v[56:59], a[44:47], v[108:111]\n" \
  "s_waitcnt lgkmcnt(4)\n" \
  "v_mfma_f32_16x16x32_bf16 v[112:115], v[40:43], a[32:35], v[112:115]\n" \
  "s_add_u32 m0, s72, 0\n" \
  "v_mfma_f32_16x16x32_bf16 v[112:115], v[44:47], a[48:51], v[112:115]\n" \
  "ds_read_b64_tr_b16 v[56:57], v3 offset:49152\n" \
  "v_mfma_f32_16x16x32_bf16 v[116:119], v[44:47], a[52:55], v[116:119]\n" \
  "buffer_load_dwordx4 v30, s[48:51], s15 offen  lds\n" \
  "v_mfma_f32_16x16x32_bf16 v[116:119], v[40:43], a[36:39], v[116:119]\n" \
  "ds_read_b64_tr_b16 v[58:59], v3 offset:51200\n" \
  "s_add_u32 m0, s72, 4096\n" \
  "v_mfma_f32_16x16x32_bf16 v[120:123], v[40:43], a[40:43], v[120:123]\n" \
  "buffer_load_dwordx4 v29, s[48:51], s16 offen  lds\n" \
  "v_mfma_f32_16x16x32_bf16 v[120:123], v[44:47], a[56:59], v[120:123]\n" \
  "ds_read_b64_tr_b16 v[60:61], v3 offset:53248\n" \
  "v_mfma_f32_16x16x32_bf16 v[124:127], v[44:47], a[60:63], v[124:127]\n" \
  "ds_read_b64_tr_b16 v[62:63], v3 offset:55296\n" \
  "v_mfma_f32_16x16x32_bf16 v[124:127], v[40:43], a[44:47], v[124:127]\n" \
  "s_waitcnt lgkmcnt(4)\n" \
  "v_mfma_f32_16x16x32_bf16 v[128:131], v[48:51], a[32:35], v[128:131]\n" \
  "buffer_load_dword v27, v26, s[60:63], s86 offen offset:1024 sc1\n" \
  "v_mfma_f32_16x16x32_bf16 v[128:131], v[52:55], a[48:51], v[128:131]\n" \
  "ds_read_b64_tr_b16 v[40:41], v2 offset:49152\n" \
  "s_add_u32 m0, s72, 8192\n" \
  "v_mfma_f32_16x16x32_bf16 v[132:135], v[52:55], a[52:55], v[132:135]\n" \
  "buffer_load_dwordx4 v30, s[48:51], s16 offen  lds\n" \
  "v_mfma_f32_16x16x32_bf16 v[132:135], v[48:51], a[36:39], v[132:135]\n" \
  "ds_read_b64_tr_b16 v[42:43], v2 offset:51200\n" \
  "s_add_u32 m0, s72, 12288\n" \
  "v_mfma_f32_16x16x32_bf16 v[136:139], v[48:51], a[40:43], v[136:139]\n" \
  "buffer_load_dwordx4 v29, s[48:51], s17 offen  lds\n" \
  "v_mfma_f32_16x16x32_bf16 v[136:139], v[52:55], a[56:59], v[136:139]\n" \
  "ds_read_b64_tr_b16 v[44:45], v2 offset:53248\n" \
  "v_mfma_f32_16x16x32_bf16 v[140:143], v[52:55], a[60:63], v[140:143]\n" \
  "ds_read_b64_tr_b16 v[46:47], v2 offset:55296\n" \
  "v_mfma_f32_16x16x32_bf16 v[140:143], v[48:51], a[44:47], v[140:143]\n" \
  "s_barrier\n" \
  "s_waitcnt lgkmcnt(4)\n" \
  "v_mfma_f32_16x16x32_bf16 v[144:147], v[56:59], a[32:35], v[144:147]\n" \
  "s_add_u32 m0, s72, 16384\n" \
  "v_mfma_f32_16x16x32_bf16 v[144:147], v[60:63], a[48:51], v[144:147]\n" \
  "ds_read_b64_tr_b16 v[48:49], v9 offset:16384\n" \
  "v_mfma_f32_16x16x32_bf16 v[148:151], v[60:63], a[52:55], v[148:151]\n" \
  "buffer_load_dwordx4 v30, s[48:51], s17 offen  lds\n" \
  "v_mfma_f32_16x16x32_bf16 v[148:151], v[56:59], a[36:39], v[148:151]\n" \
  "ds_read_b64_tr_b16 v[50:51], v9 offset:16896\n" \
  "v_mfma_f32_16x16x32_bf16 v[152:155], v[56:59], a[40:43], v[152:155]\n" \
  "v_mfma_f32_16x16x32_bf16 v[152:155], v[60:63], a[56:59], v[152:155]\n" \
  "ds_read_b64_tr_b16 v[52:53], v9 offset:17408\n" \
  "v_mfma_f32_16x16x32_bf16 v[156:159], v[60:63], a[60:63], v[156:159]\n" \
  "ds_read_b64_tr_b16 v[54:55], v9 offset:17920\n" \
  "v_mfma_f32_16x16x32_bf16 v[156:159], v[56:59], a[44:47], v[156:159]\n" \
  "s_waitcnt lgkmcnt(4)\n" \
  "v_mfma_f32_16x16x32_bf16 v[160:163], v[40:43], a[32:35], v[160:163]\n" \
  "v_mfma_f32_16x16x32_bf16 v[160:163], v[44:47], a[48:51], v[160:163]\n" \
  "ds_read_b64_tr_b16 v[56:57], v5 offset:512\n" \
  "v_mfma_f32_16x16x32_bf16 v[164:167], v[44:47], a[52:55], v[164:167]\n" \
  "v_mfma_f32_16x16x32_bf16 v[164:167], v[40:43], a[36:39], v[164:167]\n" \
  "ds_read_b64_tr_b16 v[58:59], v5 offset:2560\n" \
  "v_mfma_f32_16x16x32_bf16 v[168:171], v[40:43], a[40:43], v[168:171]\n" \
  "v_mfma_f32_16x16x32_bf16 v[168:171], v[44:47], a[56:59], v[168:171]\n" \
  "ds_read_b64_tr_b16 v[60:61], v5 offset:4608\n" \
  "v_mfma_f32_16x16x32_bf16 v[172:175], v[44:47], a[60:63], v[172:175]\n" \
  "ds_read_b64_tr_b16 v[62:63], v5 offset:6656\n" \
  "v_mfma_f32_16x16x32_bf16 v[172:175], v[40:43], a[44:47], v[172:175]\n" \
  "s_waitcnt lgkmcnt(4)\n" \
  "v_mfma_f32_16x16x32_bf16 v[96:99], v[48:51], a[64:67], v[96:99]\n" \
  "v_mfma_f32_16x16x32_bf16 v[96:99], v[52:55], a[80:83], v[96:99]\n" \
  "ds_read_b64_tr_b16 v[40:41], v4 offset:512\n" \
  "v_mfma_f32_16x16x32_bf16 v[100:103], v[52:55], a[84:87], v[100:103]\n" \
  "v_mfma_f32_16x16x32_bf16 v[100:103], v[48:51], a[68:71], v[100:103]\n" \
  "ds_read_b64_tr_b16 v[42:43], v4 offset:2560\n" \
  "v_mfma_f32_16x16x32_bf16 v[104:107], v[48:51], a[72:75], v[104:107]\n" \
  "v_mfma_f32_16x16x32_bf16 v[104:107], v[52:55], a[88:91], v[104:107]\n" \
  "ds_read_b64_tr_b16 v[44:45], v4 offset:4608\n" \
  "v_mfma_f32_16x16x32_bf16 v[108:111], v[52:55], a[92:95], v[108:111]\n" \
  "ds_read_b64_tr_b16 v[46:47], v4 offset:6656\n" \
  "v_mfma_f32_16x16x32_bf16 v[108:111], v[48:51], a[76:79], v[108:111]\n" \
  "s_waitcnt lgkmcnt(4)\n" \
  "v_mfma_f32_16x16x32_bf16 v[112:115], v[56:59], a[64:67], v[112:115]\n" \
  "v_mfma_f32_16x16x32_bf16 v[112:115], v[60:63], a[80:83], v[112:115]\n" \
  "ds_read_b64_tr_b16 v[48:49], v5 offset:0\n" \
  "v_mfma_f32_16x16x32_bf16 v[116:119], v[60:63], a[84:87], v[116:119]\n" \
  "v_mfma_f32_16x16x32_bf16 v[116:119], v[56:59], a[68:71], v[116:119]\n" \
  "ds_read_b64_tr_b16 v[50:51], v5 offset:2048\n" \
  "v_mfma_f32_16x16x32_bf16 v[120:123], v[56:59], a[72:75], v[120:123]\n" \
  "v_mfma_f32_16x16x32_bf16 v[120:123], v[60:63], a[88:91], v[120:123]\n" \
  "ds_read_b64_tr_b16 v[52:53], v5 offset:4096\n" \
  "v_mfma_f32_16x16x32_bf16 v[124:127], v[60:63], a[92:95], v[124:127]\n" \
  "ds_read_b64_tr_b16 v[54:55], v5 offset:6144\n" \
  "v_mfma_f32_16x16x32_bf16 v[124:127], v[56:59], a[76:79], v[124:127]\n" \
  "s_waitcnt lgkmcnt(4)\n" \
  "v_mfma_f32_16x16x32_bf16 v[128:131], v[40:43], a[64:67], v[128:131]\n" \
  "s_waitcnt vmcnt(6)\n" \
  "v_mfma_f32_16x16x32_bf16 v[128:131], v[44:47], a[80:83], v[128:131]\n" \
  "ds_read_b64_tr_b16 v[56:57], v4 offset:0\n" \
  "v_mfma_f32_16x16x32_bf16 v[132:135], v[44:47], a[84:87], v[132:135]\n" \
  "buffer_store_dword v28, v1, s[60:63], s81 offen offset:512 sc1\n" \
  "v_mfma_f32_16x16x32_bf16 v[132:135], v[40:43], a[68:71], v[132:135]\n" \
  "ds_read_b64_tr_b16 v[58:59], v4 offset:2048\n" \
  "s_waitcnt vmcnt(4)\n" \
  "v_mfma_f32_16x16x32_bf16 v[136:139], v[40:43], a[72:75], v[136:139]\n" \
  "v_cmp_ne_u32 vcc, s84, v27\n" \
  "s_and_b32 vcc_lo, vcc_lo, 0xe\n" \
  "s_cbranch_scc1 SLOW_HA_%=\n" \
  "BACK_HA_%=:\n" \
  "v_mfma_f32_16x16x32_bf16 v[136:139], v[44:47], a[88:91], v[136:139]\n" \
  "ds_read_b64_tr_b16 v[60:61], v4 offset:4096\n" \
  "v_mfma_f32_16x16x32_bf16 v[140:143], v[44:47], a[92:95], v[140:143]\n" \
  "ds_read_b64_tr_b16 v[62:63], v4 offset:6144\n" \
  "v_mfma_f32_16x16x32_bf16 v[140:143], v[40:43], a[76:79], v[140:143]\n" \
  "s_waitcnt lgkmcnt(4)\n" \
  "v_mfma_f32_16x16x32_bf16 v[144:147], v[48:51], a[64:67], v[144:147]\n" \
  "buffer_load_dwordx4 a[160:163], v24, s[56:59], s10 offen offset:0 sc1\n" \
  "v_mfma_f32_16x16x32_bf16 v[144:147], v[52:55], a[80:83], v[144:147]\n" \
  "ds_read_b64_tr_b16 v[40:41], v9 offset:18432\n" \
  "v_mfma_f32_16x16x32_bf16 v[148:151], v[52:55], a[84:87], v[148:151]\n" \
  "buffer_load_dwordx4 a[164:167], v24, s[56:59], s10 offen offset:1024 sc1\n" \
  "v_mfma_f32_16x16x32_bf16 v[148:151], v[48:51], a[68:71], v[148:151]\n" \
  "ds_read_b64_tr_b16 v[42:43], v9 offset:18944\n" \
  "v_mfma_f32_16x16x32_bf16 v[152:155], v[48:51], a[72:75], v[152:155]\n" \
  "buffer_load_dwordx4 a[168:171], v24, s[56:59], s10 offen offset:2048 sc1\n" \
  "v_mfma_f32_16x16x32_bf16 v[152:155], v[52:55], a[88:91], v[152:155]\n" \
  "ds_read_b64_tr_b16 v[44:45], v9 offset:19456\n" \
  "v_mfma_f32_16x16x32_bf16 v[156:159], v[52:55], a[92:95], v[156:159]\n" \
  "ds_read_b64_tr_b16 v[46:47], v9 offset:19968\n" \
  "v_mfma_f32_16x16x32_bf16 v[156:159], v[48:51], a[76:79], v[156:159]\n" \
  "s_waitcnt lgkmcnt(4)\n" \
  "v_mfma_f32_16x16x32_bf16 v[160:163], v[56:59], a[64:67], v[160:163]\n" \
  "buffer_load_dwordx4 a[172:175], v24, s[56:59], s10 offen offset:3072 sc1\n" \
  "v_mfma_f32_16x16x32_bf16 v[160:163], v[60:63], a[80:83], v[160:163]\n" \
  "ds_read_b64_tr_b16 v[48:49], v5 offset:8704\n" \
  "v_mfma_f32_16x16x32_bf16 v[164:167], v[60:63], a[84:87], v[164:167]\n" \
  "buffer_load_dwordx4 a[192:195], v24, s[56:59], s11 offen offset:0 sc1\n" \
  "v_mfma_f32_16x16x32_bf16 v[164:167], v[56:59], a[68:71], v[164:167]\n" \
  "ds_read_b64_tr_b16 v[50:51], v5 offset:10752\n" \
  "v_mfma_f32_16x16x32_bf16 v[168:171], v[56:59], a[72:75], v[168:171]\n" \
  "buffer_load_dwordx4 a[196:199], v24, s[56:59], s11 offen offset:1024 sc1\n" \
  "v_mfma_f32_16x16x32_bf16 v[168:171], v[60:63], a[88:91], v[168:171]\n" \
  "ds_read_b64_tr_b16 v[52:53], v5 offset:12800\n" \
  "v_mfma_f32_16x16x32_bf16 v[172:175], v[60:63], a[92:95], v[172:175]\n" \
  "ds_read_b64_tr_b16 v[54:55], v5 offset:14848\n" \
  "v_mfma_f32_16x16x32_bf16 v[172:175], v[56:59], a[76:79], v[172:175]\n" \
  "s_waitcnt lgkmcnt(4)\n" \
  "v_mfma_f32_16x16x32_bf16 v[96:99], v[40:43], a[96:99], v[96:99]\n" \
  "buffer_load_dwordx4 a[200:203], v24, s[56:59], s11 offen offset:2048 sc1\n" \
  "v_mfma_f32_16x16x32_bf16 v[96:99], v[44:47], a[112:115], v[96:99]\n" \
  "ds_read_b64_tr_b16 v[56:57], v4 offset:8704\n" \
  "v_mfma_f32_16x16x32_bf16 v[100:103], v[44:47], a[116:119], v[100:103]\n" \
  "buffer_load_dwordx4 a[204:207], v24, s[56:59], s11 offen offset:3072 sc1\n" \
  "v_mfma_f32_16x16x32_bf16 v[100:103], v[40:43], a[100:103], v[100:103]\n" \
  "ds_read_b64_tr_b16 v[58:59], v4 offset:10752\n" \
  "v_mfma_f32_16x16x32_bf16 v[104:107], v[40:43], a[104:107], v[104:107]\n" \
  "buffer_load_dwordx4 a[224:227], v24, s[56:59], s12 offen offset:0 sc1\n" \
  "v_mfma_f32_16x16x32_bf16 v[104:107], v[44:47], a[120:123], v[104:107]\n" \
  "ds_read_b64_tr_b16 v[60:61], v4 offset:12800\n" \
  "v_mfma_f32_16x16x32_bf16 v[108:111], v[44:47], a[124:127], v[108:111]\n" \
  "ds_read_b64_tr_b16 v[62:63], v4 offset:14848\n" \
  "v_mfma_f32_16x16x32_bf16 v[108:111], v[40:43], a[108:111], v[108:111]\n" \
  "s_waitcnt lgkmcnt(4)\n" \
  "v_mfma_f32_16x16x32_bf16 v[112:115], v[48:51], a[96:99], v[112:115]\n" \
  "buffer_load_dwordx4 a[228:231], v24, s[56:59], s12 offen offset:1024 sc1\n" \
  "v_mfma_f32_16x16x32_bf16 v[112:115], v[52:55], a[112:115], v[112:115]\n" \
  "ds_read_b64_tr_b16 v[40:41], v5 offset:8192\n" \
  "v_mfma_f32_16x16x32_bf16 v[116:119], v[52:55], a[116:119], v[116:119]\n" \
  "buffer_load_dwordx4 a[232:235], v24, s[56:59], s12 offen offset:2048 sc1\n" \
  "v_mfma_f32_16x16x32_bf16 v[116:119], v[48:51], a[100:103], v[116:119]\n" \
  "ds_read_b64_tr_b16 v[42:43], v5 offset:10240\n" \
  "v_mfma_f32_16x16x32_bf16 v[120:123], v[48:51], a[104:107], v[120:123]\n" \
  "buffer_load_dwordx4 a[236:239], v24, s[56:59], s12 offen offset:3072 sc1\n" \
  "v_mfma_f32_16x16x32_bf16 v[120:123], v[52:55], a[120:123], v[120:123]\n" \
  "ds_read_b64_tr_b16 v[44:45], v5 offset:12288\n" \
  "v_mfma_f32_16x16x32_bf16 v[124:127], v[52:55], a[124:127], v[124:127]\n" \
  "ds_read_b64_tr_b16 v[46:47], v5 offset:14336\n" \
  "v_mfma_f32_16x16x32_bf16 v[124:127], v[48:51], a[108:111], v[124:127]\n" \
  "s_waitcnt lgkmcnt(4)\n" \
  "v_mfma_f32_16x16x32_bf16 v[128:131], v[56:59], a[96:99], v[128:131]\n" \
  "buffer_load_dword v27, v26, s[60:63], s86 offen offset:512 sc1\n" \
  "v_mfma_f32_16x16x32_bf16 v[128:131], v[60:63], a[112:115], v[128:131]\n" \
  "ds_read_b64_tr_b16 v[48:49], v4 offset:8192\n" \
  "v_mfma_f32_16x16x32_bf16 v[132:135], v[60:63], a[116:119], v[132:135]\n" \
  "v_mfma_f32_16x16x32_bf16 v[132:135], v[56:59], a[100:103], v[132:135]\n" \
  "ds_read_b64_tr_b16 v[50:51], v4 offset:10240\n" \
  "v_mfma_f32_16x16x32_bf16 v[136:139], v[56:59], a[104:107], v[136:139]\n" \
  "v_mfma_f32_16x16x32_bf16 v[136:139], v[60:63], a[120:123], v[136:139]\n" \
  "ds_read_b64_tr_b16 v[52:53], v4 offset:12288\n" \
  "v_mfma_f32_16x16x32_bf16 v[140:143], v[60:63], a[124:127], v[140:143]\n" \
  "ds_read_b64_tr_b16 v[54:55], v4 offset:14336\n" \
  "v_mfma_f32_16x16x32_bf16 v[140:143], v[56:59], a[108:111], v[140:143]\n" \
  "s_barrier\n" \
  "s_waitcnt lgkmcnt(4)\n" \
  "v_mfma_f32_16x16x32_bf16 v[144:147], v[40:43], a[96:99], v[144:147]\n" \
  "v_mfma_f32_16x16x32_bf16 v[144:147], v[44:47], a[112:115], v[144:147]\n" \
  "ds_read_b64_tr_b16 v[56:57], v9 offset:36864\n" \
  "v_mfma_f32_16x16x32_bf16 v[148:151], v[44:47], a[116:119], v[148:151]\n" \
  "v_mfma_f32_16x16x32_bf16 v[148:151], v[40:43], a[100:103], v[148:151]\n" \
  "ds_read_b64_tr_b16 v[58:59], v9 offset:37376\n" \
  "v_mfma_f32_16x16x32_bf16 v[152:155], v[40:43], a[104:107], v[152:155]\n" \
  "v_mfma_f32_16x16x32_bf16 v[152:155], v[44:47], a[120:123], v[152:155]\n" \
  "ds_read_b64_tr_b16 v[60:61], v9 offset:37888\n" \
  "v_mfma_f32_16x16x32_bf16 v[156:159], v[44:47], a[124:127], v[156:159]\n" \
  "ds_read_b64_tr_b16 v[62:63], v9 offset:38400\n" \
  "v_mfma_f32_16x16x32_bf16 v[156:159], v[40:43], a[108:111], v[156:159]\n" \
  "s_waitcnt lgkmcnt(4)\n" \
  "v_mfma_f32_16x16x32_bf16 v[160:163], v[48:51], a[96:99], v[160:163]\n" \
  "v_mfma_f32_16x16x32_bf16 v[160:163], v[52:55], a[112:115], v[160:163]\n" \
  "ds_read_b64_tr_b16 v[40:41], v5 offset:20992\n" \
  "v_mfma_f32_16x16x32_bf16 v[164:167], v[52:55], a[116:119], v[164:167]\n" \
  "v_mfma_f32_16x16x32_bf16 v[164:167], v[48:51], a[100:103], v[164:167]\n" \
  "ds_read_b64_tr_b16 v[42:43], v5 offset:23040\n" \
  "v_mfma_f32_16x16x32_bf16 v[168:171], v[48:51], a[104:107], v[168:171]\n" \
  "v_mfma_f32_16x16x32_bf16 v[168:171], v[52:55], a[120:123], v[168:171]\n" \
  "ds_read_b64_tr_b16 v[44:45], v5 offset:25088\n" \
  "v_mfma_f32_16x16x32_bf16 v[172:175], v[52:55], a[124:127], v[172:175]\n" \
  "ds_read_b64_tr_b16 v[46:47], v5 offset:27136\n" \
  "v_mfma_f32_16x16x32_bf16 v[172:175], v[48:51], a[108:111], v[172:175]\n" \
  "s_waitcnt lgkmcnt(4)\n" \
  "v_mfma_f32_16x16x32_bf16 v[96:99], v[56:59], a[128:131], v[96:99]\n" \
  "v_mfma_f32_16x16x32_bf16 v[96:99], v[60:63], a[144:147], v[96:99]\n" \
  "ds_read_b64_tr_b16 v[48:49], v4 offset:20992\n" \
  "v_mfma_f32_16x16x32_bf16 v[100:103], v[60:63], a[148:151], v[100:103]\n" \
  "v_mfma_f32_16x16x32_bf16 v[100:103], v[56:59], a[132:135], v[100:103]\n" \
  "ds_read_b64_tr_b16 v[50:51], v4 offset:23040\n" \
  "v_mfma_f32_16x16x32_bf16 v[104:107], v[56:59], a[136:139], v[104:107]\n" \
  "v_mfma_f32_16x16x32_bf16 v[104:107], v[60:63], a[152:155], v[104:107]\n" \
  "ds_read_b64_tr_b16 v[52:53], v4 offset:25088\n" \
  "v_mfma_f32_16x16x32_bf16 v[108:111], v[60:63], a[156:159], v[108:111]\n" \
  "ds_read_b64_tr_b16 v[54:55], v4 offset:27136\n" \
  "v_mfma_f32_16x16x32_bf16 v[108:111], v[56:59], a[140:143], v[108:111]\n" \
  "s_waitcnt lgkmcnt(4)\n" \
  "v_mfma_f32_16x16x32_bf16 v[112:115], v[40:43], a[128:131], v[112:115]\n" \
  "s_waitcnt vmcnt(0)\n" \
  "v_mfma_f32_16x16x32_bf16 v[112:115], v[44:47], a[144:147], v[112:115]\n" \
  "ds_read_b64_tr_b16 v[56:57], v5 offset:20480\n" \
  "v_mfma_f32_16x16x32_bf16 v[116:119], v[44:47], a[148:151], v[116:119]\n" \
  "v_cmp_ne_u32 vcc, s84, v27\n" \
  "s_and_b32 vcc_lo, vcc_lo, 0xe\n" \
  "s_cbranch_scc1 SLOW_HL_%=\n" \
  "BACK_HL_%=:\n" \
  "v_mfma_f32_16x16x32_bf16 v[116:119], v[40:43], a[132:135], v[116:119]\n" \
  "ds_read_b64_tr_b16 v[58:59], v5 offset:22528\n" \
  "v_mfma_f32_16x16x32_bf16 v[120:123], v[40:43], a[136:139], v[120:123]\n" \
  "buffer_load_dwordx4 a[176:179], v25, s[56:59], s10 offen offset:0 sc1\n" \
  "v_mfma_f32_16x16x32_bf16 v[120:123], v[44:47], a[152:155], v[120:123]\n" \
  "ds_read_b64_tr_b16 v[60:61], v5 offset:24576\n" \
  "v_mfma_f32_16x16x32_bf16 v[124:127], v[44:47], a[156:159], v[124:127]\n" \
  "ds_read_b64_tr_b16 v[62:63], v5 offset:26624\n" \
  "v_mfma_f32_16x16x32_bf16 v[124:127], v[40:43], a[140:143], v[124:127]\n" \
  "s_waitcnt lgkmcnt(4)\n" \
  "v_mfma_f32_16x16x32_bf16 v[128:131], v[48:51], a[128:131], v[128:131]\n" \
  "buffer_load_dwordx4 a[180:183], v25, s[56:59], s10 offen offset:1024 sc1\n" \
  "v_mfma_f32_16x16x32_bf16 v[128:131], v[52:55], a[144:147], v[128:131]\n" \
  "ds_read_b64_tr_b16 v[40:41], v4 offset:20480\n" \
  "v_mfma_f32_16x16x32_bf16 v[132:135], v[52:55], a[148:151], v[132:135]\n" \
  "buffer_load_dwordx4 a[184:187], v25, s[56:59], s10 offen offset:2048 sc1\n" \
  "v_mfma_f32_16x16x32_bf16 v[132:135], v[48:51], a[132:135], v[132:135]\n" \
  "ds_read_b64_tr_b16 v[42:43], v4 offset:22528\n" \
  "v_mfma_f32_16x16x32_bf16 v[136:139], v[48:51], a[136:139], v[136:139]\n" \
  "buffer_load_dwordx4 a[188:191], v25, s[56:59], s10 offen offset:3072 sc1\n" \
  "v_mfma_f32_16x16x32_bf16 v[136:139], v[52:55], a[152:155], v[136:139]\n" \
  "ds_read_b64_tr_b16 v[44:45], v4 offset:24576\n" \
  "v_mfma_f32_16x16x32_bf16 v[140:143], v[52:55], a[156:159], v[140:143]\n" \
  "ds_read_b64_tr_b16 v[46:47], v4 offset:26624\n" \
  "v_mfma_f32_16x16x32_bf16 v[140:143], v[48:51], a[140:143], v[140:143]\n" \
  "s_waitcnt lgkmcnt(4)\n" \
  "v_mfma_f32_16x16x32_bf16 v[144:147], v[56:59], a[128:131], v[144:147]\n" \
  "buffer_load_dwordx4 a[208:211], v25, s[56:59], s11 offen offset:0 sc1\n" \
  "v_mfma_f32_16x16x32_bf16 v[144:147], v[60:63], a[144:147], v[144:147]\n" \
  "ds_read_b64_tr_b16 v[48:49], v9 offset:38912\n" \
  "v_mfma_f32_16x16x32_bf16 v[148:151], v[60:63], a[148:151], v[148:151]\n" \
  "buffer_load_dwordx4 a[212:215], v25, s[56:59], s11 offen offset:1024 sc1\n" \
  "v_mfma_f32_16x16x32_bf16 v[148:151], v[56:59], a[132:135], v[148:151]\n" \
  "ds_read_b64_tr_b16 v[50:51], v9 offset:39424\n" \
  "v_mfma_f32_16x16x32_bf16 v[152:155], v[56:59], a[136:139], v[152:155]\n" \
  "buffer_load_dwordx4 a[216:219], v25, s[56:59], s11 offen offset:2048 sc1\n" \
  "v_mfma_f32_16x16x32_bf16 v[152:155], v[60:63], a[152:155], v[152:155]\n" \
  "ds_read_b64_tr_b16 v[52:53], v9 offset:39936\n" \
  "v_mfma_f32_16x16x32_bf16 v[156:159], v[60:63], a[156:159], v[156:159]\n" \
  "ds_read_b64_tr_b16 v[54:55], v9 offset:40448\n" \
  "v_mfma_f32_16x16x32_bf16 v[156:159], v[56:59], a[140:143], v[156:159]\n" \
  "s_waitcnt lgkmcnt(4)\n" \
  "v_mfma_f32_16x16x32_bf16 v[160:163], v[40:43], a[128:131], v[160:163]\n" \
  "buffer_load_dwordx4 a[220:223], v25, s[56:59], s11 offen offset:3072 sc1\n" \
  "v_mfma_f32_16x16x32_bf16 v[160:163], v[44:47], a[144:147], v[160:163]\n" \
  "ds_read_b64_tr_b16 v[56:57], v5 offset:29184\n" \
  "v_mfma_f32_16x16x32_bf16 v[164:167], v[44:47], a[148:151], v[164:167]\n" \
  "buffer_load_dwordx4 a[240:243], v25, s[56:59], s12 offen offset:0 sc1\n" \
  "v_mfma_f32_16x16x32_bf16 v[164:167], v[40:43], a[132:135], v[164:167]\n" \
  "ds_read_b64_tr_b16 v[58:59], v5 offset:31232\n" \
  "v_mfma_f32_16x16x32_bf16 v[168:171], v[40:43], a[136:139], v[168:171]\n" \
  "buffer_load_dwordx4 a[244:247], v25, s[56:59], s12 offen offset:1024 sc1\n" \
  "v_mfma_f32_16x16x32_bf16 v[168:171], v[44:47], a[152:155], v[168:171]\n" \
  "ds_read_b64_tr_b16 v[60:61], v5 offset:33280\n" \
  "v_mfma_f32_16x16x32_bf16 v[172:175], v[44:47], a[156:159], v[172:175]\n" \
  "ds_read_b64_tr_b16 v[62:63], v5 offset:35328\n" \
  "v_mfma_f32_16x16x32_bf16 v[172:175], v[40:43], a[140:143], v[172:175]\n" \
  "s_waitcnt lgkmcnt(4)\n" \
  "v_mfma_f32_16x16x32_bf16 v[96:99], v[48:51], v[64:67], v[96:99]\n" \
  "buffer_load_dwordx4 a[248:251], v25, s[56:59], s12 offen offset:2048 sc1\n" \
  "v_mfma_f32_16x16x32_bf16 v[96:99], v[52:55], v[80:83], v[96:99]\n" \
  "ds_read_b64_tr_b16 v[40:41], v4 offset:29184\n" \
  "v_mfma_f32_16x16x32_bf16 v[100:103], v[52:55], v[84:87], v[100:103]\n" \
  "buffer_load_dwordx4 a[252:255], v25, s[56:59], s12 offen offset:3072 sc1\n" \
  "v_mfma_f32_16x16x32_bf16 v[100:103], v[48:51], v[68:71], v[100:103]\n" \
  "ds_read_b64_tr_b16 v[42:43], v4 offset:31232\n" \
  "v_mfma_f32_16x16x32_bf16 v[104:107], v[48:51], v[72:75], v[104:107]\n" \
  "v_mfma_f32_16x16x32_bf16 v[104:107], v[52:55], v[88:91], v[104:107]\n" \
  "ds_read_b64_tr_b16 v[44:45], v4 offset:33280\n" \
  "v_mfma_f32_16x16x32_bf16 v[108:111], v[52:55], v[92:95], v[108:111]\n" \
  "ds_read_b64_tr_b16 v[46:47], v4 offset:35328\n" \
  "v_mfma_f32_16x16x32_bf16 v[108:111], v[48:51], v[76:79], v[108:111]\n" \
  "s_waitcnt lgkmcnt(4)\n" \
  "v_mfma_f32_16x16x32_bf16 v[112:115], v[56:59], v[64:67], v[112:115]\n" \
  "v_mfma_f32_16x16x32_bf16 v[112:115], v[60:63], v[80:83], v[112:115]\n" \
  "ds_read_b64_tr_b16 v[48:49], v5 offset:28672\n" \
  "v_mfma_f32_16x16x32_bf16 v[116:119], v[60:63], v[84:87], v[116:119]\n" \
  "v_mfma_f32_16x16x32_bf16 v[116:119], v[56:59], v[68:71], v[116:119]\n" \
  "ds_read_b64_tr_b16 v[50:51], v5 offset:30720\n" \
  "v_mfma_f32_16x16x32_bf16 v[120:123], v[56:59], v[72:75], v[120:123]\n" \
  "v_mfma_f32_16x16x32_bf16 v[120:123], v[60:63], v[88:91], v[120:123]\n" \
  "ds_read_b64_tr_b16 v[52:53], v5 offset:32768\n" \
  "v_mfma_f32_16x16x32_bf16 v[124:127], v[60:63], v[92:95], v[124:127]\n" \
  "ds_read_b64_tr_b16 v[54:55], v5 offset:34816\n" \
  "v_mfma_f32_16x16x32_bf16 v[124:127], v[56:59], v[76:79], v[124:127]\n" \
  "s_waitcnt lgkmcnt(4)\n" \
  "v_mfma_f32_16x16x32_bf16 v[128:131], v[40:43], v[64:67], v[128:131]\n" \
  "v_mfma_f32_16x16x32_bf16 v[128:131], v[44:47], v[80:83], v[128:131]\n" \
  "ds_read_b64_tr_b16 v[56:57], v4 offset:28672\n" \
  "v_mfma_f32_16x16x32_bf16 v[132:135], v[44:47], v[84:87], v[132:135]\n" \
  "v_mfma_f32_16x16x32_bf16 v[132:135], v[40:43], v[68:71], v[132:135]\n" \
  "ds_read_b64_tr_b16 v[58:59], v4 offset:30720\n" \
  "v_mfma_f32_16x16x32_bf16 v[136:139], v[40:43], v[72:75], v[136:139]\n" \
  "v_mfma_f32_16x16x32_bf16 v[136:139], v[44:47], v[88:91], v[136:139]\n" \
  "ds_read_b64_tr_b16 v[60:61], v4 offset:32768\n" \
  "v_mfma_f32_16x16x32_bf16 v[140:143], v[44:47], v[92:95], v[140:143]\n" \
  "ds_read_b64_tr_b16 v[62:63], v4 offset:34816\n" \
  "v_mfma_f32_16x16x32_bf16 v[140:143], v[40:43], v[76:79], v[140:143]\n" \
  "s_barrier\n" \
  "s_waitcnt lgkmcnt(4)\n" \
  "v_mfma_f32_16x16x32_bf16 v[144:147], v[48:51], v[64:67], v[144:147]\n" \
  "s_add_u32 m0, s72, 20480\n" \
  "v_mfma_f32_16x16x32_bf16 v[144:147], v[52:55], v[80:83], v[144:147]\n" \
  "ds_read_b64_tr_b16 v[40:41], v9 offset:57344\n" \
  "v_mfma_f32_16x16x32_bf16 v[148:151], v[52:55], v[84:87], v[148:151]\n" \
  "buffer_load_dwordx4 v29, s[48:51], s18 offen  lds\n" \
  "v_mfma_f32_16x16x32_bf16 v[148:151], v[48:51], v[68:71], v[148:151]\n" \
  "ds_read_b64_tr_b16 v[42:43], v9 offset:57856\n" \
  "s_add_u32 m0, s72, 24576\n" \
  "v_mfma_f32_16x16x32_bf16 v[152:155], v[48:51], v[72:75], v[152:155]\n" \
  "buffer_load_dwordx4 v30, s[48:51], s18 offen  lds\n" \
  "v_mfma_f32_16x16x32_bf16 v[152:155], v[52:55], v[88:91], v[152:155]\n" \
  "ds_read_b64_tr_b16 v[44:45], v9 offset:58368\n" \
  "v_mfma_f32_16x16x32_bf16 v[156:159], v[52:55], v[92:95], v[156:159]\n" \
  "ds_read_b64_tr_b16 v[46:47], v9 offset:58880\n" \
  "v_mfma_f32_16x16x32_bf16 v[156:159], v[48:51], v[76:79], v[156:159]\n" \
  "s_waitcnt lgkmcnt(4)\n" \
  "v_mfma_f32_16x16x32_bf16 v[160:163], v[56:59], v[64:67], v[160:163]\n" \
  "s_add_u32 m0, s72, 28672\n" \
  "v_mfma_f32_16x16x32_bf16 v[160:163], v[60:63], v[80:83], v[160:163]\n" \
  "ds_read_b64_tr_b16 v[48:49], v9 offset:59392\n" \
  "v_mfma_f32_16x16x32_bf16 v[164:167], v[60:63], v[84:87], v[164:167]\n" \
  "buffer_load_dwordx4 v29, s[48:51], s19 offen  lds\n" \
  "v_mfma_f32_16x16x32_bf16 v[164:167], v[56:59], v[68:71], v[164:167]\n" \
  "ds_read_b64_tr_b16 v[50:51], v9 offset:59904\n" \
  "v_mfma_f32_16x16x32_bf16 v[168:171], v[56:59], v[72:75], v[168:171]\n" \
  "v_mfma_f32_16x16x32_bf16 v[168:171], v[60:63], v[88:91], v[168:171]\n" \
  "ds_read_b64_tr_b16 v[52:53], v9 offset:60416\n" \
  "v_mfma_f32_16x16x32_bf16 v[172:175], v[60:63], v[92:95], v[172:175]\n" \
  "ds_read_b64_tr_b16 v[54:55], v9 offset:60928\n" \
  "v_mfma_f32_16x16x32_bf16 v[172:175], v[56:59], v[76:79], v[172:175]\n" \
  "s_waitcnt vmcnt(11) lgkmcnt(4)\n" \
  "v_mfma_f32_16x16x32_bf16 v[96:99], v[40:43], a[160:163], v[96:99]\n" \
  "s_add_u32 m0, s72, 32768\n" \
  "v_mfma_f32_16x16x32_bf16 v[96:99], v[44:47], a[176:179], v[96:99]\n" \
  "ds_read_b64_tr_b16 v[56:57], v10 offset:16384\n" \
  "v_mfma_f32_16x16x32_bf16 v[100:103], v[44:47], a[180:183], v[100:103]\n" \
  "buffer_load_dwordx4 v30, s[48:51], s19 offen  lds\n" \
  "v_mfma_f32_16x16x32_bf16 v[100:103], v[40:43], a[164:167], v[100:103]\n" \
  "ds_read_b64_tr_b16 v[58:59], v10 offset:16896\n" \
  "v_mfma_f32_16x16x32_bf16 v[104:107], v[40:43], a[168:171], v[104:107]\n" \
  "v_mfma_f32_16x16x32_bf16 v[104:107], v[44:47], a[184:187], v[104:107]\n" \
  "ds_read_b64_tr_b16 v[60:61], v10 offset:17408\n" \
  "v_mfma_f32_16x16x32_bf16 v[108:111], v[44:47], a[188:191], v[108:111]\n" \
  "ds_read_b64_tr_b16 v[62:63], v10 offset:17920\n" \
  "v_mfma_f32_16x16x32_bf16 v[108:111], v[40:43], a[172:175], v[108:111]\n" \
  "s_waitcnt vmcnt(8) lgkmcnt(4)\n" \
  "v_mfma_f32_16x16x32_bf16 v[96:99], v[48:51], a[192:195], v[96:99]\n" \
  "s_add_u32 m0, s72, 36864\n" \
  "v_mfma_f32_16x16x32_bf16 v[96:99], v[52:55], a[208:211], v[96:99]\n" \
  "ds_read_b64_tr_b16 v[40:41], v5 offset:41472\n" \
  "v_mfma_f32_16x16x32_bf16 v[100:103], v[52:55], a[212:215], v[100:103]\n" \
  "buffer_load_dwordx4 v29, s[48:51], s20 offen  lds\n" \
  "v_mfma_f32_16x16x32_bf16 v[100:103], v[48:51], a[196:199], v[100:103]\n" \
  "ds_read_b64_tr_b16 v[42:43], v5 offset:43520\n" \
  "v_mfma_f32_16x16x32_bf16 v[104:107], v[48:51], a[200:203], v[104:107]\n" \
  "v_mfma_f32_16x16x32_bf16 v[104:107], v[52:55], a[216:219], v[104:107]\n" \
  "ds_read_b64_tr_b16 v[44:45], v5 offset:45568\n" \
  "v_mfma_f32_16x16x32_bf16 v[108:111], v[52:55], a[220:223], v[108:111]\n" \
  "ds_read_b64_tr_b16 v[46:47], v5 offset:47616\n" \
  "v_mfma_f32_16x16x32_bf16 v[108:111], v[48:51], a[204:207], v[108:111]\n" \
  "s_waitcnt vmcnt(5) lgkmcnt(4)\n" \
  "v_mfma_f32_16x16x32_bf16 v[96:99], v[56:59], a[224:227], v[96:99]\n" \
  "s_add_u32 m0, s72, 40960\n" \
  "v_mfma_f32_16x16x32_bf16 v[96:99], v[60:63], a[240:243], v[96:99]\n" \
  "ds_read_b64_tr_b16 v[48:49], v5 offset:49664\n" \
  "v_mfma_f32_16x16x32_bf16 v[100:103], v[60:63], a[244:247], v[100:103]\n" \
  "buffer_load_dwordx4 v30, s[48:51], s20 offen  lds\n" \
  "v_mfma_f32_16x16x32_bf16 v[100:103], v[56:59], a[228:231], v[100:103]\n" \
  "ds_read_b64_tr_b16 v[50:51], v5 offset:51712\n" \
  "v_mfma_f32_16x16x32_bf16 v[104:107], v[56:59], a[232:235], v[104:107]\n" \
  "PG8_%=:\n" \
  "s_cbranch_execz HOPRET_%=\n" \
  "v_mfma_f32_16x16x32_bf16 v[104:107], v[60:63], a[248:251], v[104:107]\n" \
  "ds_read_b64_tr_b16 v[52:53], v5 offset:53760\n" \
  "v_mfma_f32_16x16x32_bf16 v[108:111], v[60:63], a[252:255], v[108:111]\n" \
  "ds_read_b64_tr_b16 v[54:55], v5 offset:55808\n" \
  "v_mfma_f32_16x16x32_bf16 v[108:111], v[56:59], a[236:239], v[108:111]\n" \
  "s_waitcnt lgkmcnt(4)\n" \
  "v_mfma_f32_16x16x32_bf16 v[112:115], v[40:43], a[160:163], v[112:115]\n" \
  "v_cvt_pk_bf16_f32 v96, v96, v97\n" \
  "v_cvt_pk_bf16_f32 v97, v98, v99\n" \
  "v_mfma_f32_16x16x32_bf16 v[112:115], v[44:47], a[176:179], v[112:115]\n" \
  "ds_read_b64_tr_b16 v[56:57], v7 offset:512\n" \
  "v_cvt_pk_bf16_f32 v98, v100, v101\n" \
  "v_mfma_f32_16x16x32_bf16 v[116:119], v[44:47], a[180:183], v[116:119]\n" \
  "v_cvt_pk_bf16_f32 v99, v102, v103\n" \
  "s_add_u32 m0, s72, 45056\n" \
  "v_mfma_f32_16x16x32_bf16 v[116:119], v[40:43], a[164:167], v[116:119]\n" \
  "ds_read_b64_tr_b16 v[58:59], v7 offset:2560\n" \
  "v_mfma_f32_16x16x32_bf16 v[120:123], v[40:43], a[168:171], v[120:123]\n" \
  "buffer_store_dwordx4 v[96:99], v24, s[60:63], s97 offen offset:0 sc1\n" \
  "v_mfma_f32_16x16x32_bf16 v[120:123], v[44:47], a[184:187], v[120:123]\n" \
  "ds_read_b64_tr_b16 v[60:61], v7 offset:4608\n" \
  "v_mfma_f32_16x16x32_bf16 v[124:127], v[44:47], a[188:191], v[124:127]\n" \
  "ds_read_b64_tr_b16 v[62:63], v7 offset:6656\n" \
  "v_mfma_f32_16x16x32_bf16 v[124:127], v[40:43], a[172:175], v[124:127]\n" \
  "s_waitcnt lgkmcnt(4)\n" \
  "v_mfma_f32_16x16x32_bf16 v[112:115], v[48:51], a[192:195], v[112:115]\n" \
  "buffer_load_dwordx4 v29, s[48:51], s21 offen  lds\n" \
  "v_mfma_f32_16x16x32_bf16 v[112:115], v[52:55], a[208:211], v[112:115]\n" \
  "ds_read_b64_tr_b16 v[40:41], v4 offset:41472\n" \
  "v_cvt_pk_bf16_f32 v104, v104, v105\n" \
  "v_mfma_f32_16x16x32_bf16 v[116:119], v[52:55], a[212:215], v[116:119]\n" \
  "v_cvt_pk_bf16_f32 v105, v106, v107\n" \
  "v_cvt_pk_bf16_f32 v106, v108, v109\n" \
  "v_mfma_f32_16x16x32_bf16 v[116:119], v[48:51], a[196:199], v[116:119]\n" \
  "ds_read_b64_tr_b16 v[42:43], v4 offset:43520\n" \
  "v_cvt_pk_bf16_f32 v107, v110, v111\n" \
  "v_mfma_f32_16x16x32_bf16 v[120:123], v[48:51], a[200:203], v[120:123]\n" \
  "buffer_store_dwordx4 v[104:107], v24, s[60:63], s98 offen offset:0 sc1\n" \
  "v_mfma_f32_16x16x32_bf16 v[120:123], v[52:55], a[216:219], v[120:123]\n" \
  "ds_read_b64_tr_b16 v[44:45], v4 offset:45568\n" \
  "s_add_u32 m0, s72, 49152\n" \
  "v_mfma_f32_16x16x32_bf16 v[124:127], v[52:55], a[220:223], v[124:127]\n" \
  "ds_read_b64_tr_b16 v[46:47], v4 offset:47616\n" \
  "v_mfma_f32_16x16x32_bf16 v[124:127], v[48:51], a[204:207], v[124:127]\n" \
  "s_waitcnt lgkmcnt(4)\n" \
  "v_mfma_f32_16x16x32_bf16 v[112:115], v[56:59], a[224:227], v[112:115]\n" \
  "buffer_load_dwordx4 v30, s[48:51], s21 offen  lds\n" \
  "v_mfma_f32_16x16x32_bf16 v[112:115], v[60:63], a[240:243], v[112:115]\n" \
  "ds_read_b64_tr_b16 v[48:49], v4 offset:49664\n" \
  "v_mfma_f32_16x16x32_bf16 v[116:119], v[60:63], a[244:247], v[116:119]\n" \
  "v_mfma_f32_16x16x32_bf16 v[116:119], v[56:59], a[228:231], v[116:119]\n" \
  "ds_read_b64_tr_b16 v[50:51], v4 offset:51712\n" \
  "v_mfma_f32_16x16x32_bf16 v[120:123], v[56:59], a[232:235], v[120:123]\n" \
  "v_mfma_f32_16x16x32_bf16 v[120:123], v[60:63], a[248:251], v[120:123]\n" \
  "ds_read_b64_tr_b16 v[52:53], v4 offset:53760\n" \
  "v_mfma_f32_16x16x32_bf16 v[124:127], v[60:63], a[252:255], v[124:127]\n" \
  "ds_read_b64_tr_b16 v[54:55], v4 offset:55808\n" \
  "v_mfma_f32_16x16x32_bf16 v[124:127], v[56:59], a[236:239], v[124:127]\n" \
  "s_waitcnt lgkmcnt(4)\n" \
  "v_mfma_f32_16x16x32_bf16 v[128:131], v[40:43], a[160:163], v[128:131]\n" \
  "s_add_u32 s96, s83, 6144\n" \
  "v_cvt_pk_bf16_f32 v112, v112, v113\n" \
  "v_mfma_f32_16x16x32_bf16 v[128:131], v[44:47], a[176:179], v[128:131]\n" \
  "ds_read_b64_tr_b16 v[56:57], v6 offset:512\n" \
  "v_cvt_pk_bf16_f32 v113, v114, v115\n" \
  "v_mfma_f32_16x16x32_bf16 v[132:135], v[44:47], a[180:183], v[132:135]\n" \
  "v_cvt_pk_bf16_f32 v114, v116, v117\n" \
  "v_cvt_pk_bf16_f32 v115, v118, v119\n" \
  "v_mfma_f32_16x16x32_bf16 v[132:135], v[40:43], a[164:167], v[132:135]\n" \
  "ds_read_b64_tr_b16 v[58:59], v6 offset:2560\n" \
  "v_mfma_f32_16x16x32_bf16 v[136:139], v[40:43], a[168:171], v[136:139]\n" \
  "buffer_store_dwordx4 v[112:115], v24, s[56:59], s96 offen offset:0 sc1\n" \
  "v_mfma_f32_16x16x32_bf16 v[136:139], v[44:47], a[184:187], v[136:139]\n" \
  "ds_read_b64_tr_b16 v[60:61], v6 offset:4608\n" \
  "v_mfma_f32_16x16x32_bf16 v[140:143], v[44:47], a[188:191], v[140:143]\n" \
  "ds_read_b64_tr_b16 v[62:63], v6 offset:6656\n" \
  "v_mfma_f32_16x16x32_bf16 v[140:143], v[40:43], a[172:175], v[140:143]\n" \
  "s_waitcnt lgkmcnt(4)\n" \
  "v_mfma_f32_16x16x32_bf16 v[128:131], v[48:51], a[192:195], v[128:131]\n" \
  "v_cvt_pk_bf16_f32 v120, v120, v121\n" \
  "v_cvt_pk_bf16_f32 v121, v122, v123\n" \
  "v_mfma_f32_16x16x32_bf16 v[128:131], v[52:55], a[208:211], v[128:131]\n" \
  "ds_read_b64_tr_b16 v[40:41], v5 offset:40960\n" \
  "v_cvt_pk_bf16_f32 v122, v124, v125\n" \
  "v_mfma_f32_16x16x32_bf16 v[132:135], v[52:55], a[212:215], v[132:135]\n" \
  "v_cvt_pk_bf16_f32 v123, v126, v127\n" \
  "v_mfma_f32_16x16x32_bf16 v[132:135], v[48:51], a[196:199], v[132:135]\n" \
  "ds_read_b64_tr_b16 v[42:43], v5 offset:43008\n" \
  "v_mfma_f32_16x16x32_bf16 v[136:139], v[48:51], a[200:203], v[136:139]\n" \
  "buffer_store_dwordx4 v[120:123], v24, s[56:59], s96 offen offset:1024 sc1\n" \
  "v_mfma_f32_16x16x32_bf16 v[136:139], v[52:55], a[216:219], v[136:139]\n" \
  "ds_read_b64_tr_b16 v[44:45], v5 offset:45056\n" \
  "v_mfma_f32_16x16x32_bf16 v[140:143], v[52:55], a[220:223], v[140:143]\n" \
  "ds_read_b64_tr_b16 v[46:47], v5 offset:47104\n" \
  "v_mfma_f32_16x16x32_bf16 v[140:143], v[48:51], a[204:207], v[140:143]\n" \
  "s_waitcnt lgkmcnt(4)\n" \
  "v_mfma_f32_16x16x32_bf16 v[128:131], v[56:59], a[224:227], v[128:131]\n" \
  "v_mfma_f32_16x16x32_bf16 v[128:131], v[60:63], a[240:243], v[128:131]\n" \
  "ds_read_b64_tr_b16 v[48:49], v5 offset:49152\n" \
  "v_mfma_f32_16x16x32_bf16 v[132:135], v[60:63], a[244:247], v[132:135]\n" \
  "v_mfma_f32_16x16x32_bf16 v[132:135], v[56:59], a[228:231], v[132:135]\n" \
  "ds_read_b64_tr_b16 v[50:51], v5 offset:51200\n" \
  "v_mfma_f32_16x16x32_bf16 v[136:139], v[56:59], a[232:235], v[136:139]\n" \
  "v_mfma_f32_16x16x32_bf16 v[136:139], v[60:63], a[248:251], v[136:139]\n" \
  "ds_read_b64_tr_b16 v[52:53], v5 offset:53248\n" \
  "v_mfma_f32_16x16x32_bf16 v[140:143], v[60:63], a[252:255], v[140:143]\n" \
  "ds_read_b64_tr_b16 v[54:55], v5 offset:55296\n" \
  "v_mfma_f32_16x16x32_bf16 v[140:143], v[56:59], a[236:239], v[140:143]\n" \
  "s_waitcnt lgkmcnt(4)\n" \
  "v_mfma_f32_16x16x32_bf16 v[144:147], v[40:43], a[160:163], v[144:147]\n" \
  "s_add_u32 s96, s83, 4096\n" \
  "v_cvt_pk_bf16_f32 v128, v128, v129\n" \
  "v_mfma_f32_16x16x32_bf16 v[144:147], v[44:47], a[176:179], v[144:147]\n" \
  "ds_read_b64_tr_b16 v[56:57], v7 offset:0\n" \
  "v_cvt_pk_bf16_f32 v129, v130, v131\n" \
  "v_mfma_f32_16x16x32_bf16 v[148:151], v[44:47], a[180:183], v[148:151]\n" \
  "v_cvt_pk_bf16_f32 v130, v132, v133\n" \
  "v_cvt_pk_bf16_f32 v131, v134, v135\n" \
  "v_mfma_f32_16x16x32_bf16 v[148:151], v[40:43], a[164:167], v[148:151]\n" \
  "ds_read_b64_tr_b16 v[58:59], v7 offset:2048\n" \
  "v_mfma_f32_16x16x32_bf16 v[152:155], v[40:43], a[168:171], v[152:155]\n" \
  "buffer_store_dwordx4 v[128:131], v24, s[56:59], s96 offen offset:0 sc1\n" \
  "v_mfma_f32_16x16x32_bf16 v[152:155], v[44:47], a[184:187], v[152:155]\n" \
  "ds_read_b64_tr_b16 v[60:61], v7 offset:4096\n" \
  "v_mfma_f32_16x16x32_bf16 v[156:159], v[44:47], a[188:191], v[156:159]\n" \
  "ds_read_b64_tr_b16 v[62:63], v7 offset:6144\n" \
  "v_mfma_f32_16x16x32_bf16 v[156:159], v[40:43], a[172:175], v[156:159]\n" \
  "s_waitcnt lgkmcnt(4)\n" \
  "v_mfma_f32_16x16x32_bf16 v[144:147], v[48:51], a[192:195], v[144:147]\n" \
  "v_cvt_pk_bf16_f32 v136, v136, v137\n" \
  "v_cvt_pk_bf16_f32 v137, v138, v139\n" \
  "v_mfma_f32_16x16x32_bf16 v[144:147], v[52:55], a[208:211], v[144:147]\n" \
  "ds_read_b64_tr_b16 v[40:41], v4 offset:40960\n" \
  "v_cvt_pk_bf16_f32 v138, v140, v141\n" \
  "v_mfma_f32_16x16x32_bf16 v[148:151], v[52:55], a[212:215], v[148:151]\n" \
  "v_cvt_pk_bf16_f32 v139, v142, v143\n" \
  "v_mfma_f32_16x16x32_bf16 v[148:151], v[48:51], a[196:199], v[148:151]\n" \
  "ds_read_b64_tr_b16 v[42:43], v4 offset:43008\n" \
  "v_mfma_f32_16x16x32_bf16 v[152:155], v[48:51], a[200:203], v[152:155]\n" \
  "buffer_store_dwordx4 v[136:139], v24, s[56:59], s96 offen offset:1024 sc1\n" \
  "v_mfma_f32_16x16x32_bf16 v[152:155], v[52:55], a[216:219], v[152:155]\n" \
  "ds_read_b64_tr_b16 v[44:45], v4 offset:45056\n" \
  "v_mfma_f32_16x16x32_bf16 v[156:159], v[52:55], a[220:223], v[156:159]\n" \
  "ds_read_b64_tr_b16 v[46:47], v4 offset:47104\n" \
  "v_mfma_f32_16x16x32_bf16 v[156:159], v[48:51], a[204:207], v[156:159]\n" \
  "s_waitcnt vmcnt(4)\n" \
  "s_barrier\n" \
  "s_waitcnt lgkmcnt(4)\n" \
  "v_mfma_f32_16x16x32_bf16 v[144:147], v[56:59], a[224:227], v[144:147]\n" \
  "v_mfma_f32_16x16x32_bf16 v[144:147], v[60:63], a[240:243], v[144:147]\n" \
  "ds_read_b64_tr_b16 v[48:49], v4 offset:49152\n" \
  "v_mfma_f32_16x16x32_bf16 v[148:151], v[60:63], a[244:247], v[148:151]\n" \
  "v_mfma_f32_16x16x32_bf16 v[148:151], v[56:59], a[228:231], v[148:151]\n" \
  "ds_read_b64_tr_b16 v[50:51], v4 offset:51200\n" \
  "v_mfma_f32_16x16x32_bf16 v[152:155], v[56:59], a[232:235], v[152:155]\n" \
  "v_mfma_f32_16x16x32_bf16 v[152:155], v[60:63], a[248:251], v[152:155]\n" \
  "ds_read_b64_tr_b16 v[52:53], v4 offset:53248\n" \
  "v_mfma_f32_16x16x32_bf16 v[156:159], v[60:63], a[252:255], v[156:159]\n" \
  "ds_read_b64_tr_b16 v[54:55], v4 offset:55296\n" \
  "v_mfma_f32_16x16x32_bf16 v[156:159], v[56:59], a[236:239], v[156:159]\n" \
  "s_waitcnt lgkmcnt(4)\n" \
  "v_mfma_f32_16x16x32_bf16 v[160:163], v[40:43], a[160:163], v[160:163]\n" \
  "s_add_u32 s96, s83, 2048\n" \
  "v_cvt_pk_bf16_f32 v144, v144, v145\n" \
  "v_mfma_f32_16x16x32_bf16 v[160:163], v[44:47], a[176:179], v[160:163]\n" \
  "ds_read_b64_tr_b16 v[56:57], v6 offset:0\n" \
  "v_cvt_pk_bf16_f32 v145, v146, v147\n" \
  "v_mfma_f32_16x16x32_bf16 v[164:167], v[44:47], a[180:183], v[164:167]\n" \
  "v_cvt_pk_bf16_f32 v146, v148, v149\n" \
  "v_cvt_pk_bf16_f32 v147, v150, v151\n" \
  "v_mfma_f32_16x16x32_bf16 v[164:167], v[40:43], a[164:167], v[164:167]\n" \
  "ds_read_b64_tr_b16 v[58:59], v6 offset:2048\n" \
  "v_mfma_f32_16x16x32_bf16 v[168:171], v[40:43], a[168:171], v[168:171]\n" \
  "buffer_store_dwordx4 v[144:147], v24, s[56:59], s96 offen offset:0 sc1\n" \
  "v_mfma_f32_16x16x32_bf16 v[168:171], v[44:47], a[184:187], v[168:171]\n" \
  "ds_read_b64_tr_b16 v[60:61], v6 offset:4096\n" \
  "v_mfma_f32_16x16x32_bf16 v[172:175], v[44:47], a[188:191], v[172:175]\n" \
  "ds_read_b64_tr_b16 v[62:63], v6 offset:6144\n" \
  "v_mfma_f32_16x16x32_bf16 v[172:175], v[40:43], a[172:175], v[172:175]\n" \
  "s_waitcnt lgkmcnt(4)\n" \
  "v_mfma_f32_16x16x32_bf16 v[160:163], v[48:51], a[192:195], v[160:163]\n" \
  "v_cvt_pk_bf16_f32 v152, v152, v153\n" \
  "v_cvt_pk_bf16_f32 v153, v154, v155\n" \
  "v_mfma_f32_16x16x32_bf16 v[160:163], v[52:55], a[208:211], v[160:163]\n" \
  "ds_read_b64_tr_b16 v[40:41], v7 offset:20992\n" \
  "v_cvt_pk_bf16_f32 v154, v156, v157\n" \
  "v_mfma_f32_16x16x32_bf16 v[164:167], v[52:55], a[212:215], v[164:167]\n" \
  "v_cvt_pk_bf16_f32 v155, v158, v159\n" \
  "v_mfma_f32_16x16x32_bf16 v[164:167], v[48:51], a[196:199], v[164:167]\n" \
  "ds_read_b64_tr_b16 v[42:43], v7 offset:23040\n" \
  "v_mfma_f32_16x16x32_bf16 v[168:171], v[48:51], a[200:203], v[168:171]\n" \
  "buffer_store_dwordx4 v[152:155], v24, s[56:59], s96 offen offset:1024 sc1\n" \
  "v_mfma_f32_16x16x32_bf16 v[168:171], v[52:55], a[216:219], v[168:171]\n" \
  "ds_read_b64_tr_b16 v[44:45], v7 offset:25088\n" \
  "v_mfma_f32_16x16x32_bf16 v[172:175], v[52:55], a[220:223], v[172:175]\n" \
  "ds_read_b64_tr_b16 v[46:47], v7 offset:27136\n" \
  "v_mfma_f32_16x16x32_bf16 v[172:175], v[48:51], a[204:207], v[172:175]\n" \
  "s_waitcnt lgkmcnt(4)\n" \
  "v_mfma_f32_16x16x32_bf16 v[160:163], v[56:59], a[224:227], v[160:163]\n" \
  "v_mfma_f32_16x16x32_bf16 v[160:163], v[60:63], a[240:243], v[160:163]\n" \
  "ds_read_b64_tr_b16 v[48:49], v7 offset:29184\n" \
  "v_mfma_f32_16x16x32_bf16 v[164:167], v[60:63], a[244:247], v[164:167]\n" \
  "v_mfma_f32_16x16x32_bf16 v[164:167], v[56:59], a[228:231], v[164:167]\n" \
  "ds_read_b64_tr_b16 v[50:51], v7 offset:31232\n" \
  "v_mfma_f32_16x16x32_bf16 v[168:171], v[56:59], a[232:235], v[168:171]\n" \
  "v_mfma_f32_16x16x32_bf16 v[168:171], v[60:63], a[248:251], v[168:171]\n" \
  "ds_read_b64_tr_b16 v[52:53], v7 offset:33280\n" \
  "v_mfma_f32_16x16x32_bf16 v[172:175], v[60:63], a[252:255], v[172:175]\n" \
  "ds_read_b64_tr_b16 v[54:55], v7 offset:35328\n" \
  "v_mfma_f32_16x16x32_bf16 v[172:175], v[56:59], a[236:239], v[172:175]\n" \
  "s_waitcnt lgkmcnt(4)\n" \
  "v_mfma_f32_16x16x32_bf16 v[176:179], v[40:43], a[0:3], 0\n" \
  "s_add_u32 s96, s83, 0\n" \
  "v_cvt_pk_bf16_f32 v160, v160, v161\n" \
  "v_mfma_f32_16x16x32_bf16 v[176:179], v[44:47], a[16:19], v[176:179]\n" \
  "ds_read_b64_tr_b16 v[56:57], v7 offset:37376\n" \
  "v_cvt_pk_bf16_f32 v161, v162, v163\n" \
  "v_mfma_f32_16x16x32_bf16 v[180:183], v[44:47], a[20:23], 0\n" \
  "v_cvt_pk_bf16_f32 v162, v164, v165\n" \
  "v_cvt_pk_bf16_f32 v163, v166, v167\n" \
  "v_mfma_f32_16x16x32_bf16 v[180:183], v[40:43], a[4:7], v[180:183]\n" \
  "ds_read_b64_tr_b16 v[58:59], v7 offset:39424\n" \
  "s_waitcnt vmcnt(4)\n" \
  "v_mfma_f32_16x16x32_bf16 v[184:187], v[40:43], a[8:11], 0\n" \
  "buffer_store_dwordx4 v[160:163], v24, s[56:59], s96 offen offset:0 sc1\n" \
  "v_mfma_f32_16x16x32_bf16 v[184:187], v[44:47], a[24:27], v[184:187]\n" \
  "ds_read_b64_tr_b16 v[60:61], v3 offset:512\n" \
  "v_mfma_f32_16x16x32_bf16 v[188:191], v[44:47], a[28:31], 0\n" \
  "ds_read_b64_tr_b16 v[62:63], v3 offset:2560\n" \
  "v_mfma_f32_16x16x32_bf16 v[188:191], v[40:43], a[12:15], v[188:191]\n" \
  "s_waitcnt lgkmcnt(4)\n" \
  "v_mfma_f32_16x16x32_bf16 v[176:179], v[48:51], a[32:35], v[176:179]\n" \
  "buffer_store_dword v28, v1, s[60:63], s52 offen offset:0 sc1\n" \
  "v_mfma_f32_16x16x32_bf16 v[176:179], v[52:55], a[48:51], v[176:179]\n" \
  "ds_read_b64_tr_b16 v[40:41], v3 offset:4608\n" \
  "v_cvt_pk_bf16_f32 v168, v168, v169\n" \
  "v_mfma_f32_16x16x32_bf16 v[180:183], v[52:55], a[52:55], v[180:183]\n" \
  "v_cvt_pk_bf16_f32 v169, v170, v171\n" \
  "v_cvt_pk_bf16_f32 v170, v172, v173\n" \
  "v_mfma_f32_16x16x32_bf16 v[180:183], v[48:51], a[36:39], v[180:183]\n" \
  "ds_read_b64_tr_b16 v[42:43], v3 offset:6656\n" \
  "v_cvt_pk_bf16_f32 v171, v174, v175\n" \
  "v_mfma_f32_16x16x32_bf16 v[184:187], v[48:51], a[40:43], v[184:187]\n" \
  "buffer_store_dwordx4 v[168:171], v24, s[56:59], s96 offen offset:1024 sc1\n" \
  "v_mfma_f32_16x16x32_bf16 v[184:187], v[52:55], a[56:59], v[184:187]\n" \
  "ds_read_b64_tr_b16 v[44:45], v3 offset:8704\n" \
  "v_mfma_f32_16x16x32_bf16 v[188:191], v[52:55], a[60:63], v[188:191]\n" \
  "ds_read_b64_tr_b16 v[46:47], v3 offset:10752\n" \
  "v_mfma_f32_16x16x32_bf16 v[188:191], v[48:51], a[44:47], v[188:191]\n" \
  "s_waitcnt lgkmcnt(4)\n" \
  "v_mfma_f32_16x16x32_bf16 v[176:179], v[56:59], a[64:67], v[176:179]\n" \
  "v_mfma_f32_16x16x32_bf16 v[176:179], v[60:63], a[80:83], v[176:179]\n" \
  "ds_read_b64_tr_b16 v[48:49], v3 offset:12800\n" \
  "v_mfma_f32_16x16x32_bf16 v[180:183], v[60:63], a[84:87], v[180:183]\n" \
  "v_mfma_f32_16x16x32_bf16 v[180:183], v[56:59], a[68:71], v[180:183]\n" \
  "ds_read_b64_tr_b16 v[50:51], v3 offset:14848\n" \
  "v_mfma_f32_16x16x32_bf16 v[184:187], v[56:59], a[72:75], v[184:187]\n" \
  "v_mfma_f32_16x16x32_bf16 v[184:187], v[60:63], a[88:91], v[184:187]\n" \
  "ds_read_b64_tr_b16 v[52:53], v3 offset:16896\n" \
  "v_mfma_f32_16x16x32_bf16 v[188:191], v[60:63], a[92:95], v[188:191]\n" \
  "ds_read_b64_tr_b16 v[54:55], v3 offset:18944\n" \
  "v_mfma_f32_16x16x32_bf16 v[188:191], v[56:59], a[76:79], v[188:191]\n" \
  "s_waitcnt lgkmcnt(4)\n" \
  "v_mfma_f32_16x16x32_bf16 v[176:179], v[40:43], a[96:99], v[176:179]\n" \
  "v_lshlrev_b32 v168, 16, v250\n" \
  "v_mfma_f32_16x16x32_bf16 v[176:179], v[44:47], a[112:115], v[176:179]\n" \
  "ds_read_b64_tr_b16 v[56:57], v3 offset:20992\n" \
  "v_and_b32 v169, 0xffff0000, v250\n" \
  "v_mfma_f32_16x16x32_bf16 v[180:183], v[44:47], a[116:119], v[180:183]\n" \
  "v_lshlrev_b32 v170, 16, v251\n" \
  "v_and_b32 v171, 0xffff0000, v251\n" \
  "v_mfma_f32_16x16x32_bf16 v[180:183], v[40:43], a[100:103], v[180:183]\n" \
  "ds_read_b64_tr_b16 v[58:59], v3 offset:23040\n" \
  "v_lshlrev_b32 v172, 16, v252\n" \
  "v_mfma_f32_16x16x32_bf16 v[184:187], v[40:43], a[104:107], v[184:187]\n" \
  "v_and_b32 v173, 0xffff0000, v252\n" \
  "v_lshlrev_b32 v174, 16, v253\n" \
  "v_mfma_f32_16x16x32_bf16 v[184:187], v[44:47], a[120:123], v[184:187]\n" \
  "ds_read_b64_tr_b16 v[60:61], v3 offset:25088\n" \
  "v_and_b32 v175, 0xffff0000, v253\n" \
  "v_mfma_f32_16x16x32_bf16 v[188:191], v[44:47], a[124:127], v[188:191]\n" \
  "ds_read_b64_tr_b16 v[62:63], v3 offset:27136\n" \
  "v_lshlrev_b32 v120, 16, v240\n" \
  "v_mfma_f32_16x16x32_bf16 v[188:191], v[40:43], a[108:111], v[188:191]\n" \
  "v_and_b32 v121, 0xffff0000, v240\n" \
  "s_waitcnt lgkmcnt(4)\n" \
  "v_mfma_f32_16x16x32_bf16 v[176:179], v[48:51], a[128:131], v[176:179]\n" \
  "v_lshlrev_b32 v122, 16, v241\n" \
  "v_and_b32 v123, 0xffff0000, v241\n" \
  "v_mfma_f32_16x16x32_bf16 v[176:179], v[52:55], a[144:147], v[176:179]\n" \
  "ds_read_b64_tr_b16 v[40:41], v3 offset:29184\n" \
  "v_lshlrev_b32 v124, 16, v242\n" \
  "v_mfma_f32_16x16x32_bf16 v[180:183], v[52:55], a[148:151], v[180:183]\n" \
  "v_and_b32 v125, 0xffff0000, v242\n" \
  "v_lshlrev_b32 v126, 16, v243\n" \
  "v_mfma_f32_16x16x32_bf16 v[180:183], v[48:51], a[132:135], v[180:183]\n" \
  "ds_read_b64_tr_b16 v[42:43], v3 offset:31232\n" \
  "v_and_b32 v127, 0xffff0000, v243\n" \
  "v_mfma_f32_16x16x32_bf16 v[184:187], v[48:51], a[136:139], v[184:187]\n" \
  "v_lshlrev_b32 v136, 16, v244\n" \
  "v_and_b32 v137, 0xffff0000, v244\n" \
  "v_mfma_f32_16x16x32_bf16 v[184:187], v[52:55], a[152:155], v[184:187]\n" \
  "ds_read_b64_tr_b16 v[44:45], v3 offset:33280\n" \
  "v_lshlrev_b32 v138, 16, v245\n" \
  "v_mfma_f32_16x16x32_bf16 v[188:191], v[52:55], a[156:159], v[188:191]\n" \
  "ds_read_b64_tr_b16 v[46:47], v3 offset:35328\n" \
  "v_and_b32 v139, 0xffff0000, v245\n" \
  "v_mfma_f32_16x16x32_bf16 v[188:191], v[48:51], a[140:143], v[188:191]\n" \
  "v_lshlrev_b32 v140, 16, v246\n" \
  "s_waitcnt lgkmcnt(4)\n" \
  "v_mfma_f32_16x16x32_bf16 v[176:179], v[56:59], v[64:67], v[176:179]\n" \
  "buffer_load_dword v27, v239, s[60:63], s87 offen offset:0 sc1\n" \
  "v_mfma_f32_16x16x32_bf16 v[176:179], v[60:63], v[80:83], v[176:179]\n" \
  "ds_read_b64_tr_b16 v[48:49], v3 offset:37376\n" \
  "v_and_b32 v141, 0xffff0000, v246\n" \
  "v_mfma_f32_16x16x32_bf16 v[180:183], v[60:63], v[84:87], v[180:183]\n" \
  "v_lshlrev_b32 v142, 16, v247\n" \
  "v_and_b32 v143, 0xffff0000, v247\n" \
  "v_mfma_f32_16x16x32_bf16 v[180:183], v[56:59], v[68:71], v[180:183]\n" \
  "ds_read_b64_tr_b16 v[50:51], v3 offset:39424\n" \
  "v_lshlrev_b32 v152, 16, v248\n" \
  "v_mfma_f32_16x16x32_bf16 v[184:187], v[56:59], v[72:75], v[184:187]\n" \
  "v_and_b32 v153, 0xffff0000, v248\n" \
  "v_lshlrev_b32 v154, 16, v249\n" \
  "v_mfma_f32_16x16x32_bf16 v[184:187], v[60:63], v[88:91], v[184:187]\n" \
  "ds_read_b64_tr_b16 v[52:53], v3 offset:41472\n" \
  "v_and_b32 v155, 0xffff0000, v249\n" \
  "v_mfma_f32_16x16x32_bf16 v[188:191], v[60:63], v[92:95], v[188:191]\n" \
  "ds_read_b64_tr_b16 v[54:55], v3 offset:43520\n" \
  "v_mfma_f32_16x16x32_bf16 v[188:191], v[56:59], v[76:79], v[188:191]\n" \
  "s_waitcnt lgkmcnt(4)\n" \
  "v_mfma_f32_16x16x32_bf16 v[176:179], v[40:43], a[160:163], v[176:179]\n" \
  "v_mfma_f32_16x16x32_bf16 v[176:179], v[44:47], a[176:179], v[176:179]\n" \
  "ds_read_b64_tr_b16 v[56:57], v3 offset:45568\n" \
  "v_mfma_f32_16x16x32_bf16 v[180:183], v[44:47], a[180:183], v[180:183]\n" \
  "v_mfma_f32_16x16x32_bf16 v[180:183], v[40:43], a[164:167], v[180:183]\n" \
  "ds_read_b64_tr_b16 v[58:59], v3 offset:47616\n" \
  "v_mfma_f32_16x16x32_bf16 v[184:187], v[40:43], a[168:171], v[184:187]\n" \
  "v_mfma_f32_16x16x32_bf16 v[184:187], v[44:47], a[184:187], v[184:187]\n" \
  "ds_read_b64_tr_b16 v[60:61], v3 offset:49664\n" \
  "v_mfma_f32_16x16x32_bf16 v[188:191], v[44:47], a[188:191], v[188:191]\n" \
  "ds_read_b64_tr_b16 v[62:63], v3 offset:51712\n" \
  "v_mfma_f32_16x16x32_bf16 v[188:191], v[40:43], a[172:175], v[188:191]\n" \
  "s_waitcnt lgkmcnt(4)\n" \
  "v_mfma_f32_16x16x32_bf16 v[176:179], v[48:51], a[192:195], v[176:179]\n" \
  "v_mfma_f32_16x16x32_bf16 v[176:179], v[52:55], a[208:211], v[176:179]\n" \
  "ds_read_b64_tr_b16 v[40:41], v6 offset:20992\n" \
  "v_mfma_f32_16x16x32_bf16 v[180:183], v[52:55], a[212:215], v[180:183]\n" \
  "v_mfma_f32_16x16x32_bf16 v[180:183], v[48:51], a[196:199], v[180:183]\n" \
  "ds_read_b64_tr_b16 v[42:43], v6 offset:23040\n" \
  "v_mfma_f32_16x16x32_bf16 v[184:187], v[48:51], a[200:203], v[184:187]\n" \
  "v_mfma_f32_16x16x32_bf16 v[184:187], v[52:55], a[216:219], v[184:187]\n" \
  "ds_read_b64_tr_b16 v[44:45], v6 offset:25088\n" \
  "v_mfma_f32_16x16x32_bf16 v[188:191], v[52:55], a[220:223], v[188:191]\n" \
  "ds_read_b64_tr_b16 v[46:47], v6 offset:27136\n" \
  "v_mfma_f32_16x16x32_bf16 v[188:191], v[48:51], a[204:207], v[188:191]\n" \
  "s_waitcnt lgkmcnt(4)\n" \
  "v_mfma_f32_16x16x32_bf16 v[176:179], v[56:59], a[224:227], v[176:179]\n" \
  "v_mfma_f32_16x16x32_bf16 v[176:179], v[60:63], a[240:243], v[176:179]\n" \
  "ds_read_b64_tr_b16 v[48:49], v6 offset:29184\n" \
  "v_mfma_f32_16x16x32_bf16 v[180:183], v[60:63], a[244:247], v[180:183]\n" \
  "v_mfma_f32_16x16x32_bf16 v[180:183], v[56:59], a[228:231], v[180:183]\n" \
  "ds_read_b64_tr_b16 v[50:51], v6 offset:31232\n" \
  "v_mfma_f32_16x16x32_bf16 v[184:187], v[56:59], a[232:235], v[184:187]\n" \
  "v_mfma_f32_16x16x32_bf16 v[184:187], v[60:63], a[248:251], v[184:187]\n" \
  "ds_read_b64_tr_b16 v[52:53], v6 offset:33280\n" \
  "v_mfma_f32_16x16x32_bf16 v[188:191], v[60:63], a[252:255], v[188:191]\n" \
  "ds_read_b64_tr_b16 v[54:55], v6 offset:35328\n" \
  "v_mfma_f32_16x16x32_bf16 v[188:191], v[56:59], a[236:239], v[188:191]\n" \
  "s_waitcnt lgkmcnt(4)\n" \
  "v_mfma_f32_16x16x32_bf16 v[192:195], v[40:43], a[0:3], 0\n" \
  "s_xor_b32 s96, s95, 49152\n" \
  "v_cvt_pk_bf16_f32 v176, v176, v177\n" \
  "v_mfma_f32_16x16x32_bf16 v[192:195], v[44:47], a[16:19], v[192:195]\n" \
  "ds_read_b64_tr_b16 v[56:57], v6 offset:37376\n" \
  "v_cvt_pk_bf16_f32 v177, v178, v179\n" \
  "v_mfma_f32_16x16x32_bf16 v[196:199], v[44:47], a[20:23], 0\n" \
  "v_cvt_pk_bf16_f32 v178, v180, v181\n" \
  "v_cvt_pk_bf16_f32 v179, v182, v183\n" \
  "v_mfma_f32_16x16x32_bf16 v[196:199], v[40:43], a[4:7], v[196:199]\n" \
  "ds_read_b64_tr_b16 v[58:59], v6 offset:39424\n" \
  "v_mfma_f32_16x16x32_bf16 v[200:203], v[40:43], a[8:11], 0\n" \
  "buffer_store_dwordx4 v[176:179], v24, s[64:67], s96 offen offset:0 sc1\n" \
  "v_mfma_f32_16x16x32_bf16 v[200:203], v[44:47], a[24:27], v[200:203]\n" \
  "ds_read_b64_tr_b16 v[60:61], v2 offset:512\n" \
  "v_mfma_f32_16x16x32_bf16 v[204:207], v[44:47], a[28:31], 0\n" \
  "ds_read_b64_tr_b16 v[62:63], v2 offset:2560\n" \
  "v_mfma_f32_16x16x32_bf16 v[204:207], v[40:43], a[12:15], v[204:207]\n" \
  "s_waitcnt lgkmcnt(4)\n" \
  "v_mfma_f32_16x16x32_bf16 v[192:195], v[48:51], a[32:35], v[192:195]\n" \
  "v_cvt_pk_bf16_f32 v184, v184, v185\n" \
  "v_cvt_pk_bf16_f32 v185, v186, v187\n" \
  "v_mfma_f32_16x16x32_bf16 v[192:195], v[52:55], a[48:51], v[192:195]\n" \
  "ds_read_b64_tr_b16 v[40:41], v2 offset:4608\n" \
  "v_cvt_pk_bf16_f32 v186, v188, v189\n" \
  "v_mfma_f32_16x16x32_bf16 v[196:199], v[52:55], a[52:55], v[196:199]\n" \
  "v_cvt_pk_bf16_f32 v187, v190, v191\n" \
  "s_waitcnt vmcnt(2)\n" \
  "v_mfma_f32_16x16x32_bf16 v[196:199], v[48:51], a[36:39], v[196:199]\n" \
  "ds_read_b64_tr_b16 v[42:43], v2 offset:6656\n" \
  "v_mfma_f32_16x16x32_bf16 v[200:203], v[48:51], a[40:43], v[200:203]\n" \
  "buffer_store_dwordx4 v[184:187], v24, s[64:67], s96 offen offset:1024 sc1\n" \
  "v_mfma_f32_16x16x32_bf16 v[200:203], v[52:55], a[56:59], v[200:203]\n" \
  "ds_read_b64_tr_b16 v[44:45], v2 offset:8704\n" \
  "v_mfma_f32_16x16x32_bf16 v[204:207], v[52:55], a[60:63], v[204:207]\n" \
  "ds_read_b64_tr_b16 v[46:47], v2 offset:10752\n" \
  "v_mfma_f32_16x16x32_bf16 v[204:207], v[48:51], a[44:47], v[204:207]\n" \
  "s_waitcnt lgkmcnt(4)\n" \
  "v_mfma_f32_16x16x32_bf16 v[192:195], v[56:59], a[64:67], v[192:195]\n" \
  "buffer_store_dword v28, v1, s[60:63], s52 offen offset:128 sc1\n" \
  "v_mfma_f32_16x16x32_bf16 v[192:195], v[60:63], a[80:83], v[192:195]\n" \
  "ds_read_b64_tr_b16 v[48:49], v2 offset:12800\n" \
  "s_waitcnt vmcnt(3)\n" \
  "v_mfma_f32_16x16x32_bf16 v[196:199], v[60:63], a[84:87], v[196:199]\n" \
  "v_cmp_ne_u32 vcc, s84, v27\n" \
  "s_and_b32 vcc_lo, vcc_lo, 0xffff\n" \
  "s_cbranch_scc1 SLOW_P0_%=\n" \
  "BACK_P0_%=:\n" \
  "v_mfma_f32_16x16x32_bf16 v[196:199], v[56:59], a[68:71], v[196:199]\n" \
  "ds_read_b64_tr_b16 v[50:51], v2 offset:14848\n" \
  "v_mfma_f32_16x16x32_bf16 v[200:203], v[56:59], a[72:75], v[200:203]\n" \
  "buffer_load_dwordx4 v[16:19], v238, s[60:63], s101 offen sc1\n" \
  "v_mfma_f32_16x16x32_bf16 v[200:203], v[60:63], a[88:91], v[200:203]\n" \
  "ds_read_b64_tr_b16 v[52:53], v2 offset:16896\n" \
  "v_mfma_f32_16x16x32_bf16 v[204:207], v[60:63], a[92:95], v[204:207]\n" \
  "ds_read_b64_tr_b16 v[54:55], v2 offset:18944\n" \
  "v_mfma_f32_16x16x32_bf16 v[204:207], v[56:59], a[76:79], v[204:207]\n" \
  "s_waitcnt lgkmcnt(4)\n" \
  "v_mfma_f32_16x16x32_bf16 v[192:195], v[40:43], a[96:99], v[192:195]\n" \
  "buffer_load_dwordx4 v[20:23], v238, s[60:63], s31 offen sc1\n" \
  "v_mfma_f32_16x16x32_bf16 v[192:195], v[44:47], a[112:115], v[192:195]\n" \
  "ds_read_b64_tr_b16 v[56:57], v2 offset:20992\n" \
  "s_xor_b32 s27, s78, 7\n" \
  "v_mfma_f32_16x16x32_bf16 v[196:199], v[44:47], a[116:119], v[196:199]\n" \
  "s_mul_i32 s27, s27, 73728\n" \
  "s_add_u32 s27, s27, s53\n" \
  "v_mfma_f32_16x16x32_bf16 v[196:199], v[40:43], a[100:103], v[196:199]\n" \
  "ds_read_b64_tr_b16 v[58:59], v2 offset:23040\n" \
  "v_mfma_f32_16x16x32_bf16 v[200:203], v[40:43], a[104:107], v[200:203]\n" \
  "buffer_load_dwordx4 v[112:115], v25, s[60:63], s27 offen offset:2048 sc1\n" \
  "v_mfma_f32_16x16x32_bf16 v[200:203], v[44:47], a[120:123], v[200:203]\n" \
  "ds_read_b64_tr_b16 v[60:61], v2 offset:25088\n" \
  "v_mfma_f32_16x16x32_bf16 v[204:207], v[44:47], a[124:127], v[204:207]\n" \
  "ds_read_b64_tr_b16 v[62:63], v2 offset:27136\n" \
  "v_mfma_f32_16x16x32_bf16 v[204:207], v[40:43], a[108:111], v[204:207]\n" \
  "s_waitcnt lgkmcnt(4)\n" \
  "v_mfma_f32_16x16x32_bf16 v[192:195], v[48:51], a[128:131], v[192:195]\n" \
  "buffer_load_dwordx4 v[116:119], v25, s[60:63], s27 offen offset:3072 sc1\n" \
  "v_mfma_f32_16x16x32_bf16 v[192:195], v[52:55], a[144:147], v[192:195]\n" \
  "ds_read_b64_tr_b16 v[40:41], v2 offset:29184\n" \
  "v_mfma_f32_16x16x32_bf16 v[196:199], v[52:55], a[148:151], v[196:199]\n" \
  "v_mfma_f32_16x16x32_bf16 v[196:199], v[48:51], a[132:135], v[196:199]\n" \
  "ds_read_b64_tr_b16 v[42:43], v2 offset:31232\n" \
  "v_mfma_f32_16x16x32_bf16 v[200:203], v[48:51], a[136:139], v[200:203]\n" \
  "v_mfma_f32_16x16x32_bf16 v[200:203], v[52:55], a[152:155], v[200:203]\n" \
  "ds_read_b64_tr_b16 v[44:45], v2 offset:33280\n" \
  "v_mfma_f32_16x16x32_bf16 v[204:207], v[52:55], a[156:159], v[204:207]\n" \
  "ds_read_b64_tr_b16 v[46:47], v2 offset:35328\n" \
  "v_mfma_f32_16x16x32_bf16 v[204:207], v[48:51], a[140:143], v[204:207]\n" \
  "s_waitcnt lgkmcnt(4)\n" \
  "v_mfma_f32_16x16x32_bf16 v[192:195], v[56:59], v[64:67], v[192:195]\n" \
  "v_mfma_f32_16x16x32_bf16 v[192:195], v[60:63], v[80:83], v[192:195]\n" \
  "ds_read_b64_tr_b16 v[48:49], v2 offset:37376\n" \
  "v_mfma_f32_16x16x32_bf16 v[196:199], v[60:63], v[84:87], v[196:199]\n" \
  "v_mfma_f32_16x16x32_bf16 v[196:199], v[56:59], v[68:71], v[196:199]\n" \
  "ds_read_b64_tr_b16 v[50:51], v2 offset:39424\n" \
  "v_mfma_f32_16x16x32_bf16 v[200:203], v[56:59], v[72:75], v[200:203]\n" \
  "v_mfma_f32_16x16x32_bf16 v[200:203], v[60:63], v[88:91], v[200:203]\n" \
  "ds_read_b64_tr_b16 v[52:53], v2 offset:41472\n" \
  "v_mfma_f32_16x16x32_bf16 v[204:207], v[60:63], v[92:95], v[204:207]\n" \
  "ds_read_b64_tr_b16 v[54:55], v2 offset:43520\n" \
  "v_mfma_f32_16x16x32_bf16 v[204:207], v[56:59], v[76:79], v[204:207]\n" \
  "s_waitcnt lgkmcnt(4)\n" \
  "v_mfma_f32_16x16x32_bf16 v[192:195], v[40:43], a[160:163], v[192:195]\n" \
  "s_mov_b32 s28, 0x70\n" \
  "s_mov_b32 s29, 0\n" \
  "v_mfma_f32_16x16x32_bf16 v[192:195], v[44:47], a[176:179], v[192:195]\n" \
  "ds_read_b64_tr_b16 v[56:57], v2 offset:45568\n" \
  "v_mov_b32 v239, 0x40000000\n" \
  "v_mfma_f32_16x16x32_bf16 v[196:199], v[44:47], a[180:183], v[196:199]\n" \
  "v_cndmask_b32 v239, v239, v14, s[28:29]\n" \
  "v_mfma_f32_16x16x32_bf16 v[196:199], v[40:43], a[164:167], v[196:199]\n" \
  "ds_read_b64_tr_b16 v[58:59], v2 offset:47616\n" \
  "v_mfma_f32_16x16x32_bf16 v[200:203], v[40:43], a[168:171], v[200:203]\n" \
  "buffer_load_dword v27, v239, s[60:63], s87 offen offset:128 sc1\n" \
  "v_mfma_f32_16x16x32_bf16 v[200:203], v[44:47], a[184:187], v[200:203]\n" \
  "ds_read_b64_tr_b16 v[60:61], v2 offset:49664\n" \
  "v_mfma_f32_16x16x32_bf16 v[204:207], v[44:47], a[188:191], v[204:207]\n" \
  "ds_read_b64_tr_b16 v[62:63], v2 offset:51712\n" \
  "v_mfma_f32_16x16x32_bf16 v[204:207], v[40:43], a[172:175], v[204:207]\n" \
  "s_waitcnt lgkmcnt(4)\n" \
  "v_mfma_f32_16x16x32_bf16 v[192:195], v[48:51], a[192:195], v[192:195]\n" \
  "v_mfma_f32_16x16x32_bf16 v[192:195], v[52:55], a[208:211], v[192:195]\n" \
  "ds_read_b64_tr_b16 v[40:41], v7 offset:20480\n" \
  "v_mfma_f32_16x16x32_bf16 v[196:199], v[52:55], a[212:215], v[196:199]\n" \
  "v_mfma_f32_16x16x32_bf16 v[196:199], v[48:51], a[196:199], v[196:199]\n" \
  "ds_read_b64_tr_b16 v[42:43], v7 offset:22528\n" \
  "v_mfma_f32_16x16x32_bf16 v[200:203], v[48:51], a[200:203], v[200:203]\n" \
  "v_mfma_f32_16x16x32_bf16 v[200:203], v[52:55], a[216:219], v[200:203]\n" \
  "ds_read_b64_tr_b16 v[44:45], v7 offset:24576\n" \
  "v_mfma_f32_16x16x32_bf16 v[204:207], v[52:55], a[220:223], v[204:207]\n" \
  "ds_read_b64_tr_b16 v[46:47], v7 offset:26624\n" \
  "v_mfma_f32_16x16x32_bf16 v[204:207], v[48:51], a[204:207], v[204:207]\n" \
  "s_waitcnt lgkmcnt(4)\n" \
  "v_mfma_f32_16x16x32_bf16 v[192:195], v[56:59], a[224:227], v[192:195]\n" \
  "v_mfma_f32_16x16x32_bf16 v[192:195], v[60:63], a[240:243], v[192:195]\n" \
  "ds_read_b64_tr_b16 v[48:49], v7 offset:28672\n" \
  "v_mfma_f32_16x16x32_bf16 v[196:199], v[60:63], a[244:247], v[196:199]\n" \
  "v_mfma_f32_16x16x32_bf16 v[196:199], v[56:59], a[228:231], v[196:199]\n" \
  "ds_read_b64_tr_b16 v[50:51], v7 offset:30720\n" \
  "v_mfma_f32_16x16x32_bf16 v[200:203], v[56:59], a[232:235], v[200:203]\n" \
  "v_mfma_f32_16x16x32_bf16 v[200:203], v[60:63], a[248:251], v[200:203]\n" \
  "ds_read_b64_tr_b16 v[52:53], v7 offset:32768\n" \
  "v_mfma_f32_16x16x32_bf16 v[204:207], v[60:63], a[252:255], v[204:207]\n" \
  "ds_read_b64_tr_b16 v[54:55], v7 offset:34816\n" \
  "v_mfma_f32_16x16x32_bf16 v[204:207], v[56:59], a[236:239], v[204:207]\n" \
  "s_waitcnt lgkmcnt(4)\n" \
  "v_mfma_f32_16x16x32_bf16 v[208:211], v[40:43], a[0:3], 0\n" \
  "s_xor_b32 s96, s95, 32768\n" \
  "v_cvt_pk_bf16_f32 v192, v192, v193\n" \
  "v_mfma_f32_16x16x32_bf16 v[208:211], v[44:47], a[16:19], v[208:211]\n" \
  "ds_read_b64_tr_b16 v[56:57], v7 offset:36864\n" \
  "v_cvt_pk_bf16_f32 v193, v194, v195\n" \
  "v_mfma_f32_16x16x32_bf16 v[212:215], v[44:47], a[20:23], 0\n" \
  "v_cvt_pk_bf16_f32 v194, v196, v197\n" \
  "v_cvt_pk_bf16_f32 v195, v198, v199\n" \
  "v_mfma_f32_16x16x32_bf16 v[212:215], v[40:43], a[4:7], v[212:215]\n" \
  "ds_read_b64_tr_b16 v[58:59], v7 offset:38912\n" \
  "v_mfma_f32_16x16x32_bf16 v[216:219], v[40:43], a[8:11], 0\n" \
  "buffer_store_dwordx4 v[192:195], v24, s[64:67], s96 offen offset:0 sc1\n" \
  "v_mfma_f32_16x16x32_bf16 v[216:219], v[44:47], a[24:27], v[216:219]\n" \
  "ds_read_b64_tr_b16 v[60:61], v3 offset:0\n" \
  "v_mfma_f32_16x16x32_bf16 v[220:223], v[44:47], a[28:31], 0\n" \
  "ds_read_b64_tr_b16 v[62:63], v3 offset:2048\n" \
  "v_mfma_f32_16x16x32_bf16 v[220:223], v[40:43], a[12:15], v[220:223]\n" \
  "s_waitcnt lgkmcnt(4)\n" \
  "v_mfma_f32_16x16x32_bf16 v[208:211], v[48:51], a[32:35], v[208:211]\n" \
  "v_cvt_pk_bf16_f32 v200, v200, v201\n" \
  "v_cvt_pk_bf16_f32 v201, v202, v203\n" \
  "v_mfma_f32_16x16x32_bf16 v[208:211], v[52:55], a[48:51], v[208:211]\n" \
  "ds_read_b64_tr_b16 v[40:41], v3 offset:4096\n" \
  "PG9_%=:\n" \
  "s_cbranch_execz HOPRET_%=\n" \
  "v_cvt_pk_bf16_f32 v202, v204, v205\n" \
  "v_mfma_f32_16x16x32_bf16 v[212:215], v[52:55], a[52:55], v[212:215]\n" \
  "v_cvt_pk_bf16_f32 v203, v206, v207\n" \
  "v_mfma_f32_16x16x32_bf16 v[212:215], v[48:51], a[36:39], v[212:215]\n" \
  "ds_read_b64_tr_b16 v[42:43], v3 offset:6144\n" \
  "v_mfma_f32_16x16x32_bf16 v[216:219], v[48:51], a[40:43], v[216:219]\n" \
  "buffer_store_dwordx4 v[200:203], v24, s[64:67], s96 offen offset:1024 sc1\n" \
  "v_mfma_f32_16x16x32_bf16 v[216:219], v[52:55], a[56:59], v[216:219]\n" \
  "ds_read_b64_tr_b16 v[44:45], v3 offset:8192\n" \
  "v_mfma_f32_16x16x32_bf16 v[220:223], v[52:55], a[60:63], v[220:223]\n" \
  "ds_read_b64_tr_b16 v[46:47], v3 offset:10240\n" \
  "v_mfma_f32_16x16x32_bf16 v[220:223], v[48:51], a[44:47], v[220:223]\n" \
  "s_waitcnt lgkmcnt(4)\n" \
  "v_mfma_f32_16x16x32_bf16 v[208:211], v[56:59], a[64:67], v[208:211]\n" \
  "v_mfma_f32_16x16x32_bf16 v[208:211], v[60:63], a[80:83], v[208:211]\n" \
  "ds_read_b64_tr_b16 v[48:49], v3 offset:12288\n" \
  "v_mfma_f32_16x16x32_bf16 v[212:215], v[60:63], a[84:87], v[212:215]\n" \
  "v_mfma_f32_16x16x32_bf16 v[212:215], v[56:59], a[68:71], v[212:215]\n" \
  "ds_read_b64_tr_b16 v[50:51], v3 offset:14336\n" \
  "v_mfma_f32_16x16x32_bf16 v[216:219], v[56:59], a[72:75], v[216:219]\n" \
  "v_mfma_f32_16x16x32_bf16 v[216:219], v[60:63], a[88:91], v[216:219]\n" \
  "ds_read_b64_tr_b16 v[52:53], v3 offset:16384\n" \
  "v_mfma_f32_16x16x32_bf16 v[220:223], v[60:63], a[92:95], v[220:223]\n" \
  "ds_read_b64_tr_b16 v[54:55], v3 offset:18432\n" \
  "v_mfma_f32_16x16x32_bf16 v[220:223], v[56:59], a[76:79], v[220:223]\n" \
  "s_waitcnt lgkmcnt(4)\n" \
  "v_mfma_f32_16x16x32_bf16 v[208:211], v[40:43], a[96:99], v[208:211]\n" \
  "s_waitcnt vmcnt(2)\n" \
  "v_mfma_f32_16x16x32_bf16 v[208:211], v[44:47], a[112:115], v[208:211]\n" \
  "ds_read_b64_tr_b16 v[56:57], v3 offset:20480\n" \
  "v_mfma_f32_16x16x32_bf16 v[212:215], v[44:47], a[116:119], v[212:215]\n" \
  "v_cmp_ne_u32 vcc, s84, v27\n" \
  "s_and_b32 vcc_lo, vcc_lo, 0x70\n" \
  "s_cbranch_scc1 SLOW_P1_%=\n" \
  "BACK_P1_%=:\n" \
  "v_mfma_f32_16x16x32_bf16 v[212:215], v[40:43], a[100:103], v[212:215]\n" \
  "ds_read_b64_tr_b16 v[58:59], v3 offset:22528\n" \
  "s_xor_b32 s27, s78, 6\n" \
  "v_mfma_f32_16x16x32_bf16 v[216:219], v[40:43], a[104:107], v[216:219]\n" \
  "s_mul_i32 s27, s27, 73728\n" \
  "s_add_u32 s27, s27, s53\n" \
  "v_mfma_f32_16x16x32_bf16 v[216:219], v[44:47], a[120:123], v[216:219]\n" \
  "ds_read_b64_tr_b16 v[60:61], v3 offset:24576\n" \
  "v_mfma_f32_16x16x32_bf16 v[220:223], v[44:47], a[124:127], v[220:223]\n" \
  "ds_read_b64_tr_b16 v[62:63], v3 offset:26624\n" \
  "v_mfma_f32_16x16x32_bf16 v[220:223], v[40:43], a[108:111], v[220:223]\n" \
  "s_waitcnt lgkmcnt(4)\n" \
  "v_mfma_f32_16x16x32_bf16 v[208:211], v[48:51], a[128:131], v[208:211]\n" \
  "buffer_load_dwordx4 v[128:131], v25, s[60:63], s27 offen offset:0 sc1\n" \
  "v_mfma_f32_16x16x32_bf16 v[208:211], v[52:55], a[144:147], v[208:211]\n" \
  "ds_read_b64_tr_b16 v[40:41], v3 offset:28672\n" \
  "v_mfma_f32_16x16x32_bf16 v[212:215], v[52:55], a[148:151], v[212:215]\n" \
  "buffer_load_dwordx4 v[132:135], v25, s[60:63], s27 offen offset:1024 sc1\n" \
  "v_mfma_f32_16x16x32_bf16 v[212:215], v[48:51], a[132:135], v[212:215]\n" \
  "ds_read_b64_tr_b16 v[42:43], v3 offset:30720\n" \
  "v_mfma_f32_16x16x32_bf16 v[216:219], v[48:51], a[136:139], v[216:219]\n" \
  "v_mfma_f32_16x16x32_bf16 v[156:159], v[232:235], v[16:19], 0\n" \
  "v_mfma_f32_16x16x32_bf16 v[156:159], v[232:235], v[20:23], v[156:159]\n" \
  "s_xor_b32 s27, s78, 5\n" \
  "s_mul_i32 s27, s27, 73728\n" \
  "v_mfma_f32_16x16x32_bf16 v[216:219], v[52:55], a[152:155], v[216:219]\n" \
  "ds_read_b64_tr_b16 v[44:45], v3 offset:32768\n" \
  "s_add_u32 s27, s27, s53\n" \
  "v_mfma_f32_16x16x32_bf16 v[220:223], v[52:55], a[156:159], v[220:223]\n" \
  "ds_read_b64_tr_b16 v[46:47], v3 offset:34816\n" \
  "v_mfma_f32_16x16x32_bf16 v[220:223], v[48:51], a[140:143], v[220:223]\n" \
  "s_waitcnt lgkmcnt(4)\n" \
  "v_mfma_f32_16x16x32_bf16 v[208:211], v[56:59], v[64:67], v[208:211]\n" \
  "buffer_load_dwordx4 v[144:147], v24, s[60:63], s27 offen offset:2048 sc1\n" \
  "v_mfma_f32_16x16x32_bf16 v[208:211], v[60:63], v[80:83], v[208:211]\n" \
  "ds_read_b64_tr_b16 v[48:49], v3 offset:36864\n" \
  "v_mfma_f32_16x16x32_bf16 v[212:215], v[60:63], v[84:87], v[212:215]\n" \
  "buffer_load_dwordx4 v[148:151], v24, s[60:63], s27 offen offset:3072 sc1\n" \
  "v_mfma_f32_16x16x32_bf16 v[212:215], v[56:59], v[68:71], v[212:215]\n" \
  "ds_read_b64_tr_b16 v[50:51], v3 offset:38912\n" \
  "v_fma_f32 v156, v156, v172, v168\n" \
  "v_mfma_f32_16x16x32_bf16 v[216:219], v[56:59], v[72:75], v[216:219]\n" \
  "v_fma_f32 v157, v157, v173, v169\n" \
  "v_fma_f32 v158, v158, v174, v170\n" \
  "v_mfma_f32_16x16x32_bf16 v[216:219], v[60:63], v[88:91], v[216:219]\n" \
  "ds_read_b64_tr_b16 v[52:53], v3 offset:40960\n" \
  "v_fma_f32 v159, v159, v175, v171\n" \
  "v_mfma_f32_16x16x32_bf16 v[220:223], v[60:63], v[92:95], v[220:223]\n" \
  "ds_read_b64_tr_b16 v[54:55], v3 offset:43008\n" \
  "v_cvt_pk_bf16_f32 v156, v156, v157\n" \
  "v_mfma_f32_16x16x32_bf16 v[220:223], v[56:59], v[76:79], v[220:223]\n" \
  "v_cvt_pk_bf16_f32 v157, v158, v159\n" \
  "s_waitcnt lgkmcnt(4)\n" \
  "v_mfma_f32_16x16x32_bf16 v[208:211], v[40:43], a[160:163], v[208:211]\n" \
  "s_xor_b32 s27, s78, 4\n" \
  "s_mul_i32 s27, s27, 73728\n" \
  "v_mfma_f32_16x16x32_bf16 v[208:211], v[44:47], a[176:179], v[208:211]\n" \
  "ds_read_b64_tr_b16 v[56:57], v3 offset:45056\n" \
  "s_add_u32 s27, s27, s53\n" \
  "v_mfma_f32_16x16x32_bf16 v[212:215], v[44:47], a[180:183], v[212:215]\n" \
  "buffer_load_dwordx4 v[160:163], v24, s[60:63], s27 offen offset:0 sc1\n" \
  "v_mfma_f32_16x16x32_bf16 v[212:215], v[40:43], a[164:167], v[212:215]\n" \
  "ds_read_b64_tr_b16 v[58:59], v3 offset:47104\n" \
  "v_mfma_f32_16x16x32_bf16 v[216:219], v[40:43], a[168:171], v[216:219]\n" \
  "buffer_load_dwordx4 v[164:167], v24, s[60:63], s27 offen offset:1024 sc1\n" \
  "v_mfma_f32_16x16x32_bf16 v[216:219], v[44:47], a[184:187], v[216:219]\n" \
  "ds_read_b64_tr_b16 v[60:61], v3 offset:49152\n" \
  "v_mfma_f32_16x16x32_bf16 v[220:223], v[44:47], a[188:191], v[220:223]\n" \
  "ds_read_b64_tr_b16 v[62:63], v3 offset:51200\n" \
  "v_mfma_f32_16x16x32_bf16 v[220:223], v[40:43], a[172:175], v[220:223]\n" \
  "s_waitcnt lgkmcnt(4)\n" \
  "v_mfma_f32_16x16x32_bf16 v[208:211], v[48:51], a[192:195], v[208:211]\n" \
  "v_mfma_f32_16x16x32_bf16 v[208:211], v[52:55], a[208:211], v[208:211]\n" \
  "ds_read_b64_tr_b16 v[40:41], v6 offset:20480\n" \
  "v_mfma_f32_16x16x32_bf16 v[212:215], v[52:55], a[212:215], v[212:215]\n" \
  "v_mfma_f32_16x16x32_bf16 v[212:215], v[48:51], a[196:199], v[212:215]\n" \
  "ds_read_b64_tr_b16 v[42:43], v6 offset:22528\n" \
  "v_mfma_f32_16x16x32_bf16 v[216:219], v[48:51], a[200:203], v[216:219]\n" \
  "v_mfma_f32_16x16x32_bf16 v[216:219], v[52:55], a[216:219], v[216:219]\n" \
  "ds_read_b64_tr_b16 v[44:45], v6 offset:24576\n" \
  "v_mfma_f32_16x16x32_bf16 v[220:223], v[52:55], a[220:223], v[220:223]\n" \
  "ds_read_b64_tr_b16 v[46:47], v6 offset:26624\n" \
  "v_mfma_f32_16x16x32_bf16 v[220:223], v[48:51], a[204:207], v[220:223]\n" \
  "s_waitcnt lgkmcnt(4)\n" \
  "v_mfma_f32_16x16x32_bf16 v[208:211], v[56:59], a[224:227], v[208:211]\n" \
  "s_waitcnt vmcnt(6)\n" \
  "v_mfma_f32_16x16x32_bf16 v[208:211], v[60:63], a[240:243], v[208:211]\n" \
  "ds_read_b64_tr_b16 v[48:49], v6 offset:28672\n" \
  "v_mfma_f32_16x16x32_bf16 v[212:215], v[60:63], a[244:247], v[212:215]\n" \
  "buffer_store_dword v28, v1, s[60:63], s52 offen offset:256 sc1\n" \
  "v_mfma_f32_16x16x32_bf16 v[212:215], v[56:59], a[228:231], v[212:215]\n" \
  "ds_read_b64_tr_b16 v[50:51], v6 offset:30720\n" \
  "v_mfma_f32_16x16x32_bf16 v[216:219], v[56:59], a[232:235], v[216:219]\n" \
  "v_mfma_f32_16x16x32_bf16 v[216:219], v[60:63], a[248:251], v[216:219]\n" \
  "ds_read_b64_tr_b16 v[52:53], v6 offset:32768\n" \
  "v_mfma_f32_16x16x32_bf16 v[220:223], v[60:63], a[252:255], v[220:223]\n" \
  "ds_read_b64_tr_b16 v[54:55], v6 offset:34816\n" \
  "v_mfma_f32_16x16x32_bf16 v[220:223], v[56:59], a[236:239], v[220:223]\n" \
  "s_waitcnt lgkmcnt(4)\n" \
  "v_mfma_f32_16x16x32_bf16 v[96:99], v[40:43], a[0:3], 0\n" \
  "s_xor_b32 s96, s95, 16384\n" \
  "v_cvt_pk_bf16_f32 v208, v208, v209\n" \
  "v_mfma_f32_16x16x32_bf16 v[96:99], v[44:47], a[16:19], v[96:99]\n" \
  "ds_read_b64_tr_b16 v[56:57], v6 offset:36864\n" \
  "v_cvt_pk_bf16_f32 v209, v210, v211\n" \
  "v_mfma_f32_16x16x32_bf16 v[100:103], v[44:47], a[20:23], 0\n" \
  "v_cvt_pk_bf16_f32 v210, v212, v213\n" \
  "v_cvt_pk_bf16_f32 v211, v214, v215\n" \
  "v_mfma_f32_16x16x32_bf16 v[100:103], v[40:43], a[4:7], v[100:103]\n" \
  "ds_read_b64_tr_b16 v[58:59], v6 offset:38912\n" \
  "v_mfma_f32_16x16x32_bf16 v[104:107], v[40:43], a[8:11], 0\n" \
  "buffer_store_dwordx4 v[208:211], v24, s[64:67], s96 offen offset:0 sc1\n" \
  "v_mfma_f32_16x16x32_bf16 v[104:107], v[44:47], a[24:27], v[104:107]\n" \
  "ds_read_b64_tr_b16 v[60:61], v2 offset:0\n" \
  "v_cvt_pk_bf16_f32 v216, v216, v217\n" \
  "v_mfma_f32_16x16x32_bf16 v[108:111], v[44:47], a[28:31], 0\n" \
  "ds_read_b64_tr_b16 v[62:63], v2 offset:2048\n" \
  "v_cvt_pk_bf16_f32 v217, v218, v219\n" \
  "v_mfma_f32_16x16x32_bf16 v[108:111], v[40:43], a[12:15], v[108:111]\n" \
  "v_cvt_pk_bf16_f32 v218, v220, v221\n" \
  "s_waitcnt lgkmcnt(4)\n" \
  "v_mfma_f32_16x16x32_bf16 v[96:99], v[48:51], a[32:35], v[96:99]\n" \
  "v_cvt_pk_bf16_f32 v219, v222, v223\n" \
  "v_mfma_f32_16x16x32_bf16 v[96:99], v[52:55], a[48:51], v[96:99]\n" \
  "ds_read_b64_tr_b16 v[40:41], v2 offset:4096\n" \
  "v_mfma_f32_16x16x32_bf16 v[100:103], v[52:55], a[52:55], v[100:103]\n" \
  "buffer_store_dwordx4 v[216:219], v24, s[64:67], s96 offen offset:1024 sc1\n" \
  "v_mfma_f32_16x16x32_bf16 v[100:103], v[48:51], a[36:39], v[100:103]\n" \
  "ds_read_b64_tr_b16 v[42:43], v2 offset:6144\n" \
  "v_mfma_f32_16x16x32_bf16 v[104:107], v[48:51], a[40:43], v[104:107]\n" \
  "v_mfma_f32_16x16x32_bf16 v[104:107], v[52:55], a[56:59], v[104:107]\n" \
  "ds_read_b64_tr_b16 v[44:45], v2 offset:8192\n" \
  "v_mfma_f32_16x16x32_bf16 v[108:111], v[52:55], a[60:63], v[108:111]\n" \
  "ds_read_b64_tr_b16 v[46:47], v2 offset:10240\n" \
  "v_mfma_f32_16x16x32_bf16 v[108:111], v[48:51], a[44:47], v[108:111]\n" \
  "s_waitcnt lgkmcnt(4)\n" \
  "v_mfma_f32_16x16x32_bf16 v[96:99], v[56:59], a[64:67], v[96:99]\n" \
  "s_waitcnt vmcnt(7)\n" \
  "v_swap_b32 v114, v128\n" \
  "v_mfma_f32_16x16x32_bf16 v[96:99], v[60:63], a[80:83], v[96:99]\n" \
  "ds_read_b64_tr_b16 v[48:49], v2 offset:12288\n" \
  "v_swap_b32 v115, v129\n" \
  "v_mfma_f32_16x16x32_bf16 v[100:103], v[60:63], a[84:87], v[100:103]\n" \
  "v_swap_b32 v118, v132\n" \
  "v_swap_b32 v119, v133\n" \
  "v_mfma_f32_16x16x32_bf16 v[100:103], v[56:59], a[68:71], v[100:103]\n" \
  "ds_read_b64_tr_b16 v[50:51], v2 offset:14336\n" \
  "v_mfma_f32_16x16x32_bf16 v[96:99], v[226:229], v[112:115], v[96:99]\n" \
  "v_mfma_f32_16x16x32_bf16 v[100:103], v[226:229], v[128:131], v[100:103]\n" \
  "v_mfma_f32_16x16x32_bf16 v[104:107], v[226:229], v[116:119], v[104:107]\n" \
  "v_mfma_f32_16x16x32_bf16 v[108:111], v[226:229], v[132:135], v[108:111]\n" \
  "v_mfma_f32_16x16x32_bf16 v[104:107], v[56:59], a[72:75], v[104:107]\n" \
  "v_mfma_f32_16x16x32_bf16 v[104:107], v[60:63], a[88:91], v[104:107]\n" \
  "ds_read_b64_tr_b16 v[52:53], v2 offset:16384\n" \
  "v_mfma_f32_16x16x32_bf16 v[108:111], v[60:63], a[92:95], v[108:111]\n" \
  "ds_read_b64_tr_b16 v[54:55], v2 offset:18432\n" \
  "v_mfma_f32_16x16x32_bf16 v[108:111], v[56:59], a[76:79], v[108:111]\n" \
  "s_waitcnt lgkmcnt(4)\n" \
  "v_mfma_f32_16x16x32_bf16 v[96:99], v[40:43], a[96:99], v[96:99]\n" \
  "v_mfma_f32_16x16x32_bf16 v[96:99], v[44:47], a[112:115], v[96:99]\n" \
  "ds_read_b64_tr_b16 v[56:57], v2 offset:20480\n" \
  "v_mfma_f32_16x16x32_bf16 v[100:103], v[44:47], a[116:119], v[100:103]\n" \
  "v_mfma_f32_16x16x32_bf16 v[100:103], v[40:43], a[100:103], v[100:103]\n" \
  "ds_read_b64_tr_b16 v[58:59], v2 offset:22528\n" \
  "v_mfma_f32_16x16x32_bf16 v[104:107], v[40:43], a[104:107], v[104:107]\n" \
  "v_mfma_f32_16x16x32_bf16 v[104:107], v[44:47], a[120:123], v[104:107]\n" \
  "ds_read_b64_tr_b16 v[60:61], v2 offset:24576\n" \
  "v_mfma_f32_16x16x32_bf16 v[108:111], v[44:47], a[124:127], v[108:111]\n" \
  "ds_read_b64_tr_b16 v[62:63], v2 offset:26624\n" \
  "v_mfma_f32_16x16x32_bf16 v[108:111], v[40:43], a[108:111], v[108:111]\n" \
  "s_waitcnt lgkmcnt(4)\n" \
  "v_mfma_f32_16x16x32_bf16 v[96:99], v[48:51], a[128:131], v[96:99]\n" \
  "s_mov_b32 s28, 0xc\n" \
  "s_mov_b32 s29, 0\n" \
  "v_mfma_f32_16x16x32_bf16 v[96:99], v[52:55], a[144:147], v[96:99]\n" \
  "ds_read_b64_tr_b16 v[40:41], v2 offset:28672\n" \
  "v_mov_b32 v239, 0x40000000\n" \
  "v_mfma_f32_16x16x32_bf16 v[100:103], v[52:55], a[148:151], v[100:103]\n" \
  "v_cndmask_b32 v239, v239, v14, s[28:29]\n" \
  "v_mfma_f32_16x16x32_bf16 v[100:103], v[48:51], a[132:135], v[100:103]\n" \
  "ds_read_b64_tr_b16 v[42:43], v2 offset:30720\n" \
  "v_mfma_f32_16x16x32_bf16 v[104:107], v[48:51], a[136:139], v[104:107]\n" \
  "buffer_load_dword v27, v239, s[60:63], s87 offen offset:256 sc1\n" \
  "v_mfma_f32_16x16x32_bf16 v[104:107], v[52:55], a[152:155], v[104:107]\n" \
  "ds_read_b64_tr_b16 v[44:45], v2 offset:32768\n" \
  "s_waitcnt vmcnt(4)\n" \
  "v_mfma_f32_16x16x32_bf16 v[108:111], v[52:55], a[156:159], v[108:111]\n" \
  "ds_read_b64_tr_b16 v[46:47], v2 offset:34816\n" \
  "v_swap_b32 v146, v160\n" \
  "v_mfma_f32_16x16x32_bf16 v[108:111], v[48:51], a[140:143], v[108:111]\n" \
  "v_swap_b32 v147, v161\n" \
  "s_waitcnt lgkmcnt(4)\n" \
  "v_mfma_f32_16x16x32_bf16 v[96:99], v[56:59], v[64:67], v[96:99]\n" \
  "v_swap_b32 v150, v164\n" \
  "v_swap_b32 v151, v165\n" \
  "v_mfma_f32_16x16x32_bf16 v[96:99], v[60:63], v[80:83], v[96:99]\n" \
  "ds_read_b64_tr_b16 v[48:49], v2 offset:36864\n" \
  "v_mfma_f32_16x16x32_bf16 v[96:99], v[226:229], v[144:147], v[96:99]\n" \
  "v_mfma_f32_16x16x32_bf16 v[100:103], v[226:229], v[160:163], v[100:103]\n" \
  "v_mfma_f32_16x16x32_bf16 v[104:107], v[226:229], v[148:151], v[104:107]\n" \
  "v_mfma_f32_16x16x32_bf16 v[108:111], v[226:229], v[164:167], v[108:111]\n" \
  "v_mfma_f32_16x16x32_bf16 v[100:103], v[60:63], v[84:87], v[100:103]\n" \
  "v_mfma_f32_16x16x32_bf16 v[100:103], v[56:59], v[68:71], v[100:103]\n" \
  "ds_read_b64_tr_b16 v[50:51], v2 offset:38912\n" \
  "v_mfma_f32_16x16x32_bf16 v[104:107], v[56:59], v[72:75], v[104:107]\n" \
  "v_mfma_f32_16x16x32_bf16 v[104:107], v[60:63], v[88:91], v[104:107]\n" \
  "ds_read_b64_tr_b16 v[52:53], v2 offset:40960\n" \
  "v_mfma_f32_16x16x32_bf16 v[108:111], v[60:63], v[92:95], v[108:111]\n" \
  "ds_read_b64_tr_b16 v[54:55], v2 offset:43008\n" \
  "v_mfma_f32_16x16x32_bf16 v[108:111], v[56:59], v[76:79], v[108:111]\n" \
  "s_waitcnt lgkmcnt(4)\n" \
  "v_mfma_f32_16x16x32_bf16 v[96:99], v[40:43], a[160:163], v[96:99]\n" \
  "v_mfma_f32_16x16x32_bf16 v[96:99], v[44:47], a[176:179], v[96:99]\n" \
  "ds_read_b64_tr_b16 v[56:57], v2 offset:45056\n" \
  "v_mfma_f32_16x16x32_bf16 v[100:103], v[44:47], a[180:183], v[100:103]\n" \
  "v_mfma_f32_16x16x32_bf16 v[100:103], v[40:43], a[164:167], v[100:103]\n" \
  "ds_read_b64_tr_b16 v[58:59], v2 offset:47104\n" \
  "v_mfma_f32_16x16x32_bf16 v[104:107], v[40:43], a[168:171], v[104:107]\n" \
  "v_mfma_f32_16x16x32_bf16 v[104:107], v[44:47], a[184:187], v[104:107]\n" \
  "ds_read_b64_tr_b16 v[60:61], v2 offset:49152\n" \
  "v_mfma_f32_16x16x32_bf16 v[108:111], v[44:47], a[188:191], v[108:111]\n" \
  "ds_read_b64_tr_b16 v[62:63], v2 offset:51200\n" \
  "v_mfma_f32_16x16x32_bf16 v[108:111], v[40:43], a[172:175], v[108:111]\n" \
  "s_waitcnt lgkmcnt(4)\n" \
  "v_mfma_f32_16x16x32_bf16 v[96:99], v[48:51], a[192:195], v[96:99]\n" \
  "s_waitcnt vmcnt(1)\n" \
  "v_mfma_f32_16x16x32_bf16 v[96:99], v[52:55], a[208:211], v[96:99]\n" \
  "buffer_store_dword v28, v1, s[60:63], s52 offen offset:384 sc1\n" \
  "v_mfma_f32_16x16x32_bf16 v[100:103], v[52:55], a[212:215], v[100:103]\n" \
  "v_mfma_f32_16x16x32_bf16 v[100:103], v[48:51], a[196:199], v[100:103]\n" \
  "v_mfma_f32_16x16x32_bf16 v[104:107], v[48:51], a[200:203], v[104:107]\n" \
  "v_mfma_f32_16x16x32_bf16 v[104:107], v[52:55], a[216:219], v[104:107]\n" \
  "v_mfma_f32_16x16x32_bf16 v[108:111], v[52:55], a[220:223], v[108:111]\n" \
  "v_mfma_f32_16x16x32_bf16 v[108:111], v[48:51], a[204:207], v[108:111]\n" \
  "s_waitcnt lgkmcnt(0)\n" \
  "v_mfma_f32_16x16x32_bf16 v[96:99], v[56:59], a[224:227], v[96:99]\n" \
  "v_mfma_f32_16x16x32_bf16 v[96:99], v[60:63], a[240:243], v[96:99]\n" \
  "v_mfma_f32_16x16x32_bf16 v[100:103], v[60:63], a[244:247], v[100:103]\n" \
  "v_mfma_f32_16x16x32_bf16 v[100:103], v[56:59], a[228:231], v[100:103]\n" \
  "v_mfma_f32_16x16x32_bf16 v[104:107], v[56:59], a[232:235], v[104:107]\n" \
  "v_mfma_f32_16x16x32_bf16 v[104:107], v[60:63], a[248:251], v[104:107]\n" \
  "v_mfma_f32_16x16x32_bf16 v[108:111], v[60:63], a[252:255], v[108:111]\n" \
  "v_mfma_f32_16x16x32_bf16 v[108:111], v[56:59], a[236:239], v[108:111]\n" \
  "s_waitcnt vmcnt(1)\n" \
  "v_cmp_ne_u32 vcc, s84, v27\n" \
  "s_and_b32 vcc_lo, vcc_lo, 0xc\n" \
  "s_cbranch_scc1 SLOW_P2_%=\n" \
  "BACK_P2_%=:\n" \
  "s_xor_b32 s27, s95, 6144\n" \
  "buffer_load_dwordx4 v[176:179], v24, s[64:67], s27 offen offset:0 sc1\n" \
  "buffer_load_dwordx4 v[180:183], v24, s[64:67], s27 offen offset:1024 sc1\n" \
  "s_xor_b32 s27, s95, 4096\n" \
  "buffer_load_dwordx4 v[192:195], v24, s[64:67], s27 offen offset:0 sc1\n" \
  "buffer_load_dwordx4 v[196:199], v24, s[64:67], s27 offen offset:1024 sc1\n" \
  "s_mov_b32 s28, 0x2\n" \
  "s_mov_b32 s29, 0\n" \
  "v_mov_b32 v239, 0x40000000\n" \
  "v_cndmask_b32 v239, v239, v14, s[28:29]\n" \
  "buffer_load_dword v27, v239, s[60:63], s87 offen offset:384 sc1\n" \
  "s_waitcnt vmcnt(0)\n" \
  "v_cmp_ne_u32 vcc, s84, v27\n" \
  "s_and_b32 vcc_lo, vcc_lo, 0x2\n" \
  "s_cbranch_scc1 SLOW_P3_%=\n" \
  "BACK_P3_%=:\n" \
  "s_xor_b32 s27, s95, 2048\n" \
  "buffer_load_dwordx4 v[208:211], v24, s[64:67], s27 offen offset:0 sc1\n" \
  "buffer_load_dwordx4 v[212:215], v24, s[64:67], s27 offen offset:1024 sc1\n" \
  "v_swap_b32 v178, v192\n" \
  "v_swap_b32 v179, v193\n" \
  "v_swap_b32 v182, v196\n" \
  "v_swap_b32 v183, v197\n" \
  "v_mfma_f32_16x16x32_bf16 v[96:99], v[226:229], v[176:179], v[96:99]\n" \
  "v_mfma_f32_16x16x32_bf16 v[100:103], v[226:229], v[192:195], v[100:103]\n" \
  "v_mfma_f32_16x16x32_bf16 v[104:107], v[226:229], v[180:183], v[104:107]\n" \
  "v_mfma_f32_16x16x32_bf16 v[108:111], v[226:229], v[196:199], v[108:111]\n" \
  "s_waitcnt vmcnt(0)\n" \
  "v_mfma_f32_16x16x32_bf16 v[96:99], v[228:231], v[208:211], v[96:99]\n" \
  "v_mfma_f32_16x16x32_bf16 v[100:103], v[224:227], v[208:211], v[100:103]\n" \
  "v_mfma_f32_16x16x32_bf16 v[104:107], v[228:231], v[212:215], v[104:107]\n" \
  "v_mfma_f32_16x16x32_bf16 v[108:111], v[224:227], v[212:215], v[108:111]\n" \
  "buffer_store_dwordx2 v[156:157], v237, s[68:71], 0 offen sc0 sc1\n" \
  "s_nop 3\n" \
  "v_fma_f32 v96, v96, v152, v120\n" \
  "v_fma_f32 v97, v97, v153, v121\n" \
  "v_fma_f32 v98, v98, v154, v122\n" \
  "v_fma_f32 v99, v99, v155, v123\n" \
  "v_cvt_pk_bf16_f32 v96, v96, v97\n" \
  "v_cvt_pk_bf16_f32 v97, v98, v99\n" \
  "ds_write_b64 v254, v[96:97] offset:0\n" \
  "v_fma_f32 v100, v100, v152, v124\n" \
  "v_fma_f32 v101, v101, v153, v125\n" \
  "v_fma_f32 v102, v102, v154, v126\n" \
  "v_fma_f32 v103, v103, v155, v127\n" \
  "v_cvt_pk_bf16_f32 v100, v100, v101\n" \
  "v_cvt_pk_bf16_f32 v101, v102, v103\n" \
  "ds_write_b64 v254, v[100:101] offset:512\n" \
  "v_fma_f32 v104, v104, v152, v136\n" \
  "v_fma_f32 v105, v105, v153, v137\n" \
  "v_fma_f32 v106, v106, v154, v138\n" \
  "v_fma_f32 v107, v107, v155, v139\n" \
  "v_cvt_pk_bf16_f32 v104, v104, v105\n" \
  "v_cvt_pk_bf16_f32 v105, v106, v107\n" \
  "ds_write_b64 v254, v[104:105] offset:1024\n" \
  "v_fma_f32 v108, v108, v152, v140\n" \
  "v_fma_f32 v109, v109, v153, v141\n" \
  "v_fma_f32 v110, v110, v154, v142\n" \
  "v_fma_f32 v111, v111, v155, v143\n" \
  "v_cvt_pk_bf16_f32 v108, v108, v109\n" \
  "v_cvt_pk_bf16_f32 v109, v110, v111\n" \
  "ds_write_b64 v254, v[108:109] offset:1536\n" \
  "ds_read_b128 v[160:163], v255\n" \
  "ds_read_b128 v[164:167], v255 offset:1024\n" \
  "s_waitcnt lgkmcnt(1)\n" \
  "buffer_store_dwordx4 v[160:163], v236, s[68:71], s99 offen sc0 sc1\n" \
  "s_waitcnt lgkmcnt(0)\n" \
  "buffer_store_dwordx4 v[164:167], v236, s[68:71], s100 offen sc0 sc1\n" \
  "s_endpgm\n" \
  "HOPIN_%=:\n" \
  "s_add_u32 s100, s100, 4\n" \
  "s_addc_u32 s101, s101, 0\n" \
  "s_mov_b64 exec, 0\n" \
  "s_cmp_eq_u32 s80, 0\n" \
  "s_cbranch_scc1 HOP0_%=\n" \
  "s_cmp_eq_u32 s80, 1\n" \
  "s_cbranch_scc1 HOP1_%=\n" \
  "s_cmp_eq_u32 s80, 2\n" \
  "s_cbranch_scc1 HOP2_%=\n" \
  "s_branch HOP3_%=\n" \
  "HOPRET_%=:\n" \
  "s_mov_b64 exec, -1\n" \
  "s_setpc_b64 s[100:101]\n" \
  "SLOW_M0_%=:\n" \
  "s_mov_b32 s85, 262144\n" \
  "SL_M0_%=:\n" \
  "s_nop 1\n" \
  "s_mov_b64 exec, vcc\n" \
  "s_sleep 2\n" \
  "buffer_load_dwordx3 v[116:118], v250, s[60:63], s21 offen offset:0 sc1\n" \
  "buffer_load_dwordx3 v[120:122], v250, s[60:63], s21 offen offset:512 sc1\n" \
  "buffer_load_dwordx3 v[124:126], v250, s[60:63], s22 offen offset:0 sc1\n" \
  "buffer_load_dwordx3 v[128:130], v250, s[60:63], s22 offen offset:512 sc1\n" \
  "buffer_load_dwordx3 v[132:134], v249, s[60:63], s23 offen offset:0 sc1\n" \
  "buffer_load_dwordx3 v[136:138], v249, s[60:63], s23 offen offset:512 sc1\n" \
  "s_waitcnt vmcnt(0)\n" \
  "v_xor_b32 v27, s84, v118\n" \
  "v_bitop3_b32 v27, v27, v122, s84 bitop3:0xf6\n" \
  "v_bitop3_b32 v27, v27, v126, s84 bitop3:0xf6\n" \
  "v_bitop3_b32 v27, v27, v130, s84 bitop3:0xf6\n" \
  "v_bitop3_b32 v27, v27, v134, s84 bitop3:0xf6\n" \
  "v_bitop3_b32 v27, v27, v138, s84 bitop3:0xf6\n" \
  "v_cmp_ne_u32 vcc, 0, v27\n" \
  "s_cbranch_vccz SE_M0_%=\n" \
  "s_sub_u32 s85, s85, 1\n" \
  "s_cmp_lg_u32 s85, 0\n" \
  "s_cbranch_scc1 SL_M0_%=\n" \
  "SE_M0_%=:\n" \
  "s_mov_b64 exec, -1\n" \
  "s_nop 3\n" \
  "s_branch BACK_M0_%=\n" \
  "SLOW_ZA_%=:\n" \
  "s_mov_b32 s85, 262144\n" \
  "SL_ZA_%=:\n" \
  "s_nop 1\n" \
  "s_mov_b64 exec, vcc\n" \
  "s_sleep 2\n" \
  "buffer_load_dwordx4 v[172:175], v245, s[60:63], s6 offen sc1 \n" \
  "buffer_load_dwordx4 v[176:179], v245, s[60:63], s7 offen sc1 \n" \
  "buffer_load_dwordx4 v[180:183], v245, s[60:63], s8 offen sc1 \n" \
  "buffer_load_dwordx4 v[184:187], v245, s[60:63], s9 offen sc1 \n" \
  "s_waitcnt vmcnt(0)\n" \
  "v_xor_b32 v27, v227, v175\n" \
  "v_bitop3_b32 v27, v27, v179, v227 bitop3:0xf6\n" \
  "v_bitop3_b32 v27, v27, v183, v227 bitop3:0xf6\n" \
  "v_bitop3_b32 v27, v27, v187, v227 bitop3:0xf6\n" \
  "v_cmp_ne_u32 vcc, 0, v27\n" \
  "s_cbranch_vccz SE_ZA_%=\n" \
  "s_sub_u32 s85, s85, 1\n" \
  "s_cmp_lg_u32 s85, 0\n" \
  "s_cbranch_scc1 SL_ZA_%=\n" \
  "SE_ZA_%=:\n" \
  "s_mov_b64 exec, -1\n" \
  "s_nop 3\n" \
  "s_branch BACK_ZA_%=\n" \
  "SLOW_M1_%=:\n" \
  "s_mov_b32 s85, 262144\n" \
  "SL_M1_%=:\n" \
  "s_nop 1\n" \
  "s_mov_b64 exec, vcc\n" \
  "s_sleep 2\n" \
  "buffer_load_dwordx3 v[16:18], v250, s[60:63], s21 offen offset:0 sc1\n" \
  "buffer_load_dwordx3 v[20:22], v250, s[60:63], s21 offen offset:512 sc1\n" \
  "buffer_load_dwordx3 v[80:82], v250, s[60:63], s22 offen offset:0 sc1\n" \
  "buffer_load_dwordx3 v[84:86], v250, s[60:63], s22 offen offset:512 sc1\n" \
  "buffer_load_dwordx3 v[88:90], v250, s[60:63], s23 offen offset:0 sc1\n" \
  "buffer_load_dwordx3 v[92:94], v250, s[60:63], s23 offen offset:512 sc1\n" \
  "buffer_load_dwordx3 v[96:98], v250, s[60:63], s5 offen offset:0 sc1\n" \
  "buffer_load_dwordx3 v[100:102], v250, s[60:63], s5 offen offset:512 sc1\n" \
  "buffer_load_dwordx3 v[104:106], v249, s[60:63], s11 offen offset:0 sc1\n" \
  "buffer_load_dwordx3 v[108:110], v249, s[60:63], s11 offen offset:512 sc1\n" \
  "s_waitcnt vmcnt(0)\n" \
  "v_xor_b32 v27, s84, v18\n" \
  "v_bitop3_b32 v27, v27, v22, s84 bitop3:0xf6\n" \
  "v_bitop3_b32 v27, v27, v82, s84 bitop3:0xf6\n" \
  "v_bitop3_b32 v27, v27, v86, s84 bitop3:0xf6\n" \
  "v_bitop3_b32 v27, v27, v90, s84 bitop3:0xf6\n" \
  "v_bitop3_b32 v27, v27, v94, s84 bitop3:0xf6\n" \
  "v_bitop3_b32 v27, v27, v98, s84 bitop3:0xf6\n" \
  "v_bitop3_b32 v27, v27, v102, s84 bitop3:0xf6\n" \
  "v_bitop3_b32 v27, v27, v106, s84 bitop3:0xf6\n" \
  "v_bitop3_b32 v27, v27, v110, s84 bitop3:0xf6\n" \
  "v_cmp_ne_u32 vcc, 0, v27\n" \
  "s_cbranch_vccz SE_M1_%=\n" \
  "s_sub_u32 s85, s85, 1\n" \
  "s_cmp_lg_u32 s85, 0\n" \
  "s_cbranch_scc1 SL_M1_%=\n" \
  "SE_M1_%=:\n" \
  "s_mov_b64 exec, -1\n" \
  "s_nop 3\n" \
  "s_branch BACK_M1_%=\n" \
  "SLOW_ZB_%=:\n" \
  "s_mov_b32 s85, 262144\n" \
  "SL_ZB_%=:\n" \
  "s_nop 1\n" \
  "s_mov_b64 exec, vcc\n" \
  "s_sleep 2\n" \
  "buffer_load_dwordx4 v[104:107], v253, s[60:63], s6 offen sc1 \n" \
  "buffer_load_dwordx4 v[108:111], v250, s[60:63], s7 offen sc1 \n" \
  "buffer_load_dwordx4 v[112:115], v253, s[60:63], s8 offen sc1 \n" \
  "buffer_load_dwordx4 v[116:119], v250, s[60:63], s9 offen sc1 \n" \
  "s_waitcnt vmcnt(0)\n" \
  "v_xor_b32 v27, v227, v107\n" \
  "v_bitop3_b32 v27, v27, v111, v249 bitop3:0xf6\n" \
  "v_bitop3_b32 v27, v27, v115, v227 bitop3:0xf6\n" \
  "v_bitop3_b32 v27, v27, v119, v249 bitop3:0xf6\n" \
  "v_cmp_ne_u32 vcc, 0, v27\n" \
  "s_cbranch_vccz SE_ZB_%=\n" \
  "s_sub_u32 s85, s85, 1\n" \
  "s_cmp_lg_u32 s85, 0\n" \
  "s_cbranch_scc1 SL_ZB_%=\n" \
  "SE_ZB_%=:\n" \
  "s_mov_b64 exec, -1\n" \
  "s_nop 3\n" \
  "s_branch BACK_ZB_%=\n" \
  "SLOW_HS_%=:\n" \
  "s_mov_b32 s85, 262144\n" \
  "SL_HS_%=:\n" \
  "s_nop 1\n" \
  "s_mov_b64 exec, vcc\n" \
  "s_sleep 2\n" \
  "buffer_load_dwordx4 v[64:67], v24, s[52:55], s74 offen offset:0 sc1\n" \
  "buffer_load_dwordx4 v[68:71], v24, s[52:55], s74 offen offset:1024 sc1\n" \
  "buffer_load_dwordx4 v[72:75], v24, s[52:55], s74 offen offset:2048 sc1\n" \
  "buffer_load_dwordx4 v[76:79], v24, s[52:55], s74 offen offset:3072 sc1\n" \
  "buffer_load_dwordx4 v[80:83], v25, s[52:55], s74 offen offset:0 sc1\n" \
  "buffer_load_dwordx4 v[84:87], v25, s[52:55], s74 offen offset:1024 sc1\n" \
  "buffer_load_dwordx4 v[88:91], v25, s[52:55], s74 offen offset:2048 sc1\n" \
  "buffer_load_dwordx4 v[92:95], v25, s[52:55], s74 offen offset:3072 sc1\n" \
  "s_waitcnt vmcnt(0)\n" \
  "v_max3_u32 v27, v64, v65, v66\n" \
  "v_max3_u32 v27, v27, v67, v68\n" \
  "v_max3_u32 v27, v27, v69, v70\n" \
  "v_max3_u32 v27, v27, v71, v72\n" \
  "v_max3_u32 v27, v27, v73, v74\n" \
  "v_max3_u32 v27, v27, v75, v76\n" \
  "v_max3_u32 v27, v27, v77, v78\n" \
  "v_max3_u32 v27, v27, v79, v80\n" \
  "v_max3_u32 v27, v27, v81, v82\n" \
  "v_max3_u32 v27, v27, v83, v84\n" \
  "v_max3_u32 v27, v27, v85, v86\n" \
  "v_max3_u32 v27, v27, v87, v88\n" \
  "v_max3_u32 v27, v27, v89, v90\n" \
  "v_max3_u32 v27, v27, v91, v92\n" \
  "v_max3_u32 v27, v27, v93, v94\n" \
  "v_max3_u32 v27, v27, v95, v95\n" \
  "v_cmp_eq_u32 vcc, -1, v27\n" \
  "s_cbranch_vccz SE_HS_%=\n" \
  "s_sub_u32 s85, s85, 1\n" \
  "s_cmp_lg_u32 s85, 0\n" \
  "s_cbranch_scc1 SL_HS_%=\n" \
  "SE_HS_%=:\n" \
  "s_mov_b64 exec, -1\n" \
  "s_nop 3\n" \
  "s_branch BACK_HS_%=\n" \
  "SLOW_H2_%=:\n" \
  "s_mov_b32 s85, 262144\n" \
  "SL_H2_%=:\n" \
  "buffer_load_dword v27, v37, s[60:63], s75 offen offset:0 sc1\n" \
  "s_waitcnt vmcnt(0)\n" \
  "v_cmp_ne_u32 vcc, s84, v27\n" \
  "s_and_b32 vcc_lo, vcc_lo, 0xff\n" \
  "s_cbranch_scc0 BACK_H2_%=\n" \
  "s_sub_u32 s85, s85, 1\n" \
  "s_cmp_lg_u32 s85, 0\n" \
  "s_cbranch_scc1 SL_H2_%=\n" \
  "s_branch BACK_H2_%=\n" \
  "SLOW_HE_%=:\n" \
  "s_mov_b32 s85, 262144\n" \
  "SL_HE_%=:\n" \
  "buffer_load_dword v27, v26, s[60:63], s86 offen offset:0 sc1\n" \
  "s_waitcnt vmcnt(0)\n" \
  "v_cmp_ne_u32 vcc, s84, v27\n" \
  "s_and_b32 vcc_lo, vcc_lo, 0xe\n" \
  "s_cbranch_scc0 BACK_HE_%=\n" \
  "s_sub_u32 s85, s85, 1\n" \
  "s_cmp_lg_u32 s85, 0\n" \
  "s_cbranch_scc1 SL_HE_%=\n" \
  "s_branch BACK_HE_%=\n" \
  "SLOW_HL_%=:\n" \
  "s_mov_b32 s85, 262144\n" \
  "SL_HL_%=:\n" \
  "buffer_load_dword v27, v26, s[60:63], s86 offen offset:512 sc1\n" \
  "s_waitcnt vmcnt(0)\n" \
  "v_cmp_ne_u32 vcc, s84, v27\n" \
  "s_and_b32 vcc_lo, vcc_lo, 0xe\n" \
  "s_cbranch_scc0 BACK_HL_%=\n" \
  "s_sub_u32 s85, s85, 1\n" \
  "s_cmp_lg_u32 s85, 0\n" \
  "s_cbranch_scc1 SL_HL_%=\n" \
  "s_branch BACK_HL_%=\n" \
  "SLOW_HA_%=:\n" \
  "s_mov_b32 s85, 262144\n" \
  "SL_HA_%=:\n" \
  "buffer_load_dword v27, v26, s[60:63], s86 offen offset:1024 sc1\n" \
  "s_waitcnt vmcnt(0)\n" \
  "v_cmp_ne_u32 vcc, s84, v27\n" \
  "s_and_b32 vcc_lo, vcc_lo, 0xe\n" \
  "s_cbranch_scc0 BACK_HA_%=\n" \
  "s_sub_u32 s85, s85, 1\n" \
  "s_cmp_lg_u32 s85, 0\n" \
  "s_cbranch_scc1 SL_HA_%=\n" \
  "s_branch BACK_HA_%=\n" \
  "SLOW_P0_%=:\n" \
  "s_mov_b32 s85, 262144\n" \
  "SL_P0_%=:\n" \
  "buffer_load_dword v27, v239, s[60:63], s87 offen offset:0 sc1\n" \
  "s_waitcnt vmcnt(0)\n" \
  "v_cmp_ne_u32 vcc, s84, v27\n" \
  "s_and_b32 vcc_lo, vcc_lo, 0xffff\n" \
  "s_cbranch_scc0 BACK_P0_%=\n" \
  "s_sub_u32 s85, s85, 1\n" \
  "s_cmp_lg_u32 s85, 0\n" \
  "s_cbranch_scc1 SL_P0_%=\n" \
  "s_branch BACK_P0_%=\n" \
  "SLOW_P1_%=:\n" \
  "s_mov_b32 s85, 262144\n" \
  "SL_P1_%=:\n" \
  "buffer_load_dword v27, v239, s[60:63], s87 offen offset:128 sc1\n" \
  "s_waitcnt vmcnt(0)\n" \
  "v_cmp_ne_u32 vcc, s84, v27\n" \
  "s_and_b32 vcc_lo, vcc_lo, 0x70\n" \
  "s_cbranch_scc0 BACK_P1_%=\n" \
  "s_sub_u32 s85, s85, 1\n" \
  "s_cmp_lg_u32 s85, 0\n" \
  "s_cbranch_scc1 SL_P1_%=\n" \
  "s_branch BACK_P1_%=\n" \
  "SLOW_P2_%=:\n" \
  "s_mov_b32 s85, 262144\n" \
  "SL_P2_%=:\n" \
  "buffer_load_dword v27, v239, s[60:63], s87 offen offset:256 sc1\n" \
  "s_waitcnt vmcnt(0)\n" \
  "v_cmp_ne_u32 vcc, s84, v27\n" \
  "s_and_b32 vcc_lo, vcc_lo, 0xc\n" \
  "s_cbranch_scc0 BACK_P2_%=\n" \
  "s_sub_u32 s85, s85, 1\n" \
  "s_cmp_lg_u32 s85, 0\n" \
  "s_cbranch_scc1 SL_P2_%=\n" \
  "s_branch BACK_P2_%=\n" \
  "SLOW_P3_%=:\n" \
  "s_mov_b32 s85, 262144\n" \
  "SL_P3_%=:\n" \
  "buffer_load_dword v27, v239, s[60:63], s87 offen offset:384 sc1\n" \
  "s_waitcnt vmcnt(0)\n" \
  "v_cmp_ne_u32 vcc, s84, v27\n" \
  "s_and_b32 vcc_lo, vcc_lo, 0x2\n" \
  "s_cbranch_scc0 BACK_P3_%=\n" \
  "s_sub_u32 s85, s85, 1\n" \
  "s_cmp_lg_u32 s85, 0\n" \
  "s_cbranch_scc1 SL_P3_%=\n" \
  "s_branch BACK_P3_%=\n" \
  "END_%=:\n"

#define MEGA_NAME_0 "{g2: B, nfill: 0, g2bars: [-7, 2, 7, 17, 27, 40], bs: {22: [147, 153], 23: [158, 162], 24: [168, 171], 25: [190, 194], 26: [194, 197]}, l2: [1, 7, 12, 17], lflag: 12, lpoll: 17, lchk: 21, lld_end: 25, psp: 1, gwx: {q: 10, 6: 8, 4: 8}, pb: [[[q, 7], 9, 14, 20, 22], [[6, 5, 4], 10, 15, 21, 24], [[3, 2], 8, 13, 18, 18], [[1], 7, 16, 17, 17]]}"
#define MEGA_NV 1
#define MEGA_CLOB "v1","v2","v3","v4","v5","v6","v7","v8","v9","v10","v11","v12","v13","v14","v15","v16","v17","v18","v19","v20","v21","v22","v23","v24","v25","v26","v27","v28","v29","v30","v31","v32","v33","v34","v35","v36","v37","v38","v39","v40","v41","v42","v43","v44","v45","v46","v47","v48","v49","v50","v51","v52","v53","v54","v55","v56","v57","v58","v59","v60","v61","v62","v63","v64","v65","v66","v67","v68","v69","v70","v71","v72","v73","v74","v75","v76","v77","v78","v79","v80","v81","v82","v83","v84","v85","v86","v87","v88","v89","v90","v91","v92","v93","v94","v95","v96","v97","v98","v99","v100","v101","v102","v103","v104","v105","v106","v107","v108","v109","v110","v111","v112","v113","v114","v115","v116","v117","v118","v119","v120","v121","v122","v123","v124","v125","v126","v127","v128","v129","v130","v131","v132","v133","v134","v135","v136","v137","v138","v139","v140","v141","v142","v143","v144","v145","v146","v147","v148","v149","v150","v151","v152","v153","v154","v155","v156","v157","v158","v159","v160","v161","v162","v163","v164","v165","v166","v167","v168","v169","v170","v171","v172","v173","v174","v175","v176","v177","v178","v179","v180","v181","v182","v183","v184","v185","v186","v187","v188","v189","v190","v191","v192","v193","v194","v195","v196","v197","v198","v199","v200","v201","v202","v203","v204","v205","v206","v207","v208","v209","v210","v211","v212","v213","v214","v215","v216","v217","v218","v219","v220","v221","v222","v223","v224","v225","v226","v227","v228","v229","v230","v231","v232","v233","v234","v235","v236","v237","v238","v239","v240","v241","v242","v243","v244","v245","v246","v247","v248","v249","v250","v251","v252","v253","v254","v255","a0","a1","a2","a3","a4","a5","a6","a7","a8","a9","a10","a11","a12","a13","a14","a15","a16","a17","a18","a19","a20","a21","a22","a23","a24","a25","a26","a27","a28","a29","a30","a31","a32","a33","a34","a35","a36","a37","a38","a39","a40","a41","a42","a43","a44","a45","a46","a47","a48","a49","a50","a51","a52","a53","a54","a55","a56","a57","a58","a59","a60","a61","a62","a63","a64","a65","a66","a67","a68","a69","a70","a71","a72","a73","a74","a75","a76","a77","a78","a79","a80","a81","a82","a83","a84","a85","a86","a87","a88","a89","a90","a91","a92","a93","a94","a95","a96","a97","a98","a99","a100","a101","a102","a103","a104","a105","a106","a107","a108","a109","a110","a111","a112","a113","a114","a115","a116","a117","a118","a119","a120","a121","a122","a123","a124","a125","a126","a127","a128","a129","a130","a131","a132","a133","a134","a135","a136","a137","a138","a139","a140","a141","a142","a143","a144","a145","a146","a147","a148","a149","a150","a151","a152","a153","a154","a155","a156","a157","a158","a159","a160","a161","a162","a163","a164","a165","a166","a167","a168","a169","a170","a171","a172","a173","a174","a175","a176","a177","a178","a179","a180","a181","a182","a183","a184","a185","a186","a187","a188","a189","a190","a191","a192","a193","a194","a195","a196","a197","a198","a199","a200","a201","a202","a203","a204","a205","a206","a207","a208","a209","a210","a211","a212","a213","a214","a215","a216","a217","a218","a219","a220","a221","a222","a223","a224","a225","a226","a227","a228","a229","a230","a231","a232","a233","a234","a235","a236","a237","a238","a239","a240","a241","a242","a243","a244","a245","a246","a247","a248","a249","a250","a251","a252","a253","a254","a255","s4","s5","s6","s7","s8","s9","s10","s11","s12","s13","s14","s15","s16","s17","s18","s19","s20","s21","s22","s23","s24","s25","s26","s27","s28","s29","s30","s31","s36","s37","s38","s39","s40","s41","s42","s43","s44","s45","s46","s47","s48","s49","s50","s51","s52","s53","s54","s55","s56","s57","s58","s59","s60","s61","s62","s63","s64","s65","s66","s67","s68","s69","s70","s71","s72","s73","s74","s75","s76","s77","s78","s79","s80","s81","s82","s83","s84","s85","s86","s87","s88","s89","s90","s91","s92","s93","s94","s95","s96","s97","s98","s99","s100","s101","vcc","scc","memory","s2","s3","s34","s35"
#define LAY_H2 2097152
#define LAY_X1 4456448
#define LAY_H 6815744
#define LAY_END 54788096
__global__ __launch_bounds__(256, 1) __attribute__((aligned(4096))) void fused_block(const void* x, const void* wr, const void* wi, const void* wg, const void* wu,
                                                                                     const void* wd, const void* c, const void* wm, void* ws, void* out) {
  asm volatile(MEGA_ASM_0 :: "s"(__builtin_amdgcn_kernarg_segment_ptr()), "s"(blockIdx.x), "v"(threadIdx.x) : MEGA_CLOB);
}
extern "C" void kernel_launch(void* const* d_in, const int* in_sizes, int n_in, void* d_out, int out_size, void* d_ws, size_t ws_size, hipStream_t stream) {
  (void)hipFuncSetAttribute((const void*)fused_block, hipFuncAttributeMaxDynamicSharedMemorySize, 163840);
  fused_block<<<256, 256, 163840, stream>>>(d_in[0], d_in[1], d_in[2], d_in[4], d_in[5], d_in[6], d_in[7], d_in[8], d_ws, d_out);
}
```
